```python
import torch

def _softmax(x, axis=-1):
    return torch.softmax(x, dim=axis)
def _gelu(x):
    return torch.nn.functional.gelu(x, approximate="tanh")
def _silu(x):
    return torch.nn.functional.silu(x)
def _sigmoid(x):
    return torch.sigmoid(x)
def _layernorm(x, axis=-1, eps=1e-5):
    m = x.mean(dim=axis, keepdim=True)
    v = x.var(dim=axis, keepdim=True, unbiased=False)
    return (x - m) / torch.sqrt(v + eps)
def _scatter_add(tgt, idx, src, axis=0):
    return tgt.clone().index_add_(axis, idx, src)
def _segment_sum(src, seg, n):
    out = torch.zeros((n,) + src.shape[1:], dtype=src.dtype)
    return out.index_add_(0, seg, src)
def _pad(x, pw):
    flat = tuple(v for pair in reversed(pw) for v in pair)
    return torch.nn.functional.pad(x, flat)

kernel_name = 'parallel_sum__spectral_gfnet__mixer_token_mlp__prompt'

def setup_inputs():

    _w = (lambda a: (lambda t: t.to(torch.bfloat16) if t.is_floating_point() else t)(torch.as_tensor(a)))
    def _A_setup():
        B, S, D = 1, 2048, 576
        Df = D // 2 + 1
        return {
            "x": _w((lambda *s: torch.randn(*s, dtype=torch.bfloat16) * 0.05)(B, S, D)),
            "w_r": _w((lambda *s: torch.randn(*s, dtype=torch.bfloat16) * 3.2)(S, Df) * 0.02),
            "w_i": _w((lambda *s: torch.randn(*s, dtype=torch.bfloat16) * 3.2)(S, Df) * 0.02),
        }
    _A_in = _A_setup()

    def _B_setup():
        B, S, D, F = 1, 2048, 576, 4608
        return {
            "x": _w((lambda *s: torch.randn(*s, dtype=torch.bfloat16) * 0.05)(B, S, D)),
            "w1": _w((lambda *s: torch.randn(*s, dtype=torch.bfloat16) * 0.4)(S, F) * (S ** -0.5)),
            "b1": _w((lambda *s: torch.randn(*s, dtype=torch.bfloat16) * 3.2)(F) * 0.02),
            "w2": _w((lambda *s: torch.randn(*s, dtype=torch.bfloat16) * 0.8)(F, S) * (F ** -0.5)),
            "b2": _w((lambda *s: torch.randn(*s, dtype=torch.bfloat16) * 3.2)(S) * 0.02),
        }
    _B_in = _B_setup()

    return {
        "A_x": _A_in["x"],
        "A_w_r": _A_in["w_r"],
        "A_w_i": _A_in["w_i"],
        "B_x": _B_in["x"],
        "B_w1": _B_in["w1"],
        "B_b1": _B_in["b1"],
        "B_w2": _B_in["w2"],
        "B_b2": _B_in["b2"],
    }

def reference(A_x, A_w_r, A_w_i, B_x, B_w1, B_b1, B_w2, B_b2):
    def _A_ref(x, w_r, w_i):
        B, S, D = x.shape
        X = (lambda x, axes=(-2,-1): torch.fft.rfft2(x.float(), dim=axes))(x, axes=(-2, -1))
        W = w_r + 1j * w_i
        X = X * W
        y = (lambda x, s, axes=(-2,-1): torch.fft.irfft2(x, s=s, dim=axes).to(torch.bfloat16))(X, (S, D), axes=(-2, -1))
        return y

    def _B_ref(x, w1, b1, w2, b2):
        B, S, D = x.shape
        h = _layernorm(x, axis=-1)
        u = torch.einsum("bsd,sf->bdf", h, w1) + b1
        u = _gelu(u)
        y = torch.einsum("bdf,fs->bds", u, w2) + b2
        return (lambda x, *d: x.permute(*d))(y, 0, 2, 1)

    x = A_x
    xn = _layernorm(x, axis=-1)
    return x + _A_ref(xn, A_w_r, A_w_i) + _B_ref(xn, B_w1, B_b1, B_w2, B_b2)
```

```cpp
#include <hip/hip_runtime.h>
#include <hip/hip_bf16.h>
#include <stdint.h>

#define DI __device__ __forceinline__
#define LAS __attribute__((address_space(3)))
typedef unsigned short u16;
typedef __attribute__((ext_vector_type(2))) float f32x2;
typedef __attribute__((ext_vector_type(4))) float f32x4;
typedef __attribute__((ext_vector_type(16))) float f32x16;
typedef __attribute__((ext_vector_type(2))) _Float16 f16x2;
typedef __attribute__((ext_vector_type(8))) _Float16 f16x8;
typedef __attribute__((ext_vector_type(2))) __bf16 bf16x2;
typedef __attribute__((ext_vector_type(8))) __bf16 bf16x8;
typedef __attribute__((ext_vector_type(4))) short s16x4;
typedef __attribute__((ext_vector_type(2))) unsigned u32x2;
typedef __attribute__((ext_vector_type(4))) unsigned u32x4;

constexpr int S_ = 2048, D_ = 576, F_ = 4608, NC_ = 289;

constexpr size_t OFF_H    = 0;
constexpr size_t OFF_XN   = OFF_H + 2359296;
constexpr size_t OFF_F1   = OFF_XN + 2359296;
constexpr size_t OFF_G    = OFF_F1 + 663552;
constexpr size_t OFF_WT   = OFF_G + 663552;
constexpr size_t OFF_TAB  = OFF_WT + 2367488;
constexpr size_t OFF_WMAX = OFF_TAB + 65536;
constexpr size_t OFF_X1   = OFF_WMAX + 4096;
constexpr size_t OFF_Y1   = OFF_X1 + 2359296;
constexpr size_t OFF_U    = OFF_Y1 + 2359296;
constexpr size_t OFF_SLAB = OFF_U + 5308416;
constexpr size_t WS_END   = OFF_SLAB + 18874368;
constexpr int TAB_E1 = 0, TAB_E2 = 6144, TAB_TW1 = 30720, TAB_TW2 = 47104, TAB_BYTES = 63488;

DI unsigned pack_bf16(float a, float b) { f32x2 v = {a, b}; return __builtin_bit_cast(unsigned, __builtin_convertvector(v, bf16x2)); }
DI unsigned pack_f16(float a, float b) { f32x2 v = {a, b}; return __builtin_bit_cast(unsigned, __builtin_convertvector(v, f16x2)); }
DI float lo16(unsigned p) { return __uint_as_float(p << 16); }
DI float hi16(unsigned p) { return __uint_as_float(p & 0xffff0000u); }
DI float rbf(float a) { return lo16(pack_bf16(a, a)); }

template <int CTRL> DI float dpp_add(float v) {
  return v + __int_as_float(__builtin_amdgcn_update_dpp(0, __float_as_int(v), CTRL, 0xf, 0xf, true));
}
DI float sum32(float v) {
  v = dpp_add<0xB1>(v); v = dpp_add<0x4E>(v); v = dpp_add<0x141>(v); v = dpp_add<0x140>(v);
  auto r = __builtin_amdgcn_permlane16_swap(__float_as_uint(v), __float_as_uint(v), false, false);
  return __uint_as_float(r[0]) + __uint_as_float(r[1]);
}

#define WAIT_VM(n) asm volatile("s_waitcnt vmcnt(%0)" ::"n"(n) : "memory")
#define WAIT_LGKM0() asm volatile("s_waitcnt lgkmcnt(0)" ::: "memory")

DI u32x4 mk_rsrc(const void* p, unsigned bytes) {
  const unsigned long long a = (unsigned long long)p;
  return u32x4{(unsigned)a, (unsigned)(a >> 32) & 0xffffu, bytes, 0x00020000u};
}
DI void dma16(u32x4 rsrc, unsigned lds_off, unsigned voff, unsigned soff) {
  asm volatile("s_mov_b32 m0, %0\n\ts_nop 0\n\tbuffer_load_dwordx4 %1, %2, %3 offen lds" ::"s"(lds_off), "v"(voff), "s"(rsrc), "s"(soff) : "memory");
}

DI float dft576(int kk, int n) {
  float r = (float)n * (1.0f / 576.0f);
  return ((kk & 1) && kk != 1) ? -__builtin_amdgcn_sinf(r) : __builtin_amdgcn_cosf(r);
}

__global__ __launch_bounds__(256) void k_prep(const u16* __restrict__ x, const u16* __restrict__ wr, const u16* __restrict__ wi,
                                              char* __restrict__ ws) {
  __shared__ __attribute__((aligned(16))) char lds[19456];
  const int b = blockIdx.x, tid = threadIdx.x;
  if (b < 256) {
    constexpr int RS = 1216;
    const int kb = b >> 2, g = b & 3;
    const int rr = tid >> 5, l32 = tid & 31;
    const int s = 32 * kb + (rr >> 2) * 16 + 4 * g + (rr & 3);
    const unsigned* xr = (const unsigned*)(x + (size_t)s * D_);
    unsigned raw[9];
#pragma unroll
    for (int i = 0; i < 9; ++i) raw[i] = xr[32 * i + l32];
    float v[18];
#pragma unroll
    for (int i = 0; i < 9; ++i) { v[2 * i] = lo16(raw[i]); v[2 * i + 1] = hi16(raw[i]); }
#pragma unroll
    for (int pass = 0; pass < 2; ++pass) {
      float sm = 0.f;
#pragma unroll
      for (int i = 0; i < 18; ++i) sm += v[i];
      const float mean = sum32(sm) * (1.0f / 576.0f);
      float sq = 0.f;
#pragma unroll
      for (int i = 0; i < 18; ++i) { float dlt = v[i] - mean; sq += dlt * dlt; }
      const float var = sum32(sq) * (1.0f / 576.0f);
      const float mb = rbf(mean);
      const float sd = rbf(__builtin_sqrtf(rbf(rbf(var) + 1e-5f)));
      const float rs = 1.0f / sd;
      char* dst = lds + pass * (8 * RS) + rr * RS + l32 * 4;
#pragma unroll
      for (int i = 0; i < 9; ++i) {
        unsigned p = pack_bf16(v[2 * i] - mb, v[2 * i + 1] - mb);
        p = pack_bf16(lo16(p) * rs, hi16(p) * rs);
        v[2 * i] = lo16(p); v[2 * i + 1] = hi16(p);
        *(unsigned*)(dst + i * 128) = pass == 0 ? pack_f16(v[2 * i], v[2 * i + 1]) : p;
      }
    }
    __syncthreads();
    for (int p = tid; p < 576; p += 256) {
      const int run = p / 288, rem = p - run * 288, dp = rem >> 2, q = rem & 3;
      const int srow = 32 * kb + run * 16 + 4 * g + q;
      const int s1 = srow >> 6, s2 = srow & 63;
      const int t = s1 >> 4, ab = (s1 >> 3) & 1, h = (s1 >> 2) & 1, e = s1 & 3;
      const int T = s2 >> 5, b4 = (s2 >> 4) & 1, b32 = (s2 >> 2) & 3;
      const int RT = ((((t * 2 + h) * 2 + T) * 4 + b32) * 2 + b4) * 2 + ab;
      u32x4 val = *(const u32x4*)(lds + (run * 4 + q) * RS + dp * 16);
      *(u32x4*)(ws + OFF_XN + (size_t)RT * 18432 + dp * 256 + (4 * e + q) * 16) = val;
    }
    const int wave = tid >> 6, lane = tid & 63;
    for (int cb = wave; cb < 9; cb += 4) {
      const int gg = lane >> 4, q = (lane & 15) >> 2, pp = lane & 3;
      const char* src = lds + 8 * RS + q * RS + (cb * 64 + gg * 16 + pp * 4) * 2;
      s16x4 lo = __builtin_amdgcn_ds_read_tr16_b64_v4i16((LAS s16x4*)src);
      s16x4 hi = __builtin_amdgcn_ds_read_tr16_b64_v4i16((LAS s16x4*)(src + 4 * RS));
      u32x2 a = __builtin_bit_cast(u32x2, lo), c = __builtin_bit_cast(u32x2, hi);
      u32x4 val = {a[0], a[1], c[0], c[1]};
      const int df = cb * 4 + gg, i = lane & 15;
      *(u32x4*)(ws + OFF_H + ((size_t)df * 64 + kb) * 1024 + (g * 16 + i) * 16) = val;
    }
  } else {
    const int ab_ = b - 256;
    {
      const int rg = ab_ >> 2, cg = ab_ & 3;
      const int c0 = cg * 72 + (cg > 0), nc = cg == 0 ? 73 : 72;
      const int kq = rg >> 2, ko = rg & 3;
      unsigned mx = 0;
      for (int idx = tid; idx < 32 * 73; idx += 256) {
        const int row = idx / 73, col = idx - row * 73;
        if (col < nc) {
          const int a = row & 7, e = row >> 3;
          const int ks = 8 * ko + a + 32 * (4 * kq + e);
          const unsigned r_ = wr[ks * NC_ + c0 + col], i_ = wi[ks * NC_ + c0 + col];
          mx = max(mx, max(r_ & 0x7fffu, i_ & 0x7fffu));
          *(unsigned*)(lds + col * 144 + a * 16 + e * 4) = r_ | (i_ << 16);
        }
      }
#pragma unroll
      for (int o = 32; o; o >>= 1) mx = max(mx, (unsigned)__shfl_xor((int)mx, o));
      unsigned* red = (unsigned*)(lds + 16384);
      if ((tid & 63) == 0) red[tid >> 6] = mx;
      __syncthreads();
      if (tid == 0) ((unsigned*)(ws + OFF_WMAX))[ab_] = max(max(red[0], red[1]), max(red[2], red[3]));
      const int U = kq >> 3, h = (kq >> 2) & 1, rq = kq & 3;
      for (int p = tid; p < nc * 8; p += 256) {
        const int col = p >> 3, a = p & 7;
        u32x4 val = *(const u32x4*)(lds + col * 144 + a * 16);
        *(u32x4*)(ws + OFF_WT + (size_t)(c0 + col) * 8192 + ((U * 4 + rq) * 64 + h * 32 + 8 * ko + a) * 16) = val;
      }
    }
    for (int P = ab_ * 256 + tid; P < 86912; P += 65536) {
      u32x4 val; char* dst;
      if (P < 82944) {
        const bool isG = P >= 41472;
        const int PP = isG ? P - 41472 : P;
        const int frag = PP >> 6, lane = PP & 63, fr = frag / 18, ks = frag - fr * 18, i = lane & 15, gq = lane >> 4;
        float o[8];
        if (!isG) {
          const int kk = 16 * fr + i, c = kk == 1 ? 288 : kk >> 1, d0 = 32 * ks + 8 * gq;
          int n = (c * d0) % 576;
#pragma unroll
          for (int j = 0; j < 8; ++j) { o[j] = dft576(kk, n); n += c; if (n >= 576) n -= 576; }
        } else {
          const int d = 16 * fr + i;
#pragma unroll
          for (int j = 0; j < 8; ++j) {
            const int kk = 32 * ks + 16 * (j >> 2) + 4 * gq + (j & 3), c = kk == 1 ? 288 : kk >> 1;
            o[j] = dft576(kk, (c * d) % 576);
          }
        }
        val = u32x4{pack_f16(o[0], o[1]), pack_f16(o[2], o[3]), pack_f16(o[4], o[5]), pack_f16(o[6], o[7])};
        dst = ws + (isG ? OFF_G : OFF_F1) + (size_t)PP * 16;
      } else if (P < 82944 + 384 + 1536) {
        const int PP = P - 82944, lane = PP & 63, r = lane & 31, h = lane >> 5;
        int frag = PP >> 6;
        float o[8];
        if (frag < 6) {
          const int t = frag / 3, which = frag - 3 * t;
#pragma unroll
          for (int j = 0; j < 8; ++j) {
            const int kap = 16 * t + 8 * (j >> 2) + 4 * h + (j & 3);
            const float a = (float)((kap * r) & 31) * (1.0f / 32.0f);
            o[j] = which == 0 ? __builtin_amdgcn_cosf(a) : (which == 1 ? -__builtin_amdgcn_sinf(a) : __builtin_amdgcn_sinf(a));
          }
        } else {
          frag -= 6;
          const int idx = frag / 3, which = frag - 3 * idx, U = idx >> 2, T = (idx >> 1) & 1, u = idx & 1;
          const int a_ = 32 * U + 16 * ((r >> 2) & 1) + 4 * (r >> 3) + (r & 3);
#pragma unroll
          for (int j = 0; j < 8; ++j) {
            const int bi = 32 * T + 16 * h + 8 * u + j;
            const float a = (float)((a_ * bi) & 63) * (1.0f / 64.0f);
            o[j] = which == 0 ? __builtin_amdgcn_cosf(a) : (which == 1 ? -__builtin_amdgcn_sinf(a) : __builtin_amdgcn_sinf(a));
          }
        }
        val = u32x4{pack_f16(o[0], o[1]), pack_f16(o[2], o[3]), pack_f16(o[4], o[5]), pack_f16(o[6], o[7])};
        dst = ws + OFF_TAB + (size_t)PP * 16;
      } else {
        int PP = P - (82944 + 384 + 1536);
        const bool inv = PP >= 1024;
        const int Q = inv ? PP - 1024 : PP;
        const int lane = Q & 63, p = (Q >> 6) & 7, T = Q >> 9, r = lane & 31, h = lane >> 5;
        float o[4];
#pragma unroll
        for (int z = 0; z < 2; ++z) {
          const int rho = 2 * p + z;
          int n;
          if (!inv) n = (32 * T + 16 * h + rho) * r;
          else n = (32 * T + 16 * ((r >> 2) & 1) + 4 * (r >> 3) + (r & 3)) * ((rho & 3) + 8 * (rho >> 2) + 4 * h);
          const float a = (float)(n & 2047) * (1.0f / 2048.0f);
          o[2 * z] = __builtin_amdgcn_cosf(a);
          o[2 * z + 1] = inv ? __builtin_amdgcn_sinf(a) : -__builtin_amdgcn_sinf(a);
        }
        val = u32x4{__float_as_uint(o[0]), __float_as_uint(o[1]), __float_as_uint(o[2]), __float_as_uint(o[3])};
        dst = ws + OFF_TAB + TAB_TW1 + (size_t)PP * 16;
      }
      *(u32x4*)dst = val;
    }
  }
}

template <bool F16> DI f32x4 mfma16(u32x4 a, u32x4 b, f32x4 c) {
  if constexpr (F16) return __builtin_amdgcn_mfma_f32_16x16x32_f16(__builtin_bit_cast(f16x8, a), __builtin_bit_cast(f16x8, b), c, 0, 0, 0);
  else return __builtin_amdgcn_mfma_f32_16x16x32_bf16(__builtin_bit_cast(bf16x8, a), __builtin_bit_cast(bf16x8, b), c, 0, 0, 0);
}

struct Op {
  const char* base;
  unsigned bytes;
  unsigned s0;
  unsigned kstride;
  unsigned mstride;
};

template <int NFA_W, int NFB_W, int WA, int WB, bool A_TR, bool B_TR, int NS>
struct GC {
  static constexpr int NW = WA * WB, NFA = NFA_W * WA, NFB = NFB_W * WB;
  static constexpr int A_BYTES = NFA * 2048, B_BYTES = NFB * 2048, STAGE = A_BYTES + B_BYTES;
  static constexpr int IA = 2 * NFA, IB = 2 * NFB;
  static constexpr int GA = (IA + NW - 1) / NW, GB = (IB + NW - 1) / NW, G = GA + GB;
  static constexpr bool UNEVEN = (IA % NW) || (IB % NW);
  static constexpr int LDS_BYTES = NS * STAGE + (UNEVEN ? 1024 : 0);
  static_assert(!A_TR || NFA == 8, "TR operand tile is 128 wide");
  static_assert(!B_TR || NFB == 8, "TR operand tile is 128 wide");

  static DI unsigned tr_voff(int wave, int lane, unsigned ld_bytes) {
    const int row = 4 * wave + (lane >> 4);
    return (unsigned)row * ld_bytes + (unsigned)(((lane & 15) ^ ((row & 7) << 1)) * 16);
  }
  static DI Op lin_op(const char* base, unsigned bytes, int f0, int nks, int kt0, int wave) {
    Op o; o.base = base; o.bytes = bytes;
    o.s0 = (unsigned)((f0 + (wave >> 1)) * nks) * 1024u + (unsigned)(wave & 1) * 1024u + (unsigned)kt0 * 2048u;
    o.kstride = 2048u; o.mstride = (unsigned)((NW / 2) * nks) * 1024u;
    return o;
  }
  static DI Op tr_op(const char* base, unsigned bytes, int n0, unsigned ld_bytes, int kt0) {
    Op o; o.base = base; o.bytes = bytes;
    o.s0 = (unsigned)n0 * 2u + (unsigned)kt0 * 64u * ld_bytes;
    o.kstride = 64u * ld_bytes; o.mstride = 4u * NW * ld_bytes;
    return o;
  }
  template <int I, int GN>
  static DI void issue_op(char* smem, int ldsoff, const Op& o, unsigned voff, int kt, bool valid, int wave) {
#pragma unroll
    for (int m = 0; m < GN; ++m) {
      const bool ok = valid && ((I % NW == 0) || (m + 1 < GN) || (wave + NW * m < I));
      const int dst = ((I % NW == 0) || (m + 1 < GN) || (wave + NW * m < I)) ? ldsoff + (wave + NW * m) * 1024 : NS * STAGE;
      dma16(mk_rsrc(o.base, ok ? o.bytes : 0u), (unsigned)(size_t)(LAS char*)smem + dst, voff,
            o.s0 + (unsigned)kt * o.kstride + (unsigned)m * o.mstride);
    }
  }
  template <bool TR, int NF>
  static DI void read_frags(u32x4 (&f)[NF], const char* smem, const unsigned (&addr)[TR ? NF : 1], int sb, int hh) {
#pragma unroll
    for (int i = 0; i < NF; ++i) {
      if constexpr (TR) {
        const char* p = smem + addr[i] + sb + hh * 8192;
        s16x4 lo = __builtin_amdgcn_ds_read_tr16_b64_v4i16((LAS s16x4*)p);
        s16x4 hi = __builtin_amdgcn_ds_read_tr16_b64_v4i16((LAS s16x4*)(p + 4096));
        u32x2 a = __builtin_bit_cast(u32x2, lo), c = __builtin_bit_cast(u32x2, hi);
        f[i] = u32x4{a[0], a[1], c[0], c[1]};
      } else {
        f[i] = *(const u32x4*)(smem + addr[0] + sb + (i * 2 + hh) * 1024);
      }
    }
  }
  template <bool TR, int NF>
  static DI void frag_addr(unsigned (&addr)[TR ? NF : 1], int opoff, int wpos, int lane, unsigned lin_lane_off) {
    if constexpr (TR) {
      const int g = lane >> 4, q = (lane & 15) >> 2, p = lane & 3;
#pragma unroll
      for (int i = 0; i < NF; ++i) {
        const int c = wpos * NF + i;
        addr[i] = opoff + (4 * g + q) * 256 + (((2 * c + (p >> 1)) ^ ((4 * (g & 1) + q) << 1)) * 16) + 8 * (p & 1);
      }
    } else {
      addr[0] = opoff + wpos * NF * 2048 + lin_lane_off;
    }
  }
  template <bool F16>
  static DI void mma(f32x4 (&acc)[NFA_W][NFB_W], const u32x4 (&a)[NFA_W], const u32x4 (&b)[NFB_W]) {
#pragma unroll
    for (int i = 0; i < NFA_W; ++i)
#pragma unroll
      for (int j = 0; j < NFB_W; ++j) acc[i][j] = mfma16<F16>(a[i], b[j], acc[i][j]);
  }
};

#define GEMM_ITER(CFG, F16PREV, F16CUR, ISSUE, MIDOP)                                   \
  {                                                                                     \
    WAIT_VM((NS - 2) * CFG::G); WAIT_LGKM0(); __builtin_amdgcn_s_barrier();             \
    ISSUE(tau + NS - 1, wslot * CFG::STAGE);                                            \
    const int sb = rslot * CFG::STAGE;                                                  \
    CFG::template read_frags<A_TR, NFA_W>(fa0, smem, addrA, sb, 0);                     \
    CFG::template read_frags<B_TR, NFB_W>(fb0, smem, addrB, sb, 0);                     \
    CFG::template mma<F16PREV>(acc, fa1, fb1);                                          \
    MIDOP;                                                                              \
    CFG::template read_frags<A_TR, NFA_W>(fa1, smem, addrA, sb, 1);                     \
    CFG::template read_frags<B_TR, NFB_W>(fb1, smem, addrB, sb, 1);                     \
    CFG::template mma<F16CUR>(acc, fa0, fb0);                                           \
    wslot = rslot; rslot = (rslot + 1 == NS) ? 0 : rslot + 1;                           \
  }

__global__ __launch_bounds__(256, 2) void k_rowdft(char* __restrict__ ws) {
  extern __shared__ __attribute__((aligned(16))) char smem[];
  constexpr int NFA_W = 4, NFB_W = 3, NS = 4; constexpr bool A_TR = false, B_TR = false;
  using CFG = GC<NFA_W, NFB_W, 2, 2, A_TR, B_TR, NS>;
  const int lane = threadIdx.x & 63, wave = __builtin_amdgcn_readfirstlane(threadIdx.x >> 6);
  const int rb = blockIdx.x / 6, kb = blockIdx.x - rb * 6;
  const int wa = wave >> 1, wb = wave & 1;
  const Op oa = CFG::lin_op(ws + OFF_XN, 2359296u, rb * 8, 18, 0, wave);
  const Op ob = CFG::lin_op(ws + OFF_F1, 663552u, kb * 6, 18, 0, wave);
  const unsigned voff = lane * 16;
  const int nt = 9;
  auto issue = [&](int kt, int so) {
    CFG::issue_op<CFG::IA, CFG::GA>(smem, so, oa, voff, kt, kt < nt, wave);
    CFG::issue_op<CFG::IB, CFG::GB>(smem, so + CFG::A_BYTES, ob, voff, kt, kt < nt, wave);
  };
  unsigned addrA[1], addrB[1];
  CFG::frag_addr<false, NFA_W>(addrA, 0, wa, lane, (lane >> 4) * 256 + (4 * (lane & 3) + ((lane & 15) >> 2)) * 16);
  CFG::frag_addr<false, NFB_W>(addrB, CFG::A_BYTES, wb, lane, lane * 16);
  f32x4 acc[NFA_W][NFB_W] = {};
  u32x4 fa0[NFA_W], fb0[NFB_W], fa1[NFA_W] = {}, fb1[NFB_W] = {};
#pragma unroll
  for (int p = 0; p < NS - 1; ++p) issue(p, p * CFG::STAGE);
  int rslot = 0, wslot = NS - 1;
  for (int tau = 0; tau < nt; ++tau) GEMM_ITER(CFG, true, true, issue, (void)0)
  CFG::mma<true>(acc, fa1, fb1);
  const int q = lane >> 4;
#pragma unroll
  for (int p = 0; p < NFA_W / 2; ++p) {
    const int RT = rb * 8 + wa * NFA_W + 2 * p;
    const int b4 = (RT >> 1) & 1, b32 = (RT >> 2) & 3, T = (RT >> 4) & 1, h = (RT >> 5) & 1, t = RT >> 6;
    const int r = 8 * b32 + 4 * b4 + q;
#pragma unroll
    for (int j = 0; j < NFB_W; ++j) {
      const int kk = (kb * 6 + wb * NFB_W + j) * 16 + (lane & 15);
      const f32x4 a = acc[2 * p][j] * (1.0f / 32.0f), c = acc[2 * p + 1][j] * (1.0f / 32.0f);
      u32x4 val = {pack_f16(a[0], a[1]), pack_f16(a[2], a[3]), pack_f16(c[0], c[1]), pack_f16(c[2], c[3])};
      *(u32x4*)(ws + OFF_X1 + (size_t)kk * 4096 + (T * 2 + t) * 1024 + (h * 32 + r) * 16) = val;
    }
  }
}

DI f32x16 mfma32(u32x4 a, u32x4 b, f32x16 c) {
  return __builtin_amdgcn_mfma_f32_32x32x16_f16(__builtin_bit_cast(f16x8, a), __builtin_bit_cast(f16x8, b), c, 0, 0, 0);
}
DI u32x4 pack8(const f32x16& v, int u) {
  return u32x4{pack_f16(v[8 * u], v[8 * u + 1]), pack_f16(v[8 * u + 2], v[8 * u + 3]), pack_f16(v[8 * u + 4], v[8 * u + 5]), pack_f16(v[8 * u + 6], v[8 * u + 7])};
}
DI void twiddle(f32x16& re, f32x16& im, const char* tw) {
#pragma unroll
  for (int p = 0; p < 8; ++p) {
    const f32x4 w = *(const f32x4*)(tw + p * 1024);
    const float r0 = re[2 * p], i0 = im[2 * p], r1 = re[2 * p + 1], i1 = im[2 * p + 1];
    re[2 * p] = r0 * w[0] - i0 * w[1]; im[2 * p] = r0 * w[1] + i0 * w[0];
    re[2 * p + 1] = r1 * w[2] - i1 * w[3]; im[2 * p + 1] = r1 * w[3] + i1 * w[2];
  }
}

DI void colfft_wave(const char* tab  , char* ws, int c, int lane, float wsc) {
  const bool cplx = c != 0 && c != 288;
  const int kre = c == 288 ? 1 : 2 * c, kim = 2 * c + 1;
  u32x4 zr[2][2], zi[2][2];
#pragma unroll
  for (int T = 0; T < 2; ++T)
#pragma unroll
    for (int t = 0; t < 2; ++t) {
      zr[T][t] = *(const u32x4*)(ws + OFF_X1 + (size_t)kre * 4096 + (T * 2 + t) * 1024 + lane * 16);
      zi[T][t] = cplx ? *(const u32x4*)(ws + OFF_X1 + (size_t)kim * 4096 + (T * 2 + t) * 1024 + lane * 16) : u32x4{0, 0, 0, 0};
    }
  u32x4 wv[8];
#pragma unroll
  for (int i = 0; i < 8; ++i) wv[i] = *(const u32x4*)(ws + OFF_WT + (size_t)c * 8192 + i * 1024 + lane * 16);
#define EFRAG(off) (*(const u32x4*)(tab + (off)))
  u32x4 ar[2][2], ai[2][2];
#pragma unroll
  for (int T = 0; T < 2; ++T) {
    f32x16 re = {}, im = {};
#pragma unroll
    for (int t = 0; t < 2; ++t) {
      const u32x4 er = EFRAG(TAB_E1 + (t * 3 + 0) * 1024), ei = EFRAG(TAB_E1 + (t * 3 + 1) * 1024), en = EFRAG(TAB_E1 + (t * 3 + 2) * 1024);
      re = mfma32(zr[T][t], er, re); re = mfma32(zi[T][t], en, re);
      im = mfma32(zr[T][t], ei, im); im = mfma32(zi[T][t], er, im);
    }
    twiddle(re, im, tab + TAB_TW1 + T * 8192);
    ar[T][0] = pack8(re, 0); ar[T][1] = pack8(re, 1); ai[T][0] = pack8(im, 0); ai[T][1] = pack8(im, 1);
  }
  u32x4 pr[2][2], pi[2][2];
#pragma unroll
  for (int U = 0; U < 2; ++U) {
    f32x16 re = {}, im = {};
#pragma unroll
    for (int T = 0; T < 2; ++T)
#pragma unroll
      for (int u = 0; u < 2; ++u) {
        const int o = TAB_E2 + (((U * 2 + T) * 2 + u) * 3) * 1024;
        const u32x4 er = EFRAG(o), ei = EFRAG(o + 1024), en = EFRAG(o + 2048);
        re = mfma32(er, ar[T][u], re); re = mfma32(en, ai[T][u], re);
        im = mfma32(ei, ar[T][u], im); im = mfma32(er, ai[T][u], im);
      }
#pragma unroll
    for (int rho = 0; rho < 16; ++rho) {
      const unsigned w = wv[U * 4 + (rho >> 2)][rho & 3];
      const float a = lo16(w) * wsc, b = hi16(w) * wsc, r0 = re[rho], i0 = im[rho];
      re[rho] = r0 * a - i0 * b; im[rho] = r0 * b + i0 * a;
    }
    pr[U][0] = pack8(re, 0); pr[U][1] = pack8(re, 1); pi[U][0] = pack8(im, 0); pi[U][1] = pack8(im, 1);
  }
  const float osc = cplx ? 0.25f : 0.125f;
#pragma unroll
  for (int V = 0; V < 2; ++V) {
    f32x16 re = {}, im = {};
#pragma unroll
    for (int U = 0; U < 2; ++U)
#pragma unroll
      for (int u = 0; u < 2; ++u) {
        const int o = TAB_E2 + (((V * 2 + U) * 2 + u) * 3) * 1024;
        const u32x4 er = EFRAG(o), ei = EFRAG(o + 1024), en = EFRAG(o + 2048);
        re = mfma32(pr[U][u], er, re); re = mfma32(pi[U][u], ei, re);
        im = mfma32(pr[U][u], en, im); im = mfma32(pi[U][u], er, im);
      }
    twiddle(re, im, tab + TAB_TW2 + V * 8192);
    f32x16 yr = {}, yi = {};
#pragma unroll
    for (int u = 0; u < 2; ++u) {
      const u32x4 br = pack8(re, u), bi = pack8(im, u);
      const u32x4 er = EFRAG(TAB_E1 + (u * 3 + 0) * 1024), ei = EFRAG(TAB_E1 + (u * 3 + 1) * 1024), en = EFRAG(TAB_E1 + (u * 3 + 2) * 1024);
      yr = mfma32(br, er, yr); yr = mfma32(bi, ei, yr);
      yi = mfma32(br, en, yi); yi = mfma32(bi, er, yi);
    }
    yr *= osc; yi *= osc;
    const int so = ((lane & 31) * 64 + V * 32 + (lane >> 5) * 16) * 2;
    char* dr = ws + OFF_Y1 + (size_t)kre * 4096 + so;
    *(u32x4*)dr = pack8(yr, 0); *(u32x4*)(dr + 16) = pack8(yr, 1);
    if (cplx) {
      char* di = ws + OFF_Y1 + (size_t)kim * 4096 + so;
      *(u32x4*)di = pack8(yi, 0); *(u32x4*)(di + 16) = pack8(yi, 1);
    }
  }
#undef EFRAG
}

__global__ __launch_bounds__(256) void k_colfft(char* __restrict__ ws) {
  extern __shared__ __attribute__((aligned(16))) char smem[];
  const int tid = threadIdx.x, lane = tid & 63, wave = __builtin_amdgcn_readfirstlane(tid >> 6);
  for (int i = tid; i < TAB_BYTES / 16; i += 256) *(u32x4*)(smem + i * 16) = *(const u32x4*)(ws + OFF_TAB + i * 16);
  u32x4 mv = *(const u32x4*)(ws + OFF_WMAX + lane * 16);
  unsigned mx = max(max(mv[0], mv[1]), max(mv[2], mv[3]));
#pragma unroll
  for (int o = 32; o; o >>= 1) mx = max(mx, (unsigned)__shfl_xor((int)mx, o));
  const unsigned E = min(mx >> 7, 252u);
  const float wsc = __uint_as_float((253u - E) << 23) * (1.0f / 64.0f);
  if (blockIdx.x == 0 && tid == 0) *(float*)(ws + OFF_WMAX + 2048) = __uint_as_float((E + 1u) << 23) * (1.0f / 72.0f);
  __syncthreads();
  const int c = blockIdx.x * 4 + wave;
  if (c < NC_) colfft_wave(smem + lane * 16, ws, c, lane, wsc);
}

DI float gelu_tanh(float u) {
  const float z = u * (1.0f + 0.044715f * u * u) * (-2.0f * 0.7978845608028654f * 1.4426950408889634f);
  return u * __builtin_amdgcn_rcpf(1.0f + __builtin_amdgcn_exp2f(z));
}

__global__ __launch_bounds__(256, 2) void k_gemm1(const char* __restrict__ w1, const u16* __restrict__ b1, char* __restrict__ ws) {
  extern __shared__ __attribute__((aligned(16))) char smem[];
  constexpr int NFA_W = 4, NFB_W = 3, NS = 5; constexpr bool A_TR = true, B_TR = false;
  using CFG = GC<NFA_W, NFB_W, 2, 2, A_TR, B_TR, NS>;
  const int lane = threadIdx.x & 63, wave = __builtin_amdgcn_readfirstlane(threadIdx.x >> 6);
  const int tile = (blockIdx.x & 7) * 27 + (blockIdx.x >> 3);
  const int ft = tile / 6, dt = tile - ft * 6;
  const int wa = wave >> 1, wb = wave & 1;
  const Op oa = CFG::tr_op(w1, 18874368u, ft * 128, F_ * 2, 0);
  const Op ob = CFG::lin_op(ws + OFF_H, 2359296u, dt * 6, 64, 0, wave);
  const unsigned voffA = CFG::tr_voff(wave, lane, F_ * 2), voffB = lane * 16;
  const int nt = 32;
  auto issue = [&](int kt, int so) {
    CFG::issue_op<CFG::IA, CFG::GA>(smem, so, oa, voffA, kt, kt < nt, wave);
    CFG::issue_op<CFG::IB, CFG::GB>(smem, so + CFG::A_BYTES, ob, voffB, kt, kt < nt, wave);
  };
  unsigned addrA[NFA_W], addrB[1];
  CFG::frag_addr<true, NFA_W>(addrA, 0, wa, lane, 0);
  CFG::frag_addr<false, NFB_W>(addrB, CFG::A_BYTES, wb, lane, lane * 16);
  f32x4 acc[NFA_W][NFB_W] = {};
  u32x4 fa0[NFA_W], fb0[NFB_W], fa1[NFA_W] = {}, fb1[NFB_W] = {};
#pragma unroll
  for (int p = 0; p < NS - 1; ++p) issue(p, p * CFG::STAGE);
  int rslot = 0, wslot = NS - 1;
  for (int tau = 0; tau < nt; ++tau) GEMM_ITER(CFG, false, false, issue, (void)0)
  CFG::mma<false>(acc, fa1, fb1);
  const int q = lane >> 4;
#pragma unroll
  for (int p = 0; p < NFA_W / 2; ++p) {
    const int f0 = ft * 128 + (wa * NFA_W + 2 * p) * 16;
    const u32x2 ba = *(const u32x2*)(b1 + f0 + 4 * q), bb = *(const u32x2*)(b1 + f0 + 16 + 4 * q);
    const float bias[8] = {lo16(ba[0]), hi16(ba[0]), lo16(ba[1]), hi16(ba[1]), lo16(bb[0]), hi16(bb[0]), lo16(bb[1]), hi16(bb[1])};
#pragma unroll
    for (int j = 0; j < NFB_W; ++j) {
      float o[8];
#pragma unroll
      for (int e = 0; e < 8; e += 2) {
        const f32x4 a = acc[2 * p + (e >> 2)][j];
        unsigned pk = pack_bf16(a[e & 3], a[(e & 3) + 1]);
        pk = pack_bf16(lo16(pk) + bias[e], hi16(pk) + bias[e + 1]);
        o[e] = gelu_tanh(lo16(pk)); o[e + 1] = gelu_tanh(hi16(pk));
      }
      u32x4 val = {pack_bf16(o[0], o[1]), pack_bf16(o[2], o[3]), pack_bf16(o[4], o[5]), pack_bf16(o[6], o[7])};
      const int df = dt * 6 + wb * NFB_W + j;
      *(u32x4*)(ws + OFF_U + ((size_t)df * 144 + (f0 >> 5)) * 1024 + lane * 16) = val;
    }
  }
}

__global__ __launch_bounds__(256, 2) void k_gemm2(const char* __restrict__ w2, char* __restrict__ ws) {
  extern __shared__ __attribute__((aligned(16))) char smem[];
  constexpr int NFA_W = 9, NFB_W = 2, NS = 4; constexpr bool A_TR = false, B_TR = true;
  using CFG = GC<NFA_W, NFB_W, 1, 4, A_TR, B_TR, NS>;
  const int lane = threadIdx.x & 63, wave = __builtin_amdgcn_readfirstlane(threadIdx.x >> 6);
  const int xcd = blockIdx.x & 7, idx = blockIdx.x >> 3;
  const int ks = xcd >> 1, st = (xcd & 1) * 8 + (idx >> 2), dt = idx & 3;
  const int n1 = ks == 3 ? 9 : 0, nt = ks == 3 ? 21 : 20;
  const Op oa1 = CFG::lin_op(ws + OFF_G, 663552u, dt * 9, 18, 0, wave);
  const Op ob1 = CFG::tr_op(ws + OFF_Y1, 2359296u, st * 128, S_ * 2, 0);
  const Op oa2 = CFG::lin_op(ws + OFF_U, 5308416u, dt * 9, 144, ks * 20 - n1, wave);
  const Op ob2 = CFG::tr_op(w2, 18874368u, st * 128, S_ * 2, ks * 20 - n1);
  const unsigned voffA = lane * 16, voffB = CFG::tr_voff(wave, lane, S_ * 2);
  const float fscale = *(const float*)(ws + OFF_WMAX + 2048);
  auto issue = [&](int kt, int so) {
    const bool p1 = kt < n1;
    Op oa, ob;
    oa.base = p1 ? oa1.base : oa2.base; oa.bytes = p1 ? oa1.bytes : oa2.bytes; oa.s0 = p1 ? oa1.s0 : oa2.s0;
    oa.kstride = 2048u; oa.mstride = p1 ? oa1.mstride : oa2.mstride;
    ob.base = p1 ? ob1.base : ob2.base; ob.bytes = p1 ? ob1.bytes : ob2.bytes; ob.s0 = p1 ? ob1.s0 : ob2.s0;
    ob.kstride = ob2.kstride; ob.mstride = ob2.mstride;
    CFG::issue_op<CFG::IA, CFG::GA>(smem, so, oa, voffA, kt, kt < nt, wave);
    CFG::issue_op<CFG::IB, CFG::GB>(smem, so + CFG::A_BYTES, ob, voffB, kt, kt < nt, wave);
  };
  unsigned addrA[1], addrB[NFB_W];
  CFG::frag_addr<false, NFA_W>(addrA, 0, 0, lane, lane * 16);
  CFG::frag_addr<true, NFB_W>(addrB, CFG::A_BYTES, wave, lane, 0);
  f32x4 acc[NFA_W][NFB_W] = {};
  u32x4 fa0[NFA_W], fb0[NFB_W], fa1[NFA_W] = {}, fb1[NFB_W] = {};
#pragma unroll
  for (int p = 0; p < NS - 1; ++p) issue(p, p * CFG::STAGE);
  int rslot = 0, wslot = NS - 1;
  int tau = 0;
  for (; tau < n1; ++tau) GEMM_ITER(CFG, true, true, issue, (void)0)
  auto scale_acc = [&]() {
#pragma unroll
    for (int i = 0; i < NFA_W; ++i)
#pragma unroll
      for (int j = 0; j < NFB_W; ++j) acc[i][j] *= fscale;
  };
  GEMM_ITER(CFG, true, false, issue, scale_acc())
  for (++tau; tau < nt; ++tau) GEMM_ITER(CFG, false, false, issue, (void)0)
  CFG::mma<false>(acc, fa1, fb1);
#pragma unroll
  for (int j = 0; j < NFB_W; ++j) {
    const int sf = st * 8 + wave * NFB_W + j;
#pragma unroll
    for (int i = 0; i < NFA_W; ++i)
      *(f32x4*)(ws + OFF_SLAB + (((size_t)ks * 128 + sf) * 36 + dt * 9 + i) * 1024 + lane * 16) = acc[i][j];
  }
}

__global__ __launch_bounds__(256) void k_reduce(const u16* __restrict__ x, const u16* __restrict__ b2, const char* __restrict__ ws,
                                                u16* __restrict__ out) {
  const int lane = threadIdx.x & 63;
  const int tile = blockIdx.x * 4 + (threadIdx.x >> 6);
  const int sf = tile / 36, df = tile - sf * 36;
  const char* p = ws + OFF_SLAB + (size_t)tile * 1024 + lane * 16;
  f32x4 a = *(const f32x4*)p;
  a += *(const f32x4*)(p + 4718592);
  a += *(const f32x4*)(p + 2 * 4718592);
  a += *(const f32x4*)(p + 3 * 4718592);
  const int s = sf * 16 + (lane & 15), d = df * 16 + (lane >> 4) * 4;
  const u32x2 xv = *(const u32x2*)(x + (size_t)s * D_ + d);
  const float bias = lo16(b2[s]);
  u32x2 o = {pack_bf16(a[0] + bias + lo16(xv[0]), a[1] + bias + hi16(xv[0])), pack_bf16(a[2] + bias + lo16(xv[1]), a[3] + bias + hi16(xv[1]))};
  *(u32x2*)(out + (size_t)s * D_ + d) = o;
}

extern "C" void kernel_launch(void* const* d_in, const int* in_sizes, int n_in, void* d_out, int out_size, void* d_ws, size_t ws_size,
                              hipStream_t stream) {
  const u16* x = (const u16*)d_in[0];
  const u16* wr = (const u16*)d_in[1];
  const u16* wi = (const u16*)d_in[2];
  const char* w1 = (const char*)d_in[4];
  const u16* b1 = (const u16*)d_in[5];
  const char* w2 = (const char*)d_in[6];
  const u16* b2 = (const u16*)d_in[7];
  char* ws = (char*)d_ws;
  constexpr int L_RD = GC<4, 3, 2, 2, false, false, 4>::LDS_BYTES;
  constexpr int L_G1 = GC<4, 3, 2, 2, true, false, 5>::LDS_BYTES;
  constexpr int L_G2 = GC<9, 2, 1, 4, false, true, 4>::LDS_BYTES;
  (void)hipFuncSetAttribute((const void*)k_rowdft, hipFuncAttributeMaxDynamicSharedMemorySize, L_RD);
  (void)hipFuncSetAttribute((const void*)k_gemm1, hipFuncAttributeMaxDynamicSharedMemorySize, L_G1);
  (void)hipFuncSetAttribute((const void*)k_gemm2, hipFuncAttributeMaxDynamicSharedMemorySize, L_G2);
  (void)hipFuncSetAttribute((const void*)k_colfft, hipFuncAttributeMaxDynamicSharedMemorySize, TAB_BYTES);
  k_prep<<<512, 256, 0, stream>>>(x, wr, wi, ws);
  k_rowdft<<<96, 256, L_RD, stream>>>(ws);
  k_colfft<<<73, 256, TAB_BYTES, stream>>>(ws);
  k_gemm1<<<216, 256, L_G1, stream>>>(w1, b1, ws);
  k_gemm2<<<256, 256, L_G2, stream>>>(w2, ws);
  k_reduce<<<1152, 256, 0, stream>>>(x, b2, ws, (u16*)d_out);
}
```

```cpp
#include <hip/hip_runtime.h>
#include <hip/hip_bf16.h>
#include <stdint.h>

#define DI __device__ __forceinline__
#define LAS __attribute__((address_space(3)))
typedef unsigned short u16;
typedef __attribute__((ext_vector_type(2))) float f32x2;
typedef __attribute__((ext_vector_type(4))) float f32x4;
typedef __attribute__((ext_vector_type(16))) float f32x16;
typedef __attribute__((ext_vector_type(2))) _Float16 f16x2;
typedef __attribute__((ext_vector_type(8))) _Float16 f16x8;
typedef __attribute__((ext_vector_type(2))) __bf16 bf16x2;
typedef __attribute__((ext_vector_type(8))) __bf16 bf16x8;
typedef __attribute__((ext_vector_type(4))) short s16x4;
typedef __attribute__((ext_vector_type(2))) unsigned u32x2;
typedef __attribute__((ext_vector_type(4))) unsigned u32x4;

#define PF1_ROWS 640
#define NRD 3
#define PF2_ROWS 128
#define G2_MAP 0
#define NCF 4

constexpr int S_ = 2048, D_ = 576, F_ = 4608, NC_ = 289;

constexpr size_t OFF_H    = 0;
constexpr size_t OFF_XN   = OFF_H + 2359296;
constexpr size_t OFF_F1   = OFF_XN + 2359296;
constexpr size_t OFF_G    = OFF_F1 + 663552;
constexpr size_t OFF_WT   = OFF_G + 663552;
constexpr size_t OFF_TAB  = OFF_WT + 2367488;
constexpr size_t OFF_WMAX = OFF_TAB + 65536;
constexpr size_t OFF_X1   = OFF_WMAX + 4096;
constexpr size_t OFF_Y1   = OFF_X1 + 2359296;
constexpr size_t OFF_U    = OFF_Y1 + 2359296;
constexpr size_t OFF_SLAB = OFF_U + 5308416;
constexpr size_t WS_END   = OFF_SLAB + 18874368;
constexpr int TAB_E1 = 0, TAB_E2 = 6144, TAB_TW1 = 30720, TAB_TW2 = 47104, TAB_BYTES = 63488;

DI unsigned pack_bf16(float a, float b) { f32x2 v = {a, b}; return __builtin_bit_cast(unsigned, __builtin_convertvector(v, bf16x2)); }
DI unsigned pack_f16(float a, float b) { f32x2 v = {a, b}; return __builtin_bit_cast(unsigned, __builtin_convertvector(v, f16x2)); }
DI float lo16(unsigned p) { return __uint_as_float(p << 16); }
DI float hi16(unsigned p) { return __uint_as_float(p & 0xffff0000u); }
DI float rbf(float a) { return lo16(pack_bf16(a, a)); }

template <int CTRL> DI float dpp_add(float v) {
  return v + __int_as_float(__builtin_amdgcn_update_dpp(0, __float_as_int(v), CTRL, 0xf, 0xf, true));
}
DI float sum32(float v) {
  v = dpp_add<0xB1>(v); v = dpp_add<0x4E>(v); v = dpp_add<0x141>(v); v = dpp_add<0x140>(v);
  auto r = __builtin_amdgcn_permlane16_swap(__float_as_uint(v), __float_as_uint(v), false, false);
  return __uint_as_float(r[0]) + __uint_as_float(r[1]);
}

#define STAMP_BEGIN(k)
#define STAMP_END()
#define WAIT_VM(n) asm volatile("s_waitcnt vmcnt(%0)" ::"n"(n) : "memory")
#define WAIT_LGKM0() asm volatile("s_waitcnt lgkmcnt(0)" ::: "memory")
#define SCHED0() __builtin_amdgcn_sched_barrier(0)

DI u32x4 mk_rsrc(const void* p, unsigned bytes) {
  const unsigned long long a = (unsigned long long)p;
  return u32x4{(unsigned)a, (unsigned)(a >> 32) & 0xffffu, bytes, 0x00020000u};
}
DI void dma16(u32x4 rsrc, unsigned lds_off, unsigned voff, unsigned soff) {
  asm volatile("s_mov_b32 m0, %0\n\ts_nop 0\n\tbuffer_load_dwordx4 %1, %2, %3 offen lds" ::"s"(lds_off), "v"(voff), "s"(rsrc), "s"(soff) : "memory");
}
DI void dma4(u32x4 rsrc, unsigned lds_off, unsigned voff, unsigned soff) {
  asm volatile("s_mov_b32 m0, %0\n\ts_nop 0\n\tbuffer_load_dword %1, %2, %3 offen lds" ::"s"(lds_off), "v"(voff), "s"(rsrc), "s"(soff) : "memory");
}
DI void warm_rows(const char* base, unsigned bytes, unsigned ld, int r0, int r1, unsigned c0, int nl, int mode, int worker, int nworkers,
                  int lane, unsigned lds_dummy) {
  const u32x4 r = mk_rsrc(base, bytes);
  const int total = (r1 - r0) * nl;
  for (int i0 = worker * 64; i0 < total; i0 += nworkers * 64) {
    const int i = i0 + lane;
    const int rr = i / nl, l = i - rr * nl, row = r0 + rr;
    const unsigned prow = mode == 1 ? (unsigned)(64 * (row & 31) + (row >> 5)) : (mode == 2 ? (unsigned)((row & 3) * 1152 + (row >> 2)) : (unsigned)row);
    const unsigned voff = i < total ? prow * ld + c0 + (unsigned)l * 128u : 0xfffffff0u;
    dma4(r, lds_dummy, voff, 0u);
    WAIT_VM(7);
  }
  WAIT_VM(0);
}

DI float dft576(int kk, unsigned n) {
  const float r = (float)n * (1.0f / 576.0f);
  return ((kk & 1) && kk != 1) ? -__builtin_amdgcn_sinf(r) : __builtin_amdgcn_cosf(r);
}

__global__ __launch_bounds__(256) void k_prep(const u16* __restrict__ x, const u16* __restrict__ wr, const u16* __restrict__ wi,
                                              const char* __restrict__ w1, char* __restrict__ ws) {
  __shared__ __attribute__((aligned(16))) char lds[19456];
  const int b = blockIdx.x, tid = threadIdx.x;
  STAMP_BEGIN(0)
  if (b < 256) {
    constexpr int RS = 1216;
    const int s2 = 8 * (b & 7) + (b >> 5), t = (b >> 4) & 1, h = (b >> 3) & 1;
    const int rr = tid >> 5, l32 = tid & 31;
    const int s1 = 16 * t + 8 * (rr >> 2) + 4 * h + (rr & 3);
    const unsigned* xr = (const unsigned*)(x + (size_t)(64 * s1 + s2) * D_);
    unsigned raw[9];
#pragma unroll
    for (int i = 0; i < 9; ++i) raw[i] = xr[32 * i + l32];
    float v[18];
#pragma unroll
    for (int i = 0; i < 9; ++i) { v[2 * i] = lo16(raw[i]); v[2 * i + 1] = hi16(raw[i]); }
#pragma unroll
    for (int pass = 0; pass < 2; ++pass) {
      float sm = 0.f;
#pragma unroll
      for (int i = 0; i < 18; ++i) sm += v[i];
      const float mean = sum32(sm) * (1.0f / 576.0f);
      float sq = 0.f;
#pragma unroll
      for (int i = 0; i < 18; ++i) { float dlt = v[i] - mean; sq += dlt * dlt; }
      const float var = sum32(sq) * (1.0f / 576.0f);
      const float mb = rbf(mean);
      const float sd = rbf(__builtin_sqrtf(rbf(rbf(var) + 1e-5f)));
      const float rs = 1.0f / sd;
      char* dst = lds + pass * (8 * RS) + rr * RS + l32 * 4;
#pragma unroll
      for (int i = 0; i < 9; ++i) {
        unsigned p = pack_bf16(v[2 * i] - mb, v[2 * i + 1] - mb);
        p = pack_bf16(lo16(p) * rs, hi16(p) * rs);
        v[2 * i] = lo16(p); v[2 * i + 1] = hi16(p);
        *(unsigned*)(dst + i * 128) = pass == 0 ? pack_f16(v[2 * i], v[2 * i + 1]) : p;
      }
    }
    __syncthreads();
#pragma unroll
    for (int it = 0; it < 3; ++it) {
      const int p = tid + it * 256;
      if (p < 576) {
        const int run = p >= 288, rem = p - run * 288, dp = rem >> 2, q = rem & 3;
        u32x4 val = *(const u32x4*)(lds + (run * 4 + q) * RS + dp * 16);
        *(u32x4*)(ws + OFF_XN + (size_t)s2 * 36864 + dp * 512 + (16 * t + 8 * run + 4 * h + q) * 16) = val;
      }
    }
    const int wave = tid >> 6, lane = tid & 63;
    for (int cb = wave; cb < 9; cb += 4) {
      const int gg = lane >> 4, q = (lane & 15) >> 2, pp = lane & 3;
      const char* src = lds + 8 * RS + q * RS + (cb * 64 + gg * 16 + pp * 4) * 2;
      s16x4 lo = __builtin_amdgcn_ds_read_tr16_b64_v4i16((LAS s16x4*)src);
      s16x4 hi = __builtin_amdgcn_ds_read_tr16_b64_v4i16((LAS s16x4*)(src + 4 * RS));
      u32x2 a = __builtin_bit_cast(u32x2, lo), c = __builtin_bit_cast(u32x2, hi);
      u32x4 val = {a[0], a[1], c[0], c[1]};
      const int df = cb * 2 + (gg >> 1), i = 8 * q + 4 * (gg & 1) + pp;
      *(u32x4*)(ws + OFF_H + ((size_t)df * 128 + 2 * s2 + t) * 1024 + (h * 32 + i) * 16) = val;
    }
  } else if (b < 512) {
    const int ab_ = b - 256;
    {
      const int rg = ab_ >> 2, cg = ab_ & 3;
      const int c0 = cg * 72 + (cg > 0), nc = cg == 0 ? 73 : 72;
      const int kq = rg >> 2, ko = rg & 3;
      unsigned rv[10], iv[10];
#pragma unroll
      for (int it = 0; it < 10; ++it) {
        const int idx = tid + it * 256, row = idx / 73, col = idx - row * 73;
        const bool ok = idx < 32 * 73 && col < nc;
        const int ks = 8 * ko + (row & 7) + 32 * (4 * kq + (row >> 3));
        const int off = ok ? ks * NC_ + c0 + col : 0;
        rv[it] = wr[off]; iv[it] = wi[off];
      }
      unsigned mx = 0;
#pragma unroll
      for (int it = 0; it < 10; ++it) {
        const int idx = tid + it * 256, row = idx / 73, col = idx - row * 73;
        if (idx < 32 * 73 && col < nc) {
          mx = max(mx, max(rv[it] & 0x7fffu, iv[it] & 0x7fffu));
          *(unsigned*)(lds + col * 144 + (row & 7) * 16 + (row >> 3) * 4) = rv[it] | (iv[it] << 16);
        }
      }
#pragma unroll
      for (int o = 32; o; o >>= 1) mx = max(mx, (unsigned)__shfl_xor((int)mx, o));
      unsigned* red = (unsigned*)(lds + 16384);
      if ((tid & 63) == 0) red[tid >> 6] = mx;
      __syncthreads();
      if (tid == 0) ((unsigned*)(ws + OFF_WMAX))[ab_] = max(max(red[0], red[1]), max(red[2], red[3]));
      const int U = kq >> 3, h = (kq >> 2) & 1, rq = kq & 3;
#pragma unroll
      for (int it = 0; it < 3; ++it) {
        const int p = tid + it * 256;
        if (p < nc * 8) {
          const int col = p >> 3, a = p & 7;
          u32x4 val = *(const u32x4*)(lds + col * 144 + a * 16);
          *(u32x4*)(ws + OFF_WT + (size_t)(c0 + col) * 8192 + ((U * 4 + rq) * 64 + h * 32 + 8 * ko + a) * 16) = val;
        }
      }
    }
  } else if (b < 852) {
    const int P = (b - 512) * 256 + tid;
    if (P < 86912) {
      u32x4 val; char* dst;
      if (P < 82944) {
        const bool isG = P >= 41472;
        const int PP = isG ? P - 41472 : P;
        const int frag = PP >> 6, lane = PP & 63, fr = frag / 36, ks = frag - fr * 36, i = lane & 31, h = lane >> 5;
        float o[8];
        if (!isG) {
          const int kk = 32 * fr + i; const unsigned c = kk == 1 ? 288 : kk >> 1, d0 = 16 * ks + 8 * h;
          unsigned n = (c * d0) % 576u;
#pragma unroll
          for (int j = 0; j < 8; ++j) { o[j] = dft576(kk, n); n += c; if (n >= 576u) n -= 576u; }
        } else {
          const unsigned d = 32 * fr + 16 * ((i >> 2) & 1) + 4 * (i >> 3) + (i & 3);
#pragma unroll
          for (int j = 0; j < 8; ++j) {
            const int kk = 16 * ks + 8 * (j >> 2) + 4 * h + (j & 3); const unsigned c = kk == 1 ? 288 : kk >> 1;
            o[j] = dft576(kk, (c * d) % 576u);
          }
        }
        val = u32x4{pack_f16(o[0], o[1]), pack_f16(o[2], o[3]), pack_f16(o[4], o[5]), pack_f16(o[6], o[7])};
        dst = ws + (isG ? OFF_G : OFF_F1) + (size_t)PP * 16;
      } else if (P < 82944 + 384 + 1536) {
        const int PP = P - 82944, lane = PP & 63, r = lane & 31, h = lane >> 5;
        int frag = PP >> 6;
        float o[8];
        if (frag < 6) {
          const int t = frag / 3, which = frag - 3 * t;
#pragma unroll
          for (int j = 0; j < 8; ++j) {
            const int kap = 16 * t + 8 * (j >> 2) + 4 * h + (j & 3);
            const float a = (float)((kap * r) & 31) * (1.0f / 32.0f);
            o[j] = which == 0 ? __builtin_amdgcn_cosf(a) : (which == 1 ? -__builtin_amdgcn_sinf(a) : __builtin_amdgcn_sinf(a));
          }
        } else {
          frag -= 6;
          const int idx = frag / 3, which = frag - 3 * idx, U = idx >> 2, T = (idx >> 1) & 1, u = idx & 1;
          const int a_ = 32 * U + 16 * ((r >> 2) & 1) + 4 * (r >> 3) + (r & 3);
#pragma unroll
          for (int j = 0; j < 8; ++j) {
            const int bi = 32 * T + 16 * h + 8 * u + j;
            const float a = (float)((a_ * bi) & 63) * (1.0f / 64.0f);
            o[j] = which == 0 ? __builtin_amdgcn_cosf(a) : (which == 1 ? -__builtin_amdgcn_sinf(a) : __builtin_amdgcn_sinf(a));
          }
        }
        val = u32x4{pack_f16(o[0], o[1]), pack_f16(o[2], o[3]), pack_f16(o[4], o[5]), pack_f16(o[6], o[7])};
        dst = ws + OFF_TAB + (size_t)PP * 16;
      } else {
        const int PP = P - (82944 + 384 + 1536);
        const bool inv = PP >= 1024;
        const int Q = inv ? PP - 1024 : PP;
        const int lane = Q & 63, p = (Q >> 6) & 7, T = Q >> 9, r = lane & 31, h = lane >> 5;
        float o[4];
#pragma unroll
        for (int z = 0; z < 2; ++z) {
          const int rho = 2 * p + z;
          int n;
          if (!inv) n = (32 * T + 16 * h + rho) * r;
          else n = (32 * T + 16 * ((r >> 2) & 1) + 4 * (r >> 3) + (r & 3)) * ((rho & 3) + 8 * (rho >> 2) + 4 * h);
          const float a = (float)(n & 2047) * (1.0f / 2048.0f);
          o[2 * z] = __builtin_amdgcn_cosf(a);
          o[2 * z + 1] = inv ? __builtin_amdgcn_sinf(a) : -__builtin_amdgcn_sinf(a);
        }
        val = u32x4{__float_as_uint(o[0]), __float_as_uint(o[1]), __float_as_uint(o[2]), __float_as_uint(o[3])};
        dst = ws + OFF_TAB + TAB_TW1 + (size_t)PP * 16;
      }
      *(u32x4*)dst = val;
    }
  } else {
    const int pb = b - 856;
    if (pb < 0 || PF1_ROWS == 0) { STAMP_END(); return; }
    const int g = b & 7, wave = __builtin_amdgcn_readfirstlane(tid >> 6), nb = (gridDim.x - 856) >> 3;
    const int ft0 = (g * 27) / 6, ft1 = (g * 27 + 26) / 6;
    warm_rows(w1, 18874368u, F_ * 2, 0, PF1_ROWS, ft0 * 256u, (ft1 - ft0 + 1) * 2, 1, (pb >> 3) * 4 + wave, nb * 4, tid & 63,
              (unsigned)(size_t)(LAS char*)lds + wave * 256);
  }
  STAMP_END();
}

template <bool F16> DI f32x16 mfma32(u32x4 a, u32x4 b, f32x16 c) {
  if constexpr (F16) return __builtin_amdgcn_mfma_f32_32x32x16_f16(__builtin_bit_cast(f16x8, a), __builtin_bit_cast(f16x8, b), c, 0, 0, 0);
  else return __builtin_amdgcn_mfma_f32_32x32x16_bf16(__builtin_bit_cast(bf16x8, a), __builtin_bit_cast(bf16x8, b), c, 0, 0, 0);
}
enum { LIN = 0, TR128 = 1, TR64 = 2 };

template <int V> struct IC { static constexpr int value = V; };
template <int NA_W, int NB_W, int WA, int WB, int AK, int BK, int NL, int NS, int DPM, int DEPTH>
struct GC {
  static constexpr int NC = WA * WB, NFA = NA_W * WA, NFB = NB_W * WB;
  static constexpr int A_BYTES = NFA * 4096, B_BYTES = NFB * 4096, STAGE = A_BYTES + B_BYTES;
  static constexpr int IA = NFA * 4, IB = NFB * 4, NI = IA + IB, G = NI / NL;
  static constexpr int LDS_BYTES = NS * STAGE;
  static_assert(NI % NL == 0, "");
  static_assert(AK == LIN || NFA == (AK == TR128 ? 4 : 2), "");
  static_assert(BK == LIN || NFB == (BK == TR128 ? 4 : 2), "");

  template <int K> static DI unsigned voff(int lane, unsigned rstride) {
    if constexpr (K == LIN) return lane * 16;
    else if constexpr (K == TR128) return (unsigned)(lane >> 4) * rstride + (unsigned)(((lane & 15) ^ ((lane >> 4) << 2)) * 16);
    else return (unsigned)(lane >> 3) * rstride + (unsigned)(((lane & 7) ^ (((lane >> 4) & 1) << 2)) * 16);
  }
  template <int K, int NF> static DI void frag_addr(unsigned (&addr)[K == LIN ? 1 : NF], int opoff, int wpos, int lane) {
    if constexpr (K == LIN) addr[0] = opoff + wpos * NF * 4096 + lane * 16;
    else {
      const int h = lane >> 5, blk = (lane >> 4) & 1, q = (lane & 15) >> 2, p = lane & 3;
#pragma unroll
      for (int i = 0; i < NF; ++i) {
        const int ch = 4 * (wpos * NF + i) + 2 * blk + (p >> 1);
        if constexpr (K == TR128) addr[i] = opoff + (4 * h + q) * 256 + ((ch ^ (q << 2)) * 16) + 8 * (p & 1);
        else addr[i] = opoff + (4 * h + q) * 128 + ((ch ^ (((q >> 1) & 1) << 2)) * 16) + 8 * (p & 1);
      }
    }
  }
  template <int K, int NF> static DI void rd(u32x4 (&f)[NF], const char* smem, const unsigned (&addr)[K == LIN ? 1 : NF], int sb, int hh) {
#pragma unroll
    for (int i = 0; i < NF; ++i) {
      if constexpr (K == LIN) f[i] = *(const u32x4*)(smem + addr[0] + sb + (i * 4 + hh) * 1024);
      else {
        constexpr int RB = K == TR128 ? 256 : 128;
        const char* p = smem + addr[i] + sb + hh * 16 * RB;
        s16x4 lo = __builtin_amdgcn_ds_read_tr16_b64_v4i16((LAS s16x4*)p);
        s16x4 hi = __builtin_amdgcn_ds_read_tr16_b64_v4i16((LAS s16x4*)(p + 8 * RB));
        u32x2 a = __builtin_bit_cast(u32x2, lo), c = __builtin_bit_cast(u32x2, hi);
        f[i] = u32x4{a[0], a[1], c[0], c[1]};
      }
    }
  }
  static DI void ilv() {
    constexpr int NR = NA_W * (AK == LIN ? 1 : 2) + NB_W * (BK == LIN ? 1 : 2), NM = NA_W * NB_W;
    constexpr int NG = (NR + DPM - 1) / DPM < NM ? (NR + DPM - 1) / DPM : NM;
#pragma unroll
    for (int i = 0; i < NG; ++i) {
      __builtin_amdgcn_sched_group_barrier(0x8, 1, 0);
      if (i + 1 < NG) __builtin_amdgcn_sched_group_barrier(0x100, DPM, 0);
      else __builtin_amdgcn_sched_group_barrier(0x100, NR - DPM * (NG - 1), 0);
    }
    if constexpr (NM > NG) __builtin_amdgcn_sched_group_barrier(0x8, NM - NG, 0);
    SCHED0();
  }
  template <class SOFF>
  static DI void load(char* smem, int lw, int nt, const char* baseA, unsigned bytesA, unsigned vA, const char* baseB, unsigned bytesB,
                      unsigned vB, SOFF soff) {
    const unsigned ldsbase = (unsigned)(size_t)(LAS char*)smem + lw * G * 1024;
    unsigned vo[G];
#pragma unroll
    for (int m = 0; m < G; ++m) vo[m] = (lw * G + m) < IA ? vA : vB;
    auto issue = [&](int kt, int so) {
#pragma unroll
      for (int m = 0; m < G; ++m) {
        const bool isA = (lw * G + m) < IA;
        dma16(mk_rsrc(isA ? baseA : baseB, kt < nt ? (isA ? bytesA : bytesB) : 0u), ldsbase + so + m * 1024, vo[m], soff(kt, lw * G + m));
      }
    };
#pragma unroll
    for (int p = 0; p < NS - 2; ++p) issue(p, p * STAGE);
    int wslot = NS - 2;
    for (int tau = 0; tau < nt; ++tau) {
      WAIT_VM((NS - 3) * G);
      __builtin_amdgcn_s_barrier();
      issue(tau + NS - 2, wslot * STAGE);
      wslot = (wslot + 1 == NS) ? 0 : wslot + 1;
    }
    WAIT_VM(0);
  }
  template <bool F16>
  static DI void consume(const char* smem, int wave, int lane, int n, int& rslot, f32x16 (&acc)[NA_W][NB_W]) {
    const int wa = wave / WB, wb = wave - wa * WB;
    unsigned addrA[AK == LIN ? 1 : NA_W], addrB[BK == LIN ? 1 : NB_W];
    frag_addr<AK, NA_W>(addrA, 0, wa, lane);
    frag_addr<BK, NB_W>(addrB, A_BYTES, wb, lane);
    u32x4 fa[DEPTH][NA_W], fb[DEPTH][NB_W];
#pragma unroll
    for (int d = 1; d < DEPTH; ++d) {
#pragma unroll
      for (int i = 0; i < NA_W; ++i) fa[d][i] = u32x4{0, 0, 0, 0};
#pragma unroll
      for (int i = 0; i < NB_W; ++i) fb[d][i] = u32x4{0, 0, 0, 0};
    }
    auto mm = [&](const u32x4 (&a)[NA_W], const u32x4 (&b)[NB_W]) {
#pragma unroll
      for (int i = 0; i < NA_W; ++i)
#pragma unroll
        for (int j = 0; j < NB_W; ++j) acc[i][j] = mfma32<F16>(a[i], b[j], acc[i][j]);
    };
    auto step = [&](auto Z0) {
      constexpr int z0 = decltype(Z0)::value;
      __builtin_amdgcn_s_barrier();
      SCHED0();
      const int sb = rslot * STAGE;
#pragma unroll
      for (int hh = 0; hh < 4; ++hh) {
        rd<AK, NA_W>(fa[(z0 + hh) % DEPTH], smem, addrA, sb, hh);
        rd<BK, NB_W>(fb[(z0 + hh) % DEPTH], smem, addrB, sb, hh);
        mm(fa[(z0 + hh + 1) % DEPTH], fb[(z0 + hh + 1) % DEPTH]);
        ilv();
      }
      rslot = (rslot + 1 == NS) ? 0 : rslot + 1;
    };
    if constexpr (DEPTH == 2) {
      for (int tau = 0; tau < n; ++tau) step(IC<0>{});
      mm(fa[1], fb[1]);
    } else {
      int tau = 0;
      for (; tau + 3 <= n; tau += 3) { step(IC<0>{}); step(IC<1>{}); step(IC<2>{}); }
      if (n - tau == 2) { step(IC<0>{}); step(IC<1>{}); mm(fa[0], fb[0]); mm(fa[1], fb[1]); }
      else if (n - tau == 1) { step(IC<0>{}); mm(fa[2], fb[2]); mm(fa[0], fb[0]); }
      else { mm(fa[1], fb[1]); mm(fa[2], fb[2]); }
    }
  }
};

DI u32x4 pack8_f16(const f32x16& v, int u) {
  return u32x4{pack_f16(v[8 * u], v[8 * u + 1]), pack_f16(v[8 * u + 2], v[8 * u + 3]), pack_f16(v[8 * u + 4], v[8 * u + 5]), pack_f16(v[8 * u + 6], v[8 * u + 7])};
}
DI float gelu_tanh(float u) {
  const float z = u * (1.0f + 0.044715f * u * u) * (-2.0f * 0.7978845608028654f * 1.4426950408889634f);
  return u * __builtin_amdgcn_rcpf(1.0f + __builtin_amdgcn_exp2f(z));
}

#define K2_NS 5
using CG1 = GC<1, 3, 4, 1, TR128, LIN, 7, K2_NS, 2, 3>;
using CRD = GC<1, 3, 4, 1, LIN, LIN, 7, K2_NS, 2, 3>;

__global__ __launch_bounds__(704, 3) void k_g1(const char* __restrict__ w1, const u16* __restrict__ b1, const char* __restrict__ w2,
                                               char* __restrict__ ws) {
  extern __shared__ __attribute__((aligned(16))) char smem[];
  const int lane = threadIdx.x & 63, wave = __builtin_amdgcn_readfirstlane(threadIdx.x >> 6);
  const int g = blockIdx.x & 7, idx = blockIdx.x >> 3;
  STAMP_BEGIN(1)
  if (idx < 27) {
    const int tile = g * 27 + idx, ft = tile / 6, dt = tile - ft * 6;
    if (wave >= 4) {
      constexpr unsigned ld = F_ * 2;
      CG1::load(smem, wave - 4, 32, w1, 18874368u, CG1::voff<TR128>(lane, 64 * ld), ws + OFF_H, 2359296u, CG1::voff<LIN>(lane, 0),
                [&](int kt, int a) -> unsigned {
                  return a < 16 ? (unsigned)(((4 * a) & 31) * 64 + 2 * kt + (a >> 3)) * ld + ft * 256u
                                : (unsigned)((dt * 3 + ((a - 16) >> 2)) * 128 + kt * 4 + ((a - 16) & 3)) * 1024u;
                });
      return;
    }
    f32x16 acc[1][3] = {};
    int rslot = 0;
    CG1::consume<false>(smem, wave, lane, 32, rslot, acc);
    const int h = lane >> 5, f0 = ft * 128 + wave * 32;
    float bias[16];
#pragma unroll
    for (int q = 0; q < 4; ++q) {
      const u32x2 bv = *(const u32x2*)(b1 + f0 + 8 * q + 4 * h);
      bias[4 * q] = lo16(bv[0]); bias[4 * q + 1] = hi16(bv[0]); bias[4 * q + 2] = lo16(bv[1]); bias[4 * q + 3] = hi16(bv[1]);
    }
#pragma unroll
    for (int j = 0; j < 3; ++j)
#pragma unroll
      for (int s = 0; s < 2; ++s) {
        u32x4 val;
#pragma unroll
        for (int e = 0; e < 4; ++e) {
          const int r = 8 * s + 2 * e;
          unsigned pk = pack_bf16(acc[0][j][r], acc[0][j][r + 1]);
          pk = pack_bf16(lo16(pk) + bias[r], hi16(pk) + bias[r + 1]);
          val[e] = pack_bf16(gelu_tanh(lo16(pk)), gelu_tanh(hi16(pk)));
        }
        *(u32x4*)(ws + OFF_U + ((size_t)(dt * 3 + j) * 288 + (f0 >> 4) + s) * 1024 + lane * 16) = val;
      }
  } else if (idx < 27 + NRD) {
    constexpr int NB = NRD > 0 ? NRD : 1, ntile = 12 / NB;
    const int me = idx - 27;
    auto tile_of = [&](int ti, int& rb, int& kb) { const int tl = me * ntile + ti; kb = tl >> 1; rb = 2 * g + (tl & 1); };
    if (wave >= 4) {
      CRD::load(smem, wave - 4, ntile * 9, ws + OFF_XN, 2359296u, lane * 16, ws + OFF_F1, 663552u, lane * 16,
                [&](int kt, int a) -> unsigned {
                  const int ti = kt / 9, k = kt - ti * 9; int rb, kb; tile_of(ti, rb, kb);
                  return a < 16 ? (unsigned)((rb * 4 + (a >> 2)) * 36 + k * 4 + (a & 3)) * 1024u
                                : (unsigned)((kb * 3 + ((a - 16) >> 2)) * 36 + k * 4 + ((a - 16) & 3)) * 1024u;
                });
      return;
    }
    int rslot = 0;
    for (int ti = 0; ti < ntile; ++ti) {
      int rb, kb; tile_of(ti, rb, kb);
      f32x16 acc[1][3] = {};
      CRD::consume<true>(smem, wave, lane, 9, rslot, acc);
      const int s2 = rb * 4 + wave, T = s2 >> 5, r = 8 * ((s2 >> 2) & 3) + 4 * ((s2 >> 4) & 1) + (s2 & 3), h = lane >> 5;
#pragma unroll
      for (int j = 0; j < 3; ++j) {
        const int kk = (kb * 3 + j) * 32 + (lane & 31);
        const f32x16 a = acc[0][j] * (1.0f / 32.0f);
        char* dst = ws + OFF_X1 + (size_t)kk * 4096 + T * 2048 + (h * 32 + r) * 16;
        *(u32x4*)dst = pack8_f16(a, 0);
        *(u32x4*)(dst + 1024) = pack8_f16(a, 1);
      }
    }
  } else {
    constexpr int NP = 32 - 27 - NRD;
    const int worker = (idx - 27 - NRD) * 11 + wave;
    const unsigned dummy = (unsigned)(size_t)(LAS char*)smem + wave * 256;
    const int ft0 = (g * 27) / 6, ft1 = (g * 27 + 26) / 6;
    warm_rows(w1, 18874368u, F_ * 2, PF1_ROWS, 2048, ft0 * 256u, (ft1 - ft0 + 1) * 2, 1, worker, NP * 11, lane, dummy);
    warm_rows(w2, 18874368u, S_ * 2, (g >> 1) * 1152, (g >> 1) * 1152 + PF2_ROWS, (g & 1) * 2048u, 16, 0, worker, NP * 11, lane, dummy);
  }
  STAMP_END();
}

DI void twiddle(f32x16& re, f32x16& im, const char* tw) {
#pragma unroll
  for (int p = 0; p < 8; ++p) {
    const f32x4 w = *(const f32x4*)(tw + p * 1024);
    const float r0 = re[2 * p], i0 = im[2 * p], r1 = re[2 * p + 1], i1 = im[2 * p + 1];
    re[2 * p] = r0 * w[0] - i0 * w[1]; im[2 * p] = r0 * w[1] + i0 * w[0];
    re[2 * p + 1] = r1 * w[2] - i1 * w[3]; im[2 * p + 1] = r1 * w[3] + i1 * w[2];
  }
}

DI void colfft_wave(const char* tab  , char* ws, int c, int lane, float wsc, const u32x4 (&zr)[2][2], const u32x4 (&zi)[2][2]) {
  const bool cplx = c != 0 && c != 288;
  const int kre = c == 288 ? 1 : 2 * c, kim = 2 * c + 1;
#define EFRAG(off) (*(const u32x4*)(tab + (off)))
#define MF(a, b, c) mfma32<true>(a, b, c)
  u32x4 ar[2][2], ai[2][2];
#pragma unroll
  for (int T = 0; T < 2; ++T) {
    f32x16 re = {}, im = {};
#pragma unroll
    for (int t = 0; t < 2; ++t) {
      const u32x4 er = EFRAG(TAB_E1 + (t * 3 + 0) * 1024), ei = EFRAG(TAB_E1 + (t * 3 + 1) * 1024), en = EFRAG(TAB_E1 + (t * 3 + 2) * 1024);
      re = MF(zr[T][t], er, re); im = MF(zr[T][t], ei, im);
      re = MF(zi[T][t], en, re); im = MF(zi[T][t], er, im);
    }
    twiddle(re, im, tab + TAB_TW1 + T * 8192);
    ar[T][0] = pack8_f16(re, 0); ar[T][1] = pack8_f16(re, 1); ai[T][0] = pack8_f16(im, 0); ai[T][1] = pack8_f16(im, 1);
    SCHED0();
  }
  u32x4 pr[2][2], pi[2][2];
#pragma unroll
  for (int U = 0; U < 2; ++U) {
    u32x4 wv[4];
#pragma unroll
    for (int i = 0; i < 4; ++i) wv[i] = *(const u32x4*)(ws + OFF_WT + (size_t)c * 8192 + (U * 4 + i) * 1024 + lane * 16);
    f32x16 re = {}, im = {};
#pragma unroll
    for (int T = 0; T < 2; ++T)
#pragma unroll
      for (int u = 0; u < 2; ++u) {
        const int o = TAB_E2 + (((U * 2 + T) * 2 + u) * 3) * 1024;
        const u32x4 er = EFRAG(o), ei = EFRAG(o + 1024), en = EFRAG(o + 2048);
        re = MF(er, ar[T][u], re); im = MF(ei, ar[T][u], im);
        re = MF(en, ai[T][u], re); im = MF(er, ai[T][u], im);
      }
#pragma unroll
    for (int rho = 0; rho < 16; ++rho) {
      const unsigned w = wv[rho >> 2][rho & 3];
      const float a = lo16(w) * wsc, b = hi16(w) * wsc, r0 = re[rho], i0 = im[rho];
      re[rho] = r0 * a - i0 * b; im[rho] = r0 * b + i0 * a;
    }
    pr[U][0] = pack8_f16(re, 0); pr[U][1] = pack8_f16(re, 1); pi[U][0] = pack8_f16(im, 0); pi[U][1] = pack8_f16(im, 1);
    SCHED0();
  }
  const float osc = cplx ? 0.25f : 0.125f;
#pragma unroll
  for (int V = 0; V < 2; ++V) {
    f32x16 re = {}, im = {};
#pragma unroll
    for (int U = 0; U < 2; ++U)
#pragma unroll
      for (int u = 0; u < 2; ++u) {
        const int o = TAB_E2 + (((V * 2 + U) * 2 + u) * 3) * 1024;
        const u32x4 er = EFRAG(o), ei = EFRAG(o + 1024), en = EFRAG(o + 2048);
        re = MF(pr[U][u], er, re); im = MF(pr[U][u], en, im);
        re = MF(pi[U][u], ei, re); im = MF(pi[U][u], er, im);
      }
    twiddle(re, im, tab + TAB_TW2 + V * 8192);
    const u32x4 br0 = pack8_f16(re, 0), br1 = pack8_f16(re, 1), bi0 = pack8_f16(im, 0), bi1 = pack8_f16(im, 1);
    SCHED0();
    f32x16 yr = {}, yi = {};
#pragma unroll
    for (int u = 0; u < 2; ++u) {
      const u32x4 br = u ? br1 : br0, bi = u ? bi1 : bi0;
      const u32x4 er = EFRAG(TAB_E1 + (u * 3 + 0) * 1024), ei = EFRAG(TAB_E1 + (u * 3 + 1) * 1024), en = EFRAG(TAB_E1 + (u * 3 + 2) * 1024);
      yr = MF(br, er, yr); yi = MF(br, en, yi);
      yr = MF(bi, ei, yr); yi = MF(bi, er, yi);
    }
    yr *= osc; yi *= osc;
    const int so = ((lane & 31) * 64 + V * 32 + (lane >> 5) * 16) * 2;
    char* dr = ws + OFF_Y1 + (size_t)kre * 4096 + so;
    *(u32x4*)dr = pack8_f16(yr, 0); *(u32x4*)(dr + 16) = pack8_f16(yr, 1);
    if (cplx) {
      char* di = ws + OFF_Y1 + (size_t)kim * 4096 + so;
      *(u32x4*)di = pack8_f16(yi, 0); *(u32x4*)(di + 16) = pack8_f16(yi, 1);
    }
    SCHED0();
  }
#undef EFRAG
#undef MF
}
DI unsigned wmax_exp(const char* ws, int lane) {
  const u32x4 mv = *(const u32x4*)(ws + OFF_WMAX + lane * 16);
  unsigned mx = max(max(mv[0], mv[1]), max(mv[2], mv[3]));
#pragma unroll
  for (int o = 32; o; o >>= 1) mx = max(mx, (unsigned)__shfl_xor((int)mx, o));
  return min(mx >> 7, 252u);
}

constexpr int K3_NS = 4;
#define G2_DEPTH 2
using CG2 = GC<3, 2, 2, 2, LIN, TR128, 8, K3_NS, 2, G2_DEPTH>;

__global__ __launch_bounds__(768, 3) void k_g2(const char* __restrict__ w2, char* __restrict__ ws) {
  extern __shared__ __attribute__((aligned(16))) char smem[];
  const int lane = threadIdx.x & 63, wave = __builtin_amdgcn_readfirstlane(threadIdx.x >> 6);
  const int g = blockIdx.x & 7, idx = blockIdx.x >> 3;
  STAMP_BEGIN(2)
  if (idx < 24) {
    const int ks = g >> 1, st = (g & 1) * 8 + idx / 3, dt = idx % 3;
    if (wave >= 4) {
      constexpr unsigned ld = S_ * 2;
      CG2::load(smem, wave - 4, 18, ws + OFF_U, 5308416u, lane * 16, w2, 18874368u, CG2::voff<TR128>(lane, ld),
                [&](int kt, int a) -> unsigned {
                  return a < 24 ? (unsigned)((dt * 6 + (a >> 2)) * 288 + (ks * 18 + kt) * 4 + (a & 3)) * 1024u
                                : (unsigned)((ks * 18 + kt) * 64 + 4 * (a - 24)) * ld + st * 256u;
                });
      return;
    }
    f32x16 acc[3][2] = {};
    int rslot = 0;
    CG2::consume<false>(smem, wave, lane, 18, rslot, acc);
    const int wa = wave >> 1, wb = wave & 1;
#pragma unroll
    for (int j = 0; j < 2; ++j)
#pragma unroll
      for (int i = 0; i < 3; ++i) {
        char* dst = ws + OFF_SLAB + (((size_t)ks * 64 + st * 4 + wb * 2 + j) * 18 + dt * 6 + wa * 3 + i) * 4096 + lane * 16;
#pragma unroll
        for (int q = 0; q < 4; ++q)
          *(f32x4*)(dst + q * 1024) = f32x4{acc[i][j][4 * q], acc[i][j][4 * q + 1], acc[i][j][4 * q + 2], acc[i][j][4 * q + 3]};
      }
  } else if (idx < 24 + NCF) {
    const int me = g * NCF + (idx - 24);
    constexpr int CPB = (NC_ + 8 * NCF - 1) / (8 * (NCF > 0 ? NCF : 1));
    {
      const u32x4 r = mk_rsrc(ws + OFF_TAB, TAB_BYTES);
      for (int i = wave; i < TAB_BYTES / 1024; i += 12) dma16(r, (unsigned)(size_t)(LAS char*)smem + i * 1024, lane * 16, i * 1024u);
    }
    const unsigned E = wmax_exp(ws, lane);
    const float wsc = __uint_as_float((253u - E) << 23) * (1.0f / 64.0f);
    bool first = true;
    for (int w = wave; w < CPB || first; w += 12) {
      const int c = me * CPB + w;
      const bool ok = w < CPB && c < NC_;
      const bool cplx = c != 0 && c != 288;
      const int kre = c == 288 ? 1 : 2 * c, kim = 2 * c + 1;
      u32x4 zr[2][2], zi[2][2];
      if (ok) {
#pragma unroll
        for (int T = 0; T < 2; ++T)
#pragma unroll
          for (int t = 0; t < 2; ++t) {
            zr[T][t] = *(const u32x4*)(ws + OFF_X1 + (size_t)kre * 4096 + (T * 2 + t) * 1024 + lane * 16);
            zi[T][t] = cplx ? *(const u32x4*)(ws + OFF_X1 + (size_t)kim * 4096 + (T * 2 + t) * 1024 + lane * 16) : u32x4{0, 0, 0, 0};
          }
      }
      if (first) { WAIT_VM(0); __builtin_amdgcn_s_barrier(); first = false; }
      if (ok) colfft_wave(smem + lane * 16, ws, c, lane, wsc, zr, zi);
    }
  } else {
    constexpr int NP = 32 - 24 - NCF;
    const int worker = (idx - 24 - NCF) * 12 + wave;
    warm_rows(w2, 18874368u, S_ * 2, (g >> 1) * 1152 + PF2_ROWS, (g >> 1) * 1152 + 1152, (g & 1) * 2048u, 16, 0, worker, NP * 12, lane,
              (unsigned)(size_t)(LAS char*)smem + wave * 256);
  }
  STAMP_END();
}

constexpr int K4_NS = 5;
using CG4 = GC<1, 1, 3, 2, LIN, TR64, 5, K4_NS, 3, 3>;

__global__ __launch_bounds__(704, 3) void k_fin(const u16* __restrict__ x, const u16* __restrict__ b2, char* __restrict__ ws, u16* __restrict__ out) {
  extern __shared__ __attribute__((aligned(16))) char smem[];
  const int lane = threadIdx.x & 63, wave = __builtin_amdgcn_readfirstlane(threadIdx.x >> 6);
  const int g = blockIdx.x & 7, idx = blockIdx.x >> 3;
  const int st = g * 4 + idx / 6, dt = idx % 6;
  STAMP_BEGIN(3)
  if (wave >= 6) {
    constexpr unsigned ld = S_ * 2;
    CG4::load(smem, wave - 6, 9, ws + OFF_G, 663552u, lane * 16, ws + OFF_Y1, 2359296u, CG4::voff<TR64>(lane, ld),
              [&](int kt, int a) -> unsigned {
                return a < 12 ? (unsigned)((dt * 3 + (a >> 2)) * 36 + kt * 4 + (a & 3)) * 1024u
                              : (unsigned)(kt * 64 + 8 * (a - 12)) * ld + st * 128u;
              });
    return;
  }
  const int wa = wave >> 1, wb = wave & 1, h = lane >> 5;
  const int sf = st * 2 + wb, df = dt * 3 + wa;
  const int s = sf * 32 + (lane & 31), d0 = df * 32 + 16 * h;
  f32x4 sl[4][4];
#pragma unroll
  for (int q = 0; q < 4; ++q)
#pragma unroll
    for (int r = 0; r < 4; ++r) sl[q][r] = *(const f32x4*)(ws + OFF_SLAB + (((size_t)q * 64 + sf) * 18 + df) * 4096 + r * 1024 + lane * 16);
  const u32x4 x0 = *(const u32x4*)(x + (size_t)s * D_ + d0), x1 = *(const u32x4*)(x + (size_t)s * D_ + d0 + 8);
  const float bias = lo16(b2[s]);
  const unsigned E = wmax_exp(ws, lane);
  const float fs = __uint_as_float((E + 1u) << 23) * (1.0f / 72.0f);
  f32x16 acc[1][1] = {};
  int rslot = 0;
  CG4::consume<true>(smem, wave, lane, 9, rslot, acc);
  u32x4 o[2];
#pragma unroll
  for (int r = 0; r < 4; ++r) {
    const f32x4 sum = (sl[0][r] + sl[1][r]) + (sl[2][r] + sl[3][r]);
    const unsigned xa = r < 2 ? x0[2 * r] : x1[2 * r - 4], xb = r < 2 ? x0[2 * r + 1] : x1[2 * r - 3];
    const float v0 = acc[0][0][4 * r] * fs + sum[0] + bias + lo16(xa), v1 = acc[0][0][4 * r + 1] * fs + sum[1] + bias + hi16(xa);
    const float v2 = acc[0][0][4 * r + 2] * fs + sum[2] + bias + lo16(xb), v3 = acc[0][0][4 * r + 3] * fs + sum[3] + bias + hi16(xb);
    o[r >> 1][(r & 1) * 2] = pack_bf16(v0, v1); o[r >> 1][(r & 1) * 2 + 1] = pack_bf16(v2, v3);
  }
  *(u32x4*)(out + (size_t)s * D_ + d0) = o[0];
  *(u32x4*)(out + (size_t)s * D_ + d0 + 8) = o[1];
  STAMP_END();
}

#define PF1_BLOCKS 128
extern "C" void kernel_launch(void* const* d_in, const int* in_sizes, int n_in, void* d_out, int out_size, void* d_ws, size_t ws_size,
                              hipStream_t stream) {
  const u16* x = (const u16*)d_in[0];
  const u16* wr = (const u16*)d_in[1];
  const u16* wi = (const u16*)d_in[2];
  const char* w1 = (const char*)d_in[4];
  const u16* b1 = (const u16*)d_in[5];
  const char* w2 = (const char*)d_in[6];
  const u16* b2 = (const u16*)d_in[7];
  char* ws = (char*)d_ws;
  (void)hipFuncSetAttribute((const void*)k_g1, hipFuncAttributeMaxDynamicSharedMemorySize, CG1::LDS_BYTES);
  (void)hipFuncSetAttribute((const void*)k_g2, hipFuncAttributeMaxDynamicSharedMemorySize, CG2::LDS_BYTES);
  (void)hipFuncSetAttribute((const void*)k_fin, hipFuncAttributeMaxDynamicSharedMemorySize, CG4::LDS_BYTES);
  k_prep<<<856 + PF1_BLOCKS, 256, 0, stream>>>(x, wr, wi, w1, ws);
  k_g1<<<256, 704, CG1::LDS_BYTES, stream>>>(w1, b1, w2, ws);
  k_g2<<<256, 768, CG2::LDS_BYTES, stream>>>(w2, ws);
  k_fin<<<192, 704, CG4::LDS_BYTES, stream>>>(x, b2, ws, (u16*)d_out);
}
```

```cpp
#define PF1_ROWS 768
#define PF2_ROWS 384
#define PF2_END 384
#define NTF2 6
#include <hip/hip_runtime.h>
#include <hip/hip_bf16.h>
#include <stdint.h>

#define DI __device__ __forceinline__
#define LAS __attribute__((address_space(3)))
typedef unsigned short u16;
typedef __attribute__((ext_vector_type(2))) float f32x2;
typedef __attribute__((ext_vector_type(4))) float f32x4;
typedef __attribute__((ext_vector_type(16))) float f32x16;
typedef __attribute__((ext_vector_type(2))) _Float16 f16x2;
typedef __attribute__((ext_vector_type(8))) _Float16 f16x8;
typedef __attribute__((ext_vector_type(2))) __bf16 bf16x2;
typedef __attribute__((ext_vector_type(8))) __bf16 bf16x8;
typedef __attribute__((ext_vector_type(4))) short s16x4;
typedef __attribute__((ext_vector_type(2))) unsigned u32x2;
typedef __attribute__((ext_vector_type(4))) unsigned u32x4;

#define PF1_BLOCKS 128
#define NRD 3
#define G2_MAP 0
#define PF3_SPLIT 70
#define NT1 0
#define NT2 0
#define NT3 0
#define NTF1 99
#define PF1_END 2048
#define NCF 4

constexpr int S_ = 2048, D_ = 576, F_ = 4608, NC_ = 289;

constexpr size_t OFF_H    = 0;
constexpr size_t OFF_WT   = OFF_H + 2359296;
constexpr size_t OFF_TAB  = OFF_WT + 2367488;
constexpr size_t OFF_WMAX = OFF_TAB + 65536;
constexpr size_t OFF_X1   = OFF_WMAX + 4096;
constexpr size_t OFF_Y1   = OFF_X1 + 2359296;
constexpr size_t OFF_U    = OFF_Y1 + 2359296;
constexpr size_t OFF_SLAB = OFF_U + 5308416;
constexpr size_t WS_END   = OFF_SLAB + 9437184;
constexpr int TAB_E1 = 0, TAB_E2 = 6144, TAB_TW1 = 30720, TAB_TW2 = 47104, TAB_BYTES = 63488;

DI unsigned pack_bf16(float a, float b) { f32x2 v = {a, b}; return __builtin_bit_cast(unsigned, __builtin_convertvector(v, bf16x2)); }
DI unsigned pack_f16(float a, float b) { f32x2 v = {a, b}; return __builtin_bit_cast(unsigned, __builtin_convertvector(v, f16x2)); }
DI float lo16(unsigned p) { return __uint_as_float(p << 16); }
DI float hi16(unsigned p) { return __uint_as_float(p & 0xffff0000u); }
DI float rbf(float a) { return lo16(pack_bf16(a, a)); }

template <int CTRL> DI float dpp_add(float v) {
  return v + __int_as_float(__builtin_amdgcn_update_dpp(0, __float_as_int(v), CTRL, 0xf, 0xf, true));
}
DI float sum32(float v) {
  v = dpp_add<0xB1>(v); v = dpp_add<0x4E>(v); v = dpp_add<0x141>(v); v = dpp_add<0x140>(v);
  auto r = __builtin_amdgcn_permlane16_swap(__float_as_uint(v), __float_as_uint(v), false, false);
  return __uint_as_float(r[0]) + __uint_as_float(r[1]);
}

#define STAMP_BEGIN(k)
#define STAMP_END()
#define WAIT_VM(n) asm volatile("s_waitcnt vmcnt(%0)" ::"n"(n) : "memory")
#define WAIT_LGKM0() asm volatile("s_waitcnt lgkmcnt(0)" ::: "memory")
#define SCHED0() __builtin_amdgcn_sched_barrier(0)

DI u32x4 mk_rsrc(const void* p, unsigned bytes) {
  const unsigned long long a = (unsigned long long)p;
  return u32x4{(unsigned)a, (unsigned)(a >> 32) & 0xffffu, bytes, 0x00020000u};
}
DI void dma16(u32x4 rsrc, unsigned lds_off, unsigned voff, unsigned soff) {
  asm volatile("s_mov_b32 m0, %0\n\ts_nop 0\n\tbuffer_load_dwordx4 %1, %2, %3 offen lds" ::"s"(lds_off), "v"(voff), "s"(rsrc), "s"(soff) : "memory");
}
DI void dma16nt(u32x4 rsrc, unsigned lds_off, unsigned voff, unsigned soff) {
  asm volatile("s_mov_b32 m0, %0\n\ts_nop 0\n\tbuffer_load_dwordx4 %1, %2, %3 offen nt lds" ::"s"(lds_off), "v"(voff), "s"(rsrc), "s"(soff) : "memory");
}
template <bool NT> DI void dma4(u32x4 rsrc, unsigned lds_off, unsigned voff, unsigned soff) {
  if constexpr (NT) asm volatile("s_mov_b32 m0, %0\n\ts_nop 0\n\tbuffer_load_dword %1, %2, %3 offen nt lds" ::"s"(lds_off), "v"(voff), "s"(rsrc), "s"(soff) : "memory");
  else asm volatile("s_mov_b32 m0, %0\n\ts_nop 0\n\tbuffer_load_dword %1, %2, %3 offen lds" ::"s"(lds_off), "v"(voff), "s"(rsrc), "s"(soff) : "memory");
}
template <bool NT = false>
DI void warm_rows(const char* base, unsigned bytes, unsigned ld, int r0, int r1, unsigned c0, int nl, int mode, int worker, int nworkers,
                  int lane, unsigned lds_dummy) {
  const u32x4 r = mk_rsrc(base, bytes);
  const int total = (r1 - r0) * nl;
  for (int i0 = worker * 64; i0 < total; i0 += nworkers * 64) {
    const int i = i0 + lane;
    const int rr = i / nl, l = i - rr * nl, row = r0 + rr;
    const unsigned prow = mode == 1 ? (unsigned)(64 * (row & 31) + (row >> 5)) : (mode == 2 ? (unsigned)((row & 3) * 1152 + (row >> 2)) : (unsigned)row);
    const unsigned voff = i < total ? prow * ld + c0 + (unsigned)l * 128u : 0xfffffff0u;
    dma4<NT>(r, lds_dummy, voff, 0u);
    WAIT_VM(7);
  }
  WAIT_VM(0);
}

template <bool F16> DI f32x16 mfma32(u32x4 a, u32x4 b, f32x16 c) {
  if constexpr (F16) return __builtin_amdgcn_mfma_f32_32x32x16_f16(__builtin_bit_cast(f16x8, a), __builtin_bit_cast(f16x8, b), c, 0, 0, 0);
  else return __builtin_amdgcn_mfma_f32_32x32x16_bf16(__builtin_bit_cast(bf16x8, a), __builtin_bit_cast(bf16x8, b), c, 0, 0, 0);
}
DI u32x4 pack8_f16(const f32x16& v, int u) {
  return u32x4{pack_f16(v[8 * u], v[8 * u + 1]), pack_f16(v[8 * u + 2], v[8 * u + 3]), pack_f16(v[8 * u + 4], v[8 * u + 5]), pack_f16(v[8 * u + 6], v[8 * u + 7])};
}
DI u32x4 tr_frag(const char* p0, const char* p1) {
  s16x4 lo = __builtin_amdgcn_ds_read_tr16_b64_v4i16((LAS s16x4*)p0);
  s16x4 hi = __builtin_amdgcn_ds_read_tr16_b64_v4i16((LAS s16x4*)p1);
  u32x2 a = __builtin_bit_cast(u32x2, lo), c = __builtin_bit_cast(u32x2, hi);
  return u32x4{a[0], a[1], c[0], c[1]};
}
DI float opq(float v) { asm("" : "+v"(v)); return v; }
DI void tw_piece(f32x16& re, f32x16& im, int p, const f32x4 w) {
  const float r0 = re[2 * p], r1 = re[2 * p + 1], i0 = im[2 * p], i1 = im[2 * p + 1];
  re[2 * p] = opq(__builtin_fmaf(r0, w[0], -(i0 * w[2]))); im[2 * p] = opq(__builtin_fmaf(r0, w[2], i0 * w[0]));
  re[2 * p + 1] = opq(__builtin_fmaf(r1, w[1], -(i1 * w[3]))); im[2 * p + 1] = opq(__builtin_fmaf(r1, w[3], i1 * w[1]));
}
DI void tw_load(f32x4 (&t)[8], const char* tw) {
#pragma unroll
  for (int p = 0; p < 8; ++p) t[p] = *(const f32x4*)(tw + p * 1024);
}
DI void tw_apply(f32x16& re, f32x16& im, const f32x4 (&t)[8]) {
#pragma unroll
  for (int p = 0; p < 8; ++p) tw_piece(re, im, p, t[p]);
}
DI float cs(int which, float rev) {
  return which == 0 ? __builtin_amdgcn_cosf(rev) : (which == 1 ? -__builtin_amdgcn_sinf(rev) : __builtin_amdgcn_sinf(rev));
}
DI u32x4 e32_frag(int frag, int lane) {
  const int t = frag / 3, which = frag - 3 * t, r = lane & 31, h = lane >> 5;
  float o[8];
#pragma unroll
  for (int j = 0; j < 8; ++j) o[j] = cs(which, (float)(((16 * t + 8 * (j >> 2) + 4 * h + (j & 3)) * r) & 31) * (1.0f / 32.0f));
  return u32x4{pack_f16(o[0], o[1]), pack_f16(o[2], o[3]), pack_f16(o[4], o[5]), pack_f16(o[6], o[7])};
}

constexpr int K1_RS = 1216, K1_XS = 0, K1_HS = 8 * K1_RS, K1_C1 = 16 * K1_RS, K1_E = K1_C1 + 6144, K1_TW = K1_E + 6144, K1_O = K1_TW + 8192,
              K1_LDS = K1_O + 9216;

__global__ __launch_bounds__(256, 2) void k_prep(const u16* __restrict__ x, const u16* __restrict__ wr, const u16* __restrict__ wi,
                                              const char* __restrict__ w1, char* __restrict__ ws) {
  __shared__ __attribute__((aligned(16))) char lds[K1_LDS];
  const int b = blockIdx.x, tid = threadIdx.x;
  STAMP_BEGIN(0)
  if (b < 256) {
    constexpr int RS = K1_RS;
    const int s2 = b >> 2, t = (b >> 1) & 1, h = b & 1;
    const int rr = tid >> 5, l32 = tid & 31, wave = tid >> 6, lane = tid & 63;
    const int s1 = 16 * t + 8 * (rr >> 2) + 4 * h + (rr & 3);
    const unsigned* xr = (const unsigned*)(x + (size_t)(64 * s1 + s2) * D_);
    unsigned raw[9];
#pragma unroll
    for (int i = 0; i < 9; ++i) raw[i] = xr[32 * i + l32];
#pragma unroll
    for (int z = 0; z < 5; ++z) {
      const int fr = wave + 4 * z;
      u32x4 val;
      const int col = lane & 31, hh = lane >> 5, r = col >> 4, k1 = col & 15;
      if (fr < 6) {
        const int ks = fr >> 1, which = fr & 1;
        float o[8];
#pragma unroll
        for (int j = 0; j < 8; ++j) {
          int row, d1;
          if (ks < 2) { row = ks; d1 = 8 * (j >> 2) + 4 * hh + (j & 3); }
          else { row = (hh == 0 && j < 4) ? (j >> 1) : 2; d1 = 16 + (j & 1); }
          const float v = cs(which, (float)((d1 * k1) % 18) * (1.0f / 18.0f));
          o[j] = (row == r && k1 < 10) ? v : 0.f;
        }
        val = u32x4{pack_f16(o[0], o[1]), pack_f16(o[2], o[3]), pack_f16(o[4], o[5]), pack_f16(o[6], o[7])};
      } else if (fr < 12) {
        val = e32_frag(fr - 6, lane);
      } else {
        const int p = fr - 12;
#pragma unroll
        for (int y = 0; y < 2; ++y) {
          const int rho = 2 * p + y, d2 = (rho & 3) + 8 * (rho >> 2) + 4 * hh;
          const float a = (float)(d2 * k1) * (1.0f / 576.0f);
          val[y] = __float_as_uint(__builtin_amdgcn_cosf(a)); val[2 + y] = __float_as_uint(-__builtin_amdgcn_sinf(a));
        }
      }
      *(u32x4*)(lds + K1_C1 + fr * 1024 + lane * 16) = val;
    }
    float v[18];
#pragma unroll
    for (int i = 0; i < 9; ++i) { v[2 * i] = lo16(raw[i]); v[2 * i + 1] = hi16(raw[i]); }
#pragma unroll
    for (int pass = 0; pass < 2; ++pass) {
      float sm = 0.f;
#pragma unroll
      for (int i = 0; i < 18; ++i) sm += v[i];
      const float mean = sum32(sm) * (1.0f / 576.0f);
      float sq = 0.f;
#pragma unroll
      for (int i = 0; i < 18; ++i) { float dlt = v[i] - mean; sq += dlt * dlt; }
      const float var = sum32(sq) * (1.0f / 576.0f);
      const float mb = rbf(mean);
      const float sd = rbf(__builtin_sqrtf(rbf(rbf(var) + 1e-5f)));
      const float rs = 1.0f / sd;
      char* dst = lds + pass * (8 * RS) + rr * RS + l32 * 4;
#pragma unroll
      for (int i = 0; i < 9; ++i) {
        unsigned p = pack_bf16(v[2 * i] - mb, v[2 * i + 1] - mb);
        p = pack_bf16(lo16(p) * rs, hi16(p) * rs);
        v[2 * i] = lo16(p); v[2 * i + 1] = hi16(p);
        *(unsigned*)(dst + i * 128) = pass == 0 ? pack_f16(v[2 * i], v[2 * i + 1]) : p;
      }
    }
    __syncthreads();
    for (int cb = wave; cb < 9; cb += 4) {
      const int gg = lane >> 4, q = (lane & 15) >> 2, pp = lane & 3;
      const char* src = lds + K1_HS + q * RS + (cb * 64 + gg * 16 + pp * 4) * 2;
      const u32x4 val = tr_frag(src, src + 4 * RS);
      const int df = cb * 2 + (gg >> 1), i = 8 * q + 4 * (gg & 1) + pp;
      *(u32x4*)(ws + OFF_H + ((size_t)df * 128 + 2 * s2 + t) * 1024 + (h * 32 + i) * 16) = val;
    }
    {
      const int hh = lane >> 5, blk = (lane >> 4) & 1, q = (lane & 15) >> 2, p = lane & 3;
      const char* xa = lds + K1_XS + (2 * wave) * RS + blk * 32 + p * 8;
      const char* cf = lds + K1_C1 + lane * 16;
      f32x16 re = {}, im = {};
      f32x4 tw[8];
      tw_load(tw, lds + K1_TW + lane * 16);
#pragma unroll
      for (int ks = 0; ks < 3; ++ks) {
        const char* a0 = ks < 2 ? xa + ks * RS + (4 * hh + q) * 64 : xa + (q >> 1) * RS + (16 + (q & 1)) * 64;
        const u32x4 a = tr_frag(a0, ks < 2 ? a0 + 512 : a0);
        re = mfma32<true>(a, *(const u32x4*)(cf + (2 * ks) * 1024), re);
        im = mfma32<true>(a, *(const u32x4*)(cf + (2 * ks + 1) * 1024), im);
      }
      tw_apply(re, im, tw);
      f32x16 xr_ = {}, xi_ = {};
#pragma unroll
      for (int u = 0; u < 2; ++u) {
        const u32x4 ar = pack8_f16(re, u), ai = pack8_f16(im, u);
        const char* ef = lds + K1_E + u * 3072 + lane * 16;
        const u32x4 er = *(const u32x4*)ef, ei = *(const u32x4*)(ef + 1024), en = *(const u32x4*)(ef + 2048);
        xr_ = mfma32<true>(er, ar, xr_); xi_ = mfma32<true>(ei, ar, xi_);
        xr_ = mfma32<true>(en, ai, xr_); xi_ = mfma32<true>(er, ai, xi_);
      }
      const int col = lane & 31, k1 = col & 15, row2 = 2 * (2 * wave + (col >> 4));
      if (k1 < 10) {
        char* o1 = lds + K1_O + 32 * k1 + 576 * 4 * hh + row2;
        char* o2 = lds + K1_O + 32 * (576 - k1) - 576 * (4 * hh + 27) + row2;
#pragma unroll
        for (int rho = 0; rho < 8; ++rho) {
          const unsigned pk = pack_f16(xr_[rho] * (1.0f / 32.0f), xi_[rho] * (1.0f / 32.0f));
          char* d = o1 + 576 * ((rho & 3) + 8 * (rho >> 2));
          *(u16*)d = (u16)pk;
          if (rho != 0 || k1 != 0 || hh != 0) *(u16*)(d + 16) = (u16)(pk >> 16);
        }
        if (k1 >= 1 && k1 <= 8) {
#pragma unroll
          for (int rho = 8; rho < 16; ++rho) {
            const unsigned pk = pack_f16(xr_[rho] * (1.0f / 32.0f), xi_[rho] * (-1.0f / 32.0f));
            char* d = o2 + 576 * (27 - ((rho & 3) + 8 * (rho >> 2)));
            *(u16*)d = (u16)pk;
            *(u16*)(d + 16) = (u16)(pk >> 16);
          }
        } else if (k1 == 0 && hh == 0) {
          *(u16*)(lds + K1_O + 16 + row2) = (u16)pack_f16(xr_[8] * (1.0f / 32.0f), 0.f);
        }
      }
    }
    __syncthreads();
    const int P = ((s2 >> 5) * 2 + t) * 64 + h * 32 + 8 * ((s2 >> 2) & 3) + 4 * ((s2 >> 4) & 1) + (s2 & 3);
#pragma unroll
    for (int it = 0; it < 3; ++it) {
      const int p = tid + it * 256;
      if (p < 576) *(u32x4*)(ws + OFF_X1 + (size_t)P * 9216 + p * 16) = *(const u32x4*)(lds + K1_O + p * 16);
    }
  } else if (b < 512) {
    const int ab_ = b - 256;
    const int rg = ab_ >> 2, cg = ab_ & 3;
    const int c0 = cg * 72 + (cg > 0), nc = cg == 0 ? 73 : 72;
    const int kq = rg >> 2, ko = rg & 3;
    unsigned rv[10], iv[10];
#pragma unroll
    for (int it = 0; it < 10; ++it) {
      const int idx = tid + it * 256, row = idx / 73, col = idx - row * 73;
      const bool ok = idx < 32 * 73 && col < nc;
      const int ks = 8 * ko + (row & 7) + 32 * (4 * kq + (row >> 3));
      const int off = ok ? ks * NC_ + c0 + col : 0;
      rv[it] = wr[off]; iv[it] = wi[off];
    }
    unsigned mx = 0;
#pragma unroll
    for (int it = 0; it < 10; ++it) {
      const int idx = tid + it * 256, row = idx / 73, col = idx - row * 73;
      if (idx < 32 * 73 && col < nc) {
        mx = max(mx, max(rv[it] & 0x7fffu, iv[it] & 0x7fffu));
        *(unsigned*)(lds + col * 144 + (row & 7) * 16 + (row >> 3) * 4) = rv[it] | (iv[it] << 16);
      }
    }
#pragma unroll
    for (int o = 32; o; o >>= 1) mx = max(mx, (unsigned)__shfl_xor((int)mx, o));
    unsigned* red = (unsigned*)(lds + 16384);
    if ((tid & 63) == 0) red[tid >> 6] = mx;
    __syncthreads();
    if (tid == 0) ((unsigned*)(ws + OFF_WMAX))[ab_] = max(max(red[0], red[1]), max(red[2], red[3]));
    const int U = kq >> 3, h = (kq >> 2) & 1, rq = kq & 3;
#pragma unroll
    for (int it = 0; it < 3; ++it) {
      const int p = tid + it * 256;
      if (p < nc * 8) {
        const int col = p >> 3, a = p & 7;
        u32x4 val = *(const u32x4*)(lds + col * 144 + a * 16);
        *(u32x4*)(ws + OFF_WT + (size_t)(c0 + col) * 8192 + ((U * 4 + rq) * 64 + h * 32 + 8 * ko + a) * 16) = val;
      }
    }
  } else if (b < 528) {
    const int PP = (b - 512) * 256 + tid;
    if (PP < TAB_BYTES / 16) {
      u32x4 val;
      if (PP < 384) val = e32_frag(PP >> 6, PP & 63);
      else if (PP < 384 + 1536) {
        const int lane = PP & 63, r = lane & 31, h = lane >> 5, frag = (PP >> 6) - 6;
        const int idx = frag / 3, which = frag - 3 * idx, U = idx >> 2, T = (idx >> 1) & 1, u = idx & 1;
        const int a_ = 32 * U + 16 * ((r >> 2) & 1) + 4 * (r >> 3) + (r & 3);
        float o[8];
#pragma unroll
        for (int j = 0; j < 8; ++j) o[j] = cs(which, (float)((a_ * (32 * T + 16 * h + 8 * u + j)) & 63) * (1.0f / 64.0f));
        val = u32x4{pack_f16(o[0], o[1]), pack_f16(o[2], o[3]), pack_f16(o[4], o[5]), pack_f16(o[6], o[7])};
      } else {
        const int Q0 = PP - (384 + 1536);
        const bool inv = Q0 >= 1024;
        const int Q = inv ? Q0 - 1024 : Q0;
        const int lane = Q & 63, p = (Q >> 6) & 7, T = Q >> 9, r = lane & 31, h = lane >> 5;
#pragma unroll
        for (int z = 0; z < 2; ++z) {
          const int rho = 2 * p + z;
          int n;
          if (!inv) n = (32 * T + 16 * h + rho) * r;
          else n = (32 * T + 16 * ((r >> 2) & 1) + 4 * (r >> 3) + (r & 3)) * ((rho & 3) + 8 * (rho >> 2) + 4 * h);
          const float a = (float)(n & 2047) * (1.0f / 2048.0f);
          val[z] = __float_as_uint(__builtin_amdgcn_cosf(a) * (inv ? 0.125f : 1.0f));
          val[2 + z] = __float_as_uint(inv ? __builtin_amdgcn_sinf(a) * 0.125f : -__builtin_amdgcn_sinf(a));
        }
      }
      *(u32x4*)(ws + OFF_TAB + (size_t)PP * 16) = val;
    }
  } else {
    const int pb = b - 528;
    const int g = b & 7, wave = __builtin_amdgcn_readfirstlane(tid >> 6);
    const int ft0 = (g * 27) / 6, ft1 = (g * 27 + 26) / 6;
    warm_rows<NT1>(w1, 18874368u, F_ * 2, 0, PF1_ROWS, ft0 * 256u, (ft1 - ft0 + 1) * 2, 1, (pb >> 3) * 4 + wave, (PF1_BLOCKS >> 3) * 4, tid & 63,
              (unsigned)(size_t)(LAS char*)lds + wave * 256);
  }
  STAMP_END();
}

enum { LIN = 0, TR128 = 1, TR64 = 2 };

template <int V> struct IC { static constexpr int value = V; };
template <int NA_W, int NB_W, int WA, int WB, int AK, int BK, int NL, int NS, int DPM, int DEPTH>
struct GC {
  static constexpr int NC = WA * WB, NFA = NA_W * WA, NFB = NB_W * WB;
  static constexpr int A_BYTES = NFA * 4096, B_BYTES = NFB * 4096, STAGE = A_BYTES + B_BYTES;
  static constexpr int IA = NFA * 4, IB = NFB * 4, NI = IA + IB, G = NI / NL;
  static constexpr int LDS_BYTES = NS * STAGE;
  static_assert(NI % NL == 0, "");
  static_assert(AK == LIN || NFA == (AK == TR128 ? 4 : 2), "");
  static_assert(BK == LIN || NFB == (BK == TR128 ? 4 : 2), "");

  template <int K> static DI unsigned voff(int lane, unsigned rstride) {
    if constexpr (K == LIN) return lane * 16;
    else if constexpr (K == TR128) return (unsigned)(lane >> 4) * rstride + (unsigned)(((lane & 15) ^ ((lane >> 4) << 2)) * 16);
    else return (unsigned)(lane >> 3) * rstride + (unsigned)(((lane & 7) ^ (((lane >> 4) & 1) << 2)) * 16);
  }
  template <int K, int NF> static DI void frag_addr(unsigned (&addr)[K == LIN ? 1 : NF], int opoff, int wpos, int lane) {
    if constexpr (K == LIN) addr[0] = opoff + wpos * NF * 4096 + lane * 16;
    else {
      const int h = lane >> 5, blk = (lane >> 4) & 1, q = (lane & 15) >> 2, p = lane & 3;
#pragma unroll
      for (int i = 0; i < NF; ++i) {
        const int ch = 4 * (wpos * NF + i) + 2 * blk + (p >> 1);
        if constexpr (K == TR128) addr[i] = opoff + (4 * h + q) * 256 + ((ch ^ (q << 2)) * 16) + 8 * (p & 1);
        else addr[i] = opoff + (4 * h + q) * 128 + ((ch ^ (((q >> 1) & 1) << 2)) * 16) + 8 * (p & 1);
      }
    }
  }
  template <int K, int NF> static DI void rd(u32x4 (&f)[NF], const char* smem, const unsigned (&addr)[K == LIN ? 1 : NF], int sb, int hh) {
#pragma unroll
    for (int i = 0; i < NF; ++i) {
      if constexpr (K == LIN) f[i] = *(const u32x4*)(smem + addr[0] + sb + (i * 4 + hh) * 1024);
      else {
        constexpr int RB = K == TR128 ? 256 : 128;
        const char* p = smem + addr[i] + sb + hh * 16 * RB;
        s16x4 lo = __builtin_amdgcn_ds_read_tr16_b64_v4i16((LAS s16x4*)p);
        s16x4 hi = __builtin_amdgcn_ds_read_tr16_b64_v4i16((LAS s16x4*)(p + 8 * RB));
        u32x2 a = __builtin_bit_cast(u32x2, lo), c = __builtin_bit_cast(u32x2, hi);
        f[i] = u32x4{a[0], a[1], c[0], c[1]};
      }
    }
  }
  static DI void ilv() {
    constexpr int NR = NA_W * (AK == LIN ? 1 : 2) + NB_W * (BK == LIN ? 1 : 2), NM = NA_W * NB_W;
    constexpr int NG = (NR + DPM - 1) / DPM < NM ? (NR + DPM - 1) / DPM : NM;
#pragma unroll
    for (int i = 0; i < NG; ++i) {
      __builtin_amdgcn_sched_group_barrier(0x8, 1, 0);
      if (i + 1 < NG) __builtin_amdgcn_sched_group_barrier(0x100, DPM, 0);
      else __builtin_amdgcn_sched_group_barrier(0x100, NR - DPM * (NG - 1), 0);
    }
    if constexpr (NM > NG) __builtin_amdgcn_sched_group_barrier(0x8, NM - NG, 0);
    SCHED0();
  }
  template <class SOFF>
  static DI void load(char* smem, int lw, int nt, const char* baseA, unsigned bytesA, unsigned vA, const char* baseB, unsigned bytesB,
                      unsigned vB, SOFF soff, int ntfrom) {
    const unsigned ldsbase = (unsigned)(size_t)(LAS char*)smem + lw * G * 1024;
    unsigned vo[G]; u32x4 rs[G];
#pragma unroll
    for (int m = 0; m < G; ++m) { const bool isA = (lw * G + m) < IA; vo[m] = isA ? vA : vB; rs[m] = mk_rsrc(isA ? baseA : baseB, isA ? bytesA : bytesB); }
    auto issue = [&](int kt, int so) {
#pragma unroll
      for (int m = 0; m < G; ++m) {
        const bool isW = ((lw * G + m) < IA) == (AK != LIN);
        if (isW && kt >= ntfrom) dma16nt(rs[m], ldsbase + so + m * 1024, vo[m], soff(kt, lw * G + m));
        else dma16(rs[m], ldsbase + so + m * 1024, vo[m], soff(kt, lw * G + m));
      }
    };
#pragma unroll
    for (int p = 0; p < NS - 2; ++p) issue(p, p * STAGE);
    int wslot = NS - 2;
    for (int tau = 0; tau < nt - (NS - 2); ++tau) {
      WAIT_VM((NS - 3) * G);
      __builtin_amdgcn_s_barrier();
      issue(tau + NS - 2, wslot * STAGE);
      wslot = (wslot + 1 == NS) ? 0 : wslot + 1;
    }
    if constexpr (NS >= 5) { WAIT_VM(2 * G); __builtin_amdgcn_s_barrier(); }
    if constexpr (NS >= 4) { WAIT_VM(G); __builtin_amdgcn_s_barrier(); }
    WAIT_VM(0); __builtin_amdgcn_s_barrier();
    static_assert(NS >= 3 && NS <= 5, "");
  }
  template <bool F16>
  static DI void consume(const char* smem, int wave, int lane, int n, int& rslot, f32x16 (&acc)[NA_W][NB_W]) {
    const int wa = wave / WB, wb = wave - wa * WB;
    unsigned addrA[AK == LIN ? 1 : NA_W], addrB[BK == LIN ? 1 : NB_W];
    frag_addr<AK, NA_W>(addrA, 0, wa, lane);
    frag_addr<BK, NB_W>(addrB, A_BYTES, wb, lane);
    u32x4 fa[DEPTH][NA_W], fb[DEPTH][NB_W];
#pragma unroll
    for (int d = 1; d < DEPTH; ++d) {
#pragma unroll
      for (int i = 0; i < NA_W; ++i) fa[d][i] = u32x4{0, 0, 0, 0};
#pragma unroll
      for (int i = 0; i < NB_W; ++i) fb[d][i] = u32x4{0, 0, 0, 0};
    }
    auto mm = [&](const u32x4 (&a)[NA_W], const u32x4 (&b)[NB_W]) {
#pragma unroll
      for (int i = 0; i < NA_W; ++i)
#pragma unroll
        for (int j = 0; j < NB_W; ++j) acc[i][j] = mfma32<F16>(a[i], b[j], acc[i][j]);
    };
    auto step = [&](auto Z0) {
      constexpr int z0 = decltype(Z0)::value;
      __builtin_amdgcn_s_barrier();
      SCHED0();
      const int sb = rslot * STAGE;
#pragma unroll
      for (int hh = 0; hh < 4; ++hh) {
        rd<AK, NA_W>(fa[(z0 + hh) % DEPTH], smem, addrA, sb, hh);
        rd<BK, NB_W>(fb[(z0 + hh) % DEPTH], smem, addrB, sb, hh);
        mm(fa[(z0 + hh + 1) % DEPTH], fb[(z0 + hh + 1) % DEPTH]);
        ilv();
      }
      rslot = (rslot + 1 == NS) ? 0 : rslot + 1;
    };
    if constexpr (DEPTH == 2) {
      for (int tau = 0; tau < n; ++tau) step(IC<0>{});
      mm(fa[1], fb[1]);
    } else {
      int tau = 0;
      for (; tau + 3 <= n; tau += 3) { step(IC<0>{}); step(IC<1>{}); step(IC<2>{}); }
      if (n - tau == 2) { step(IC<0>{}); step(IC<1>{}); mm(fa[0], fb[0]); mm(fa[1], fb[1]); }
      else if (n - tau == 1) { step(IC<0>{}); mm(fa[2], fb[2]); mm(fa[0], fb[0]); }
      else { mm(fa[1], fb[1]); mm(fa[2], fb[2]); }
    }
  }
};

DI float gelu_tanh(float u) {
  const float z = u * (1.0f + 0.044715f * u * u) * (-2.0f * 0.7978845608028654f * 1.4426950408889634f);
  return u * __builtin_amdgcn_rcpf(1.0f + __builtin_amdgcn_exp2f(z));
}

#define K2_NS 5
using CG1 = GC<1, 3, 4, 1, TR128, LIN, 7, K2_NS, 2, 3>;

__global__ __launch_bounds__(704, 3) void k_g1(const char* __restrict__ w1, const u16* __restrict__ b1, const char* __restrict__ w2,
                                               char* __restrict__ ws) {
  extern __shared__ __attribute__((aligned(16))) char smem[];
  const int lane = threadIdx.x & 63, wave = __builtin_amdgcn_readfirstlane(threadIdx.x >> 6);
  const int g = blockIdx.x & 7, idx = blockIdx.x >> 3;
  STAMP_BEGIN(1)
  if (idx < 27) {
    const int tile = g * 27 + idx, ft = tile / 6, dt = tile - ft * 6;
    char* ebuf = smem + (29 % K2_NS) * CG1::STAGE;
    if (wave >= 4) {
      constexpr unsigned ld = F_ * 2;
      CG1::load(smem, wave - 4, 32, w1, 18874368u, CG1::voff<TR128>(lane, 64 * ld), ws + OFF_H, 2359296u, CG1::voff<LIN>(lane, 0),
                [&](int kt, int a) -> unsigned {
                  return a < 16 ? (unsigned)(((4 * a) & 31) * 64 + 2 * kt + (a >> 3)) * ld + ft * 256u
                                : (unsigned)((dt * 3 + ((a - 16) >> 2)) * 128 + kt * 4 + ((a - 16) & 3)) * 1024u;
                }, NTF1);
    } else {
      f32x16 acc[1][3] = {};
      int rslot = 0;
      CG1::consume<false>(smem, wave, lane, 32, rslot, acc);
#pragma unroll
      for (int j = 0; j < 3; ++j)
#pragma unroll
        for (int s = 0; s < 2; ++s) {
          u32x4 val;
#pragma unroll
          for (int e = 0; e < 4; ++e) val[e] = pack_bf16(acc[0][j][8 * s + 2 * e], acc[0][j][8 * s + 2 * e + 1]);
          *(u32x4*)(ebuf + ((wave * 2 + s) * 3 + j) * 1024 + lane * 16) = val;
        }
    }
    const int h = lane >> 5;
    u32x2 bv[3][2];
#pragma unroll
    for (int it = 0; it < 3; ++it) {
      const int i = min(wave + 11 * it, 23), fz = ft * 128 + (i / 3) * 16 + 4 * h;
      bv[it][0] = *(const u32x2*)(b1 + fz); bv[it][1] = *(const u32x2*)(b1 + fz + 8);
    }
    WAIT_LGKM0();
    __builtin_amdgcn_s_barrier();
#pragma unroll
    for (int it = 0; it < 3; ++it) {
      const int i = wave + 11 * it;
      if (i < 24) {
        const int z = i / 3, j = i - 3 * z;
        const u32x4 a = *(const u32x4*)(ebuf + i * 1024 + lane * 16);
        u32x4 val;
#pragma unroll
        for (int e = 0; e < 4; ++e) {
          const unsigned bb = bv[it][e >> 1][e & 1];
          const unsigned pk = pack_bf16(lo16(a[e]) + lo16(bb), hi16(a[e]) + hi16(bb));
          val[e] = pack_bf16(gelu_tanh(lo16(pk)), gelu_tanh(hi16(pk)));
        }
        *(u32x4*)(ws + OFF_U + ((size_t)(dt * 3 + j) * 288 + ft * 8 + z) * 1024 + lane * 16) = val;
      }
    }
  } else {
    const int worker = (idx - 27) * 11 + wave;
    const unsigned dummy = (unsigned)(size_t)(LAS char*)smem + wave * 256;
    const int ft0 = (g * 27) / 6, ft1 = (g * 27 + 26) / 6;
    warm_rows<NT2>(w1, 18874368u, F_ * 2, PF1_ROWS, PF1_END, ft0 * 256u, (ft1 - ft0 + 1) * 2, 1, worker, 55, lane, dummy);
    warm_rows(w2, 18874368u, S_ * 2, (g >> 1) * 1152, (g >> 1) * 1152 + PF2_ROWS, (g & 1) * 2048u, 16, 0, worker, 55, lane, dummy);
  }
  STAMP_END();
}

struct EF { u32x4 r, i, n; };
DI EF ef_load(const char* p) { EF e; e.r = *(const u32x4*)p; e.i = *(const u32x4*)(p + 1024); e.n = *(const u32x4*)(p + 2048); return e; }
#define MF(a, b, c) mfma32<true>(a, b, c)
DI void cm_dA(f32x16& re, f32x16& im, u32x4 dr, u32x4 di, const EF& e) { re = MF(dr, e.r, re); im = MF(dr, e.i, im); re = MF(di, e.n, re); im = MF(di, e.r, im); }
DI void cm_dB(f32x16& re, f32x16& im, u32x4 dr, u32x4 di, const EF& e) { re = MF(e.r, dr, re); im = MF(e.i, dr, im); re = MF(e.n, di, re); im = MF(e.r, di, im); }
DI void cm_dAc(f32x16& re, f32x16& im, u32x4 dr, u32x4 di, const EF& e) { re = MF(dr, e.r, re); im = MF(dr, e.n, im); re = MF(di, e.i, re); im = MF(di, e.r, im); }
DI void wmul(f32x16& re, f32x16& im, const u32x4 (&wv)[4], float wsc) {
#pragma unroll
  for (int p = 0; p < 8; ++p) {
    const unsigned w0 = wv[p >> 1][(2 * p) & 3], w1 = wv[p >> 1][(2 * p + 1) & 3];
    tw_piece(re, im, p, f32x4{opq(lo16(w0) * wsc), opq(lo16(w1) * wsc), opq(hi16(w0) * wsc), opq(hi16(w1) * wsc)});
  }
}
DI void colfft_wave(const char* tab  , const char* ws, int c, int lane, float wsc, const u32x4 (&zr)[2][2], const u32x4 (&zi)[2][2],
                    char* img, int slot_re, int slot_im) {
  f32x4 tw[8];
  f32x16 r0 = {}, i0 = {}, r1 = {}, i1 = {};
  u32x4 ar[2][2], ai[2][2];
  {
    const EF e0 = ef_load(tab + TAB_E1), e1 = ef_load(tab + TAB_E1 + 3072);
    tw_load(tw, tab + TAB_TW1);
    SCHED0();
    cm_dA(r0, i0, zr[0][0], zi[0][0], e0); cm_dA(r0, i0, zr[0][1], zi[0][1], e1);
    cm_dA(r1, i1, zr[1][0], zi[1][0], e0); cm_dA(r1, i1, zr[1][1], zi[1][1], e1);
    SCHED0();
  }
#pragma unroll
  for (int p = 0; p < 8; ++p) { tw_piece(r0, i0, p, tw[p]); tw[p] = *(const f32x4*)(tab + TAB_TW1 + 8192 + p * 1024); }
  ar[0][0] = pack8_f16(r0, 0); ar[0][1] = pack8_f16(r0, 1); ai[0][0] = pack8_f16(i0, 0); ai[0][1] = pack8_f16(i0, 1);
  EF ea = ef_load(tab + TAB_E2), eb = ef_load(tab + TAB_E2 + 3072);
  SCHED0();
  r0 = f32x16{}; i0 = f32x16{};
  cm_dB(r0, i0, ar[0][0], ai[0][0], ea); cm_dB(r0, i0, ar[0][1], ai[0][1], eb);
  tw_apply(r1, i1, tw);
  ar[1][0] = pack8_f16(r1, 0); ar[1][1] = pack8_f16(r1, 1); ai[1][0] = pack8_f16(i1, 0); ai[1][1] = pack8_f16(i1, 1);
  ea = ef_load(tab + TAB_E2 + 2 * 3072); eb = ef_load(tab + TAB_E2 + 3 * 3072);
  SCHED0();
  u32x4 wv0[4], wv1[4];
#pragma unroll
  for (int i = 0; i < 4; ++i) wv0[i] = *(const u32x4*)(ws + OFF_WT + (size_t)c * 8192 + i * 1024 + lane * 16);
#pragma unroll
  for (int i = 0; i < 4; ++i) wv1[i] = *(const u32x4*)(ws + OFF_WT + (size_t)c * 8192 + (4 + i) * 1024 + lane * 16);
  cm_dB(r0, i0, ar[1][0], ai[1][0], ea); ea = ef_load(tab + TAB_E2 + 4 * 3072);
  cm_dB(r0, i0, ar[1][1], ai[1][1], eb); eb = ef_load(tab + TAB_E2 + 5 * 3072);
  SCHED0();
  r1 = f32x16{}; i1 = f32x16{};
  cm_dB(r1, i1, ar[0][0], ai[0][0], ea); ea = ef_load(tab + TAB_E2 + 6 * 3072);
  cm_dB(r1, i1, ar[0][1], ai[0][1], eb); eb = ef_load(tab + TAB_E2 + 7 * 3072);
  SCHED0();
  cm_dB(r1, i1, ar[1][0], ai[1][0], ea); ea = ef_load(tab + TAB_E2);
  cm_dB(r1, i1, ar[1][1], ai[1][1], eb); eb = ef_load(tab + TAB_E2 + 3072);
  wmul(r0, i0, wv0, wsc);
  u32x4 pr[2][2], pi[2][2];
  pr[0][0] = pack8_f16(r0, 0); pr[0][1] = pack8_f16(r0, 1); pi[0][0] = pack8_f16(i0, 0); pi[0][1] = pack8_f16(i0, 1);
  SCHED0();
  r0 = f32x16{}; i0 = f32x16{};
  cm_dAc(r0, i0, pr[0][0], pi[0][0], ea); ea = ef_load(tab + TAB_E2 + 2 * 3072);
  cm_dAc(r0, i0, pr[0][1], pi[0][1], eb); eb = ef_load(tab + TAB_E2 + 3 * 3072);
  wmul(r1, i1, wv1, wsc);
  pr[1][0] = pack8_f16(r1, 0); pr[1][1] = pack8_f16(r1, 1); pi[1][0] = pack8_f16(i1, 0); pi[1][1] = pack8_f16(i1, 1);
  SCHED0();
  tw_load(tw, tab + TAB_TW2);
  cm_dAc(r0, i0, pr[1][0], pi[1][0], ea); ea = ef_load(tab + TAB_E2 + 4 * 3072);
  cm_dAc(r0, i0, pr[1][1], pi[1][1], eb); eb = ef_load(tab + TAB_E2 + 5 * 3072);
  SCHED0();
  r1 = f32x16{}; i1 = f32x16{};
  cm_dAc(r1, i1, pr[0][0], pi[0][0], ea); ea = ef_load(tab + TAB_E2 + 6 * 3072);
  cm_dAc(r1, i1, pr[0][1], pi[0][1], eb); eb = ef_load(tab + TAB_E2 + 7 * 3072);
  SCHED0();
  cm_dAc(r1, i1, pr[1][0], pi[1][0], ea); ea = ef_load(tab + TAB_E1);
  cm_dAc(r1, i1, pr[1][1], pi[1][1], eb); eb = ef_load(tab + TAB_E1 + 3072);
#pragma unroll
  for (int p = 0; p < 8; ++p) { tw_piece(r0, i0, p, tw[p]); tw[p] = *(const f32x4*)(tab + TAB_TW2 + 8192 + p * 1024); }
  ar[0][0] = pack8_f16(r0, 0); ar[0][1] = pack8_f16(r0, 1); ai[0][0] = pack8_f16(i0, 0); ai[0][1] = pack8_f16(i0, 1);
  SCHED0();
  r0 = f32x16{}; i0 = f32x16{};
  cm_dAc(r0, i0, ar[0][0], ai[0][0], ea); cm_dAc(r0, i0, ar[0][1], ai[0][1], eb);
  tw_apply(r1, i1, tw);
  ar[1][0] = pack8_f16(r1, 0); ar[1][1] = pack8_f16(r1, 1); ai[1][0] = pack8_f16(i1, 0); ai[1][1] = pack8_f16(i1, 1);
  SCHED0();
  r1 = f32x16{}; i1 = f32x16{};
  cm_dAc(r1, i1, ar[1][0], ai[1][0], ea); cm_dAc(r1, i1, ar[1][1], ai[1][1], eb);
  auto put = [&](f32x16& yr, f32x16& yi, int V) {
#pragma unroll
    for (int u = 0; u < 2; ++u) {
      const int Q = (lane & 31) * 8 + V * 4 + (lane >> 5) * 2 + u, sw = (Q >> 3) & 15;
      *(u32x4*)(img + Q * 256 + ((slot_re ^ sw) * 16)) = pack8_f16(yr, u);
      if (slot_im >= 0) *(u32x4*)(img + Q * 256 + ((slot_im ^ sw) * 16)) = pack8_f16(yi, u);
    }
  };
  put(r0, i0, 0);
  SCHED0();
  put(r1, i1, 1);
}
#undef MF
DI unsigned wmax_exp(const char* ws, int lane) {
  const u32x4 mv = *(const u32x4*)(ws + OFF_WMAX + lane * 16);
  unsigned mx = max(max(mv[0], mv[1]), max(mv[2], mv[3]));
#pragma unroll
  for (int o = 32; o; o >>= 1) mx = max(mx, (unsigned)__shfl_xor((int)mx, o));
  return min(mx >> 7, 252u);
}

constexpr int K3_NS = 4;
#define G2_DEPTH 2
using CG2 = GC<3, 2, 2, 2, LIN, TR128, 8, K3_NS, 2, G2_DEPTH>;

__global__ __launch_bounds__(768, 3) void k_g2(const char* __restrict__ w2, char* __restrict__ ws) {
  extern __shared__ __attribute__((aligned(16))) char smem[];
  const int lane = threadIdx.x & 63, wave = __builtin_amdgcn_readfirstlane(threadIdx.x >> 6);
  const int g = blockIdx.x & 7, idx = blockIdx.x >> 3;
  STAMP_BEGIN(2)
  if (idx < 24) {
    const int ks = g >> 1, st = (g & 1) * 8 + idx / 3, dt = idx % 3;
    if (wave >= 4) {
      constexpr unsigned ld = S_ * 2;
      CG2::load(smem, wave - 4, 18, ws + OFF_U, 5308416u, lane * 16, w2, 18874368u, CG2::voff<TR128>(lane, ld),
                [&](int kt, int a) -> unsigned {
                  return a < 24 ? (unsigned)((dt * 6 + (a >> 2)) * 288 + (ks * 18 + kt) * 4 + (a & 3)) * 1024u
                                : (unsigned)((ks * 18 + kt) * 64 + 4 * (a - 24)) * ld + st * 256u;
                }, NTF2);
      return;
    }
    f32x16 acc[3][2] = {};
    int rslot = 0;
    CG2::consume<false>(smem, wave, lane, 18, rslot, acc);
    const int wa = wave >> 1, wb = wave & 1;
#pragma unroll
    for (int j = 0; j < 2; ++j)
#pragma unroll
      for (int i = 0; i < 3; ++i) {
        char* dst = ws + OFF_SLAB + (((size_t)ks * 64 + st * 4 + wb * 2 + j) * 18 + dt * 6 + wa * 3 + i) * 2048 + lane * 16;
#pragma unroll
        for (int u = 0; u < 2; ++u) {
          u32x4 val;
#pragma unroll
          for (int e = 0; e < 4; ++e) val[e] = pack_bf16(acc[i][j][8 * u + 2 * e], acc[i][j][8 * u + 2 * e + 1]);
          *(u32x4*)(dst + u * 1024) = val;
        }
      }
  } else {
    const int me = (idx - 24) * 8 + g;
    const unsigned ldsb = (unsigned)(size_t)(LAS char*)smem;
    if (me < 37) {
      const int cbk = me == 36 ? 0 : me;
      char* img = smem + 65536;
      {
        const u32x4 r = mk_rsrc(ws + OFF_X1, 2359296u);
        for (int i = wave; i < 64; i += 12) {
          const int P = 4 * i + (lane >> 4);
          dma16(r, ldsb + 65536 + i * 1024, (unsigned)P * 9216u + (unsigned)(((lane & 15) ^ (P & 15)) * 16), cbk * 256u);
        }
        const u32x4 r2 = mk_rsrc(ws + OFF_TAB, TAB_BYTES);
        for (int i = wave; i < TAB_BYTES / 1024; i += 12) dma16(r2, ldsb + i * 1024, lane * 16, i * 1024u);
      }
      const bool act = me == 36 ? wave == 0 : wave < 8;
      const int c = me == 36 ? 288 : me * 8 + wave;
      const unsigned E = wmax_exp(ws, lane);
      const float wsc = __uint_as_float((253u - E) << 23) * (1.0f / 64.0f);
      WAIT_VM(0);
      __builtin_amdgcn_s_barrier();
      const int slot_re = me == 36 ? 1 : 2 * wave, slot_im = (c == 0 || c == 288) ? -1 : 2 * wave + 1;
      u32x4 zr[2][2], zi[2][2];
      if (act) {
#pragma unroll
        for (int z = 0; z < 4; ++z) {
          const char* p = img + (z * 64 + lane) * 256;
          zr[z >> 1][z & 1] = *(const u32x4*)(p + ((slot_re ^ (lane & 15)) * 16));
          zi[z >> 1][z & 1] = slot_im >= 0 ? *(const u32x4*)(p + ((slot_im ^ (lane & 15)) * 16)) : u32x4{0, 0, 0, 0};
        }
      }
      WAIT_LGKM0();
      __builtin_amdgcn_s_barrier();
      if (act) colfft_wave(smem + lane * 16, ws, c, lane, wsc, zr, zi, img, slot_re, slot_im);
      WAIT_LGKM0();
      __builtin_amdgcn_s_barrier();
      for (int i = wave; i < 64; i += 12) {
        const int Q = 4 * i + (lane >> 4);
        const u32x4 v = *(const u32x4*)(img + Q * 256 + (((lane & 15) ^ ((Q >> 3) & 15)) * 16));
        if (me == 36 ? (lane & 15) == 1 : (me != 0 || (lane & 15) != 1)) *(u32x4*)(ws + OFF_Y1 + (size_t)Q * 9216 + cbk * 256 + (lane & 15) * 16) = v;
      }
    }
    const int r0 = (g >> 1) * 1152 + PF2_ROWS, r1 = (g >> 1) * 1152 + PF2_END, rm = r0 + ((r1 - r0) * PF3_SPLIT) / 100;
    const int nlate = (37 - g + 7) / 8, nearly = 8 - nlate, k = idx - 24;
    if (me >= 37) warm_rows<NT3>(w2, 18874368u, S_ * 2, r0, rm, (g & 1) * 2048u, 16, 0, (k - nlate) * 12 + wave, nearly * 12, lane, ldsb + wave * 256);
    else warm_rows<NT3>(w2, 18874368u, S_ * 2, rm, r1, (g & 1) * 2048u, 16, 0, k * 12 + wave, nlate * 12, lane, ldsb + wave * 256);
  }
  STAMP_END();
}

constexpr int K4_Y = 0, K4_C = 9216, K4_E = K4_C + 2048, K4_TW = K4_E + 6144, K4_O = K4_TW + 8192, K4_OS = 2336, K4_LDS = K4_O + 8 * K4_OS;

__global__ __launch_bounds__(256, 2) void k_fin(const u16* __restrict__ x, const u16* __restrict__ b2, char* __restrict__ ws, u16* __restrict__ out) {
  __shared__ __attribute__((aligned(16))) char lds[K4_LDS];
  const int tid = threadIdx.x, lane = tid & 63, wave = tid >> 6, Q = blockIdx.x;
  STAMP_BEGIN(3)
  u32x4 yv[3];
#pragma unroll
  for (int it = 0; it < 3; ++it) { const int p = min(tid + it * 256, 575); yv[it] = *(const u32x4*)(ws + OFF_Y1 + (size_t)Q * 9216 + p * 16); }
  u32x4 sl[3][4], xv[3];
#pragma unroll
  for (int it = 0; it < 3; ++it) {
    const int p = min(tid + it * 256, 575), sr = p & 7, dp = p >> 3, s = Q * 8 + sr;
    const size_t so = OFF_SLAB + ((size_t)(s >> 5) * 18 + (dp >> 2)) * 2048 + (dp & 1) * 1024 + (((dp >> 1) & 1) * 32 + (s & 31)) * 16;
#pragma unroll
    for (int q = 0; q < 4; ++q) sl[it][q] = *(const u32x4*)(ws + so + (size_t)q * 2359296);
    xv[it] = *(const u32x4*)(x + (size_t)s * D_ + dp * 8);
  }
  const float bias = lo16(b2[Q * 8 + (tid & 7)]);
  const unsigned E = wmax_exp(ws, lane);
  const float fs = __uint_as_float((E + 1u) << 23) * (1.0f / 9.0f);
#pragma unroll
  for (int z = 0; z < 4; ++z) {
    const int fr = wave + 4 * z, r = lane & 31, hh = lane >> 5;
    u32x4 val;
    if (fr < 2) {
      float o[8];
#pragma unroll
      for (int j = 0; j < 8; ++j) {
        const int k1 = 8 * (j >> 2) + 4 * hh + (j & 3);
        const float v = cs(fr, (float)((r * k1) % 18) * (1.0f / 18.0f)) * ((k1 == 0 || k1 == 9) ? 1.f : 2.f);
        o[j] = (k1 < 10 && r < 18) ? v : 0.f;
      }
      val = u32x4{pack_f16(o[0], o[1]), pack_f16(o[2], o[3]), pack_f16(o[4], o[5]), pack_f16(o[6], o[7])};
    } else if (fr < 8) {
      val = e32_frag(fr - 2, lane);
    } else {
      const int p = fr - 8;
#pragma unroll
      for (int y = 0; y < 2; ++y) {
        const int rho = 2 * p + y, k1 = ((rho & 3) + 8 * (rho >> 2) + 4 * hh) & 15;
        const float a = (float)(k1 * r) * (1.0f / 576.0f);
        val[y] = __float_as_uint(__builtin_amdgcn_cosf(a) * 0.125f); val[2 + y] = __float_as_uint(__builtin_amdgcn_sinf(a) * 0.125f);
      }
    }
    *(u32x4*)(lds + K4_C + fr * 1024 + lane * 16) = val;
  }
#pragma unroll
  for (int it = 0; it < 3; ++it) { const int p = tid + it * 256; if (p < 576) *(u32x4*)(lds + K4_Y + p * 16) = yv[it]; }
  __syncthreads();
  {
    const int row = lane & 31, hh = lane >> 5, k1 = row & 15, rr = 2 * wave + (row >> 4);
    const bool ok = k1 < 10;
    const int k1c = ok ? k1 : 0;
    f32x16 re = {}, im = {};
    f32x4 tw[8];
    tw_load(tw, lds + K4_TW + lane * 16);
#pragma unroll
    for (int u = 0; u < 2; ++u) {
      unsigned ar[4], ai[4];
#pragma unroll
      for (int j = 0; j < 8; ++j) {
        const int k2 = 16 * u + 8 * (j >> 2) + (j & 3);
        const int k = k1c + 18 * (k2 + 4 * hh), c = u == 0 ? k : 576 - k;
        const char* p = lds + K4_Y + rr * 2 + (c == 288 ? 16 : 32 * c);
        unsigned vr = *(const u16*)p, vi = *(const u16*)(p + 16);
        if (c == 0 || c == 288) vi = 0;
        if (j & 1) { ar[j >> 1] |= vr << 16; ai[j >> 1] |= vi << 16; } else { ar[j >> 1] = vr; ai[j >> 1] = vi; }
      }
      const unsigned sg = u ? 0x80008000u : 0u;
      const u32x4 a_r = ok ? u32x4{ar[0], ar[1], ar[2], ar[3]} : u32x4{0, 0, 0, 0};
      const u32x4 a_i = ok ? u32x4{ai[0] ^ sg, ai[1] ^ sg, ai[2] ^ sg, ai[3] ^ sg} : u32x4{0, 0, 0, 0};
      const char* ef = lds + K4_E + u * 3072 + lane * 16;
      const u32x4 er = *(const u32x4*)ef, ei = *(const u32x4*)(ef + 1024), en = *(const u32x4*)(ef + 2048);
      re = mfma32<true>(a_r, er, re); im = mfma32<true>(a_r, en, im);
      re = mfma32<true>(a_i, ei, re); im = mfma32<true>(a_i, er, im);
    }
    tw_apply(re, im, tw);
    const u32x4 cr = *(const u32x4*)(lds + K4_C + lane * 16), ci = *(const u32x4*)(lds + K4_C + 1024 + lane * 16);
#pragma unroll
    for (int u = 0; u < 2; ++u) {
      f32x16 y = {};
      y = mfma32<true>(cr, pack8_f16(re, u), y);
      y = mfma32<true>(ci, pack8_f16(im, u), y);
      float* o = (float*)(lds + K4_O + (2 * wave + u) * K4_OS) + (lane & 31) + 128 * hh;
#pragma unroll
      for (int rho = 0; rho < 8; ++rho) o[32 * ((rho & 3) + 8 * (rho >> 2))] = y[rho];
      if (hh == 0) { o[32 * 16] = y[8]; o[32 * 17] = y[9]; }
    }
  }
  __syncthreads();
#pragma unroll
  for (int it = 0; it < 3; ++it) {
    const int p = tid + it * 256;
    if (p < 576) {
      const int sr = p & 7, dp = p >> 3;
      const f32x4 y0 = *(const f32x4*)(lds + K4_O + sr * K4_OS + dp * 32), y1 = *(const f32x4*)(lds + K4_O + sr * K4_OS + dp * 32 + 16);
      u32x4 o;
#pragma unroll
      for (int e = 0; e < 4; ++e) {
        const float ya = e < 2 ? y0[2 * e] : y1[2 * e - 4], yb = e < 2 ? y0[2 * e + 1] : y1[2 * e - 3];
        const float a = (lo16(sl[it][0][e]) + lo16(sl[it][1][e])) + (lo16(sl[it][2][e]) + lo16(sl[it][3][e])) + bias + lo16(xv[it][e]) + ya * fs;
        const float b = (hi16(sl[it][0][e]) + hi16(sl[it][1][e])) + (hi16(sl[it][2][e]) + hi16(sl[it][3][e])) + bias + hi16(xv[it][e]) + yb * fs;
        o[e] = pack_bf16(a, b);
      }
      *(u32x4*)(out + (size_t)(Q * 8 + sr) * D_ + dp * 8) = o;
    }
  }
  STAMP_END();
}

extern "C" void kernel_launch(void* const* d_in, const int* in_sizes, int n_in, void* d_out, int out_size, void* d_ws, size_t ws_size,
                              hipStream_t stream) {
  const u16* x = (const u16*)d_in[0];
  const u16* wr = (const u16*)d_in[1];
  const u16* wi = (const u16*)d_in[2];
  const char* w1 = (const char*)d_in[4];
  const u16* b1 = (const u16*)d_in[5];
  const char* w2 = (const char*)d_in[6];
  const u16* b2 = (const u16*)d_in[7];
  char* ws = (char*)d_ws + ((ws_size - WS_END) & ~(size_t)4095);
  (void)hipFuncSetAttribute((const void*)k_g1, hipFuncAttributeMaxDynamicSharedMemorySize, CG1::LDS_BYTES);
  (void)hipFuncSetAttribute((const void*)k_g2, hipFuncAttributeMaxDynamicSharedMemorySize, CG2::LDS_BYTES);
  k_prep<<<528 + PF1_BLOCKS, 256, 0, stream>>>(x, wr, wi, w1, ws);
  k_g1<<<256, 704, CG1::LDS_BYTES, stream>>>(w1, b1, w2, ws);
  k_g2<<<256, 768, CG2::LDS_BYTES, stream>>>(w2, ws);
  k_fin<<<256, 256, 0, stream>>>(x, b2, ws, (u16*)d_out);
}
```

```cpp
#define PF1_ROWS 1024
#define PF2_ROWS 0
#define PF2_END 0
#define PF2T 6
#define NTF2 10
#include <hip/hip_runtime.h>
#include <hip/hip_bf16.h>
#include <stdint.h>

#define DI __device__ __forceinline__
#define LAS __attribute__((address_space(3)))
typedef unsigned short u16;
typedef __attribute__((ext_vector_type(2))) float f32x2;
typedef __attribute__((ext_vector_type(4))) float f32x4;
typedef __attribute__((ext_vector_type(16))) float f32x16;
typedef __attribute__((ext_vector_type(2))) _Float16 f16x2;
typedef __attribute__((ext_vector_type(8))) _Float16 f16x8;
typedef __attribute__((ext_vector_type(2))) __bf16 bf16x2;
typedef __attribute__((ext_vector_type(8))) __bf16 bf16x8;
typedef __attribute__((ext_vector_type(4))) short s16x4;
typedef __attribute__((ext_vector_type(2))) unsigned u32x2;
typedef __attribute__((ext_vector_type(4))) unsigned u32x4;

#define PF1_BLOCKS 128
#define NRD 3
#define G2_MAP 0
#define PF3_SPLIT 70
#define NT1 0
#define NT2 0
#define NT3 0
#define NTF1 99
#define PF1_END 2048
#define SLEEP_SLOW 16
#define SLEEP_FAST 2
#define PF2W 1
#define PKMATH 1
#define NCF 4

constexpr int S_ = 2048, D_ = 576, F_ = 4608, NC_ = 289;

constexpr size_t OFF_H    = 0;
constexpr size_t OFF_WT   = OFF_H + 2359296;
constexpr size_t OFF_TAB  = OFF_WT + 2367488;
constexpr size_t OFF_RT   = OFF_TAB + 65536;
constexpr size_t OFF_Y288 = OFF_RT + 16384;
constexpr size_t OFF_WMAX = OFF_Y288 + 4096;
constexpr size_t OFF_X1   = OFF_WMAX + 4096;
constexpr size_t OFF_Y1   = OFF_X1 + 2359296;
constexpr size_t OFF_U    = OFF_Y1 + 2359296;
constexpr size_t OFF_SLAB = OFF_U + 5308416;
constexpr size_t OFF_R    = OFF_SLAB + 9437184;
constexpr size_t OFF_SYNC = OFF_R + 2359296;
constexpr size_t WS_END   = OFF_SYNC + 32768;
constexpr int TAB_E1 = 0, TAB_EO = 6144, TAB_EOI = 12288, TAB_TW1 = 18432, TAB_TW2 = 34816, TAB_BYTES = 51200;

DI unsigned pack_bf16(float a, float b) { f32x2 v = {a, b}; return __builtin_bit_cast(unsigned, __builtin_convertvector(v, bf16x2)); }
DI unsigned pack_f16(float a, float b) { f32x2 v = {a, b}; return __builtin_bit_cast(unsigned, __builtin_convertvector(v, f16x2)); }
DI float lo16(unsigned p) { return __uint_as_float(p << 16); }
DI float hi16(unsigned p) { return __uint_as_float(p & 0xffff0000u); }
DI float rbf(float a) { return lo16(pack_bf16(a, a)); }

template <int CTRL> DI float dpp_add(float v) {
  return v + __int_as_float(__builtin_amdgcn_update_dpp(0, __float_as_int(v), CTRL, 0xf, 0xf, true));
}
DI float sum32(float v) {
  v = dpp_add<0xB1>(v); v = dpp_add<0x4E>(v); v = dpp_add<0x141>(v); v = dpp_add<0x140>(v);
  auto r = __builtin_amdgcn_permlane16_swap(__float_as_uint(v), __float_as_uint(v), false, false);
  return __uint_as_float(r[0]) + __uint_as_float(r[1]);
}

#define STAMP_BEGIN(k)
#define STAMP_END()
#define WAIT_VM(n) asm volatile("s_waitcnt vmcnt(%0)" ::"n"(n) : "memory")
#define WAIT_LGKM0() asm volatile("s_waitcnt lgkmcnt(0)" ::: "memory")
#define BAR() do { asm volatile("" ::: "memory"); __builtin_amdgcn_s_barrier(); asm volatile("" ::: "memory"); } while (0)
#define SCHED0() __builtin_amdgcn_sched_barrier(0)

DI u32x4 mk_rsrc(const void* p, unsigned bytes) {
  const unsigned long long a = (unsigned long long)p;
  return u32x4{(unsigned)a, (unsigned)(a >> 32) & 0xffffu, bytes, 0x00020000u};
}
DI void dma16(u32x4 rsrc, unsigned lds_off, unsigned voff, unsigned soff) {
  asm volatile("s_mov_b32 m0, %0\n\ts_nop 0\n\tbuffer_load_dwordx4 %1, %2, %3 offen lds" ::"s"(lds_off), "v"(voff), "s"(rsrc), "s"(soff) : "memory");
}
DI void dma16nt(u32x4 rsrc, unsigned lds_off, unsigned voff, unsigned soff) {
  asm volatile("s_mov_b32 m0, %0\n\ts_nop 0\n\tbuffer_load_dwordx4 %1, %2, %3 offen nt lds" ::"s"(lds_off), "v"(voff), "s"(rsrc), "s"(soff) : "memory");
}
template <bool NT> DI void dma4(u32x4 rsrc, unsigned lds_off, unsigned voff, unsigned soff) {
  if constexpr (NT) asm volatile("s_mov_b32 m0, %0\n\ts_nop 0\n\tbuffer_load_dword %1, %2, %3 offen nt lds" ::"s"(lds_off), "v"(voff), "s"(rsrc), "s"(soff) : "memory");
  else asm volatile("s_mov_b32 m0, %0\n\ts_nop 0\n\tbuffer_load_dword %1, %2, %3 offen lds" ::"s"(lds_off), "v"(voff), "s"(rsrc), "s"(soff) : "memory");
}
template <bool NT = false>
DI void warm_rows(const char* base, unsigned bytes, unsigned ld, int r0, int r1, unsigned c0, int nl, int mode, int worker, int nworkers,
                  int lane, unsigned lds_dummy) {
  const u32x4 r = mk_rsrc(base, bytes);
  const int total = (r1 - r0) * nl;
  for (int i0 = worker * 64; i0 < total; i0 += nworkers * 64) {
    const int i = i0 + lane;
    const int rr = i / nl, l = i - rr * nl, row = r0 + rr;
    const unsigned prow = mode == 1 ? (unsigned)(64 * (row & 31) + (row >> 5)) : (mode == 2 ? (unsigned)((row & 3) * 1152 + (row >> 2)) : (unsigned)row);
    const unsigned voff = i < total ? prow * ld + c0 + (unsigned)l * 128u : 0xfffffff0u;
    dma4<NT>(r, lds_dummy, voff, 0u);
    WAIT_VM(7);
  }
  WAIT_VM(0);
}

template <bool F16> DI f32x16 mfma32(u32x4 a, u32x4 b, f32x16 c) {
  if constexpr (F16) return __builtin_amdgcn_mfma_f32_32x32x16_f16(__builtin_bit_cast(f16x8, a), __builtin_bit_cast(f16x8, b), c, 0, 0, 0);
  else return __builtin_amdgcn_mfma_f32_32x32x16_bf16(__builtin_bit_cast(bf16x8, a), __builtin_bit_cast(bf16x8, b), c, 0, 0, 0);
}
DI u32x4 pack8_f16(const f32x16& v, int u) {
  return u32x4{pack_f16(v[8 * u], v[8 * u + 1]), pack_f16(v[8 * u + 2], v[8 * u + 3]), pack_f16(v[8 * u + 4], v[8 * u + 5]), pack_f16(v[8 * u + 6], v[8 * u + 7])};
}
DI u32x4 tr_frag(const char* p0, const char* p1) {
  s16x4 lo = __builtin_amdgcn_ds_read_tr16_b64_v4i16((LAS s16x4*)p0);
  s16x4 hi = __builtin_amdgcn_ds_read_tr16_b64_v4i16((LAS s16x4*)p1);
  u32x2 a = __builtin_bit_cast(u32x2, lo), c = __builtin_bit_cast(u32x2, hi);
  return u32x4{a[0], a[1], c[0], c[1]};
}
DI float opq(float v) { asm("" : "+v"(v)); return v; }
DI void tw_piece(f32x16& re, f32x16& im, int p, const f32x4 w) {
  const f32x2 r = {re[2 * p], re[2 * p + 1]}, i = {im[2 * p], im[2 * p + 1]}, wr = {w[0], w[1]}, wi = {w[2], w[3]};
  const f32x2 a = __builtin_elementwise_fma(r, wr, -(i * wi)), b = __builtin_elementwise_fma(r, wi, i * wr);
  re[2 * p] = a[0]; re[2 * p + 1] = a[1]; im[2 * p] = b[0]; im[2 * p + 1] = b[1];
}
DI void tw_load(f32x4 (&t)[8], const char* tw) {
#pragma unroll
  for (int p = 0; p < 8; ++p) t[p] = *(const f32x4*)(tw + p * 1024);
}
DI void tw_apply(f32x16& re, f32x16& im, const f32x4 (&t)[8]) {
#pragma unroll
  for (int p = 0; p < 8; ++p) tw_piece(re, im, p, t[p]);
}
DI float cs(int which, float rev) {
  return which == 0 ? __builtin_amdgcn_cosf(rev) : (which == 1 ? -__builtin_amdgcn_sinf(rev) : __builtin_amdgcn_sinf(rev));
}
DI u32x4 e32_frag(int frag, int lane) {
  const int t = frag / 3, which = frag - 3 * t, r = lane & 31, h = lane >> 5;
  float o[8];
#pragma unroll
  for (int j = 0; j < 8; ++j) o[j] = cs(which, (float)(((16 * t + 8 * (j >> 2) + 4 * h + (j & 3)) * r) & 31) * (1.0f / 32.0f));
  return u32x4{pack_f16(o[0], o[1]), pack_f16(o[2], o[3]), pack_f16(o[4], o[5]), pack_f16(o[6], o[7])};
}

constexpr int K1_RS = 1216, K1_XS = 0, K1_HS = 8 * K1_RS, K1_C1 = 16 * K1_RS, K1_E = K1_C1 + 6144, K1_TW = K1_E + 6144, K1_O = K1_TW + 8192,
              K1_LDS = K1_O + 9216;

__global__ __launch_bounds__(256, 2) void k_prep(const u16* __restrict__ x, const u16* __restrict__ wr, const u16* __restrict__ wi,
                                              const char* __restrict__ w1, char* __restrict__ ws) {
  __shared__ __attribute__((aligned(16))) char lds[K1_LDS];
  const int b = blockIdx.x, tid = threadIdx.x;
  STAMP_BEGIN(0)
  if (b < 256) {
    constexpr int RS = K1_RS;
    const int s2 = b >> 2, t = (b >> 1) & 1, h = b & 1;
    const int rr = tid >> 5, l32 = tid & 31, wave = tid >> 6, lane = tid & 63;
    const int s1 = 16 * t + 8 * (rr >> 2) + 4 * h + (rr & 3);
    const unsigned* xr = (const unsigned*)(x + (size_t)(64 * s1 + s2) * D_);
    unsigned raw[9];
#pragma unroll
    for (int i = 0; i < 9; ++i) raw[i] = xr[32 * i + l32];
#pragma unroll
    for (int z = 0; z < 5; ++z) {
      const int fr = wave + 4 * z;
      u32x4 val;
      const int col = lane & 31, hh = lane >> 5, r = col >> 4, k1 = col & 15;
      if (fr < 6) {
        const int ks = fr >> 1, which = fr & 1;
        float o[8];
#pragma unroll
        for (int j = 0; j < 8; ++j) {
          int row, d1;
          if (ks < 2) { row = ks; d1 = 8 * (j >> 2) + 4 * hh + (j & 3); }
          else { row = (hh == 0 && j < 4) ? (j >> 1) : 2; d1 = 16 + (j & 1); }
          const float v = cs(which, (float)((d1 * k1) % 18) * (1.0f / 18.0f));
          o[j] = (row == r && k1 < 10) ? v : 0.f;
        }
        val = u32x4{pack_f16(o[0], o[1]), pack_f16(o[2], o[3]), pack_f16(o[4], o[5]), pack_f16(o[6], o[7])};
      } else if (fr < 12) {
        val = e32_frag(fr - 6, lane);
      } else {
        const int p = fr - 12;
#pragma unroll
        for (int y = 0; y < 2; ++y) {
          const int rho = 2 * p + y, d2 = (rho & 3) + 8 * (rho >> 2) + 4 * hh;
          const float a = (float)(d2 * k1) * (1.0f / 576.0f);
          val[y] = __float_as_uint(__builtin_amdgcn_cosf(a)); val[2 + y] = __float_as_uint(-__builtin_amdgcn_sinf(a));
        }
      }
      *(u32x4*)(lds + K1_C1 + fr * 1024 + lane * 16) = val;
    }
    float v[18];
#pragma unroll
    for (int i = 0; i < 9; ++i) { v[2 * i] = lo16(raw[i]); v[2 * i + 1] = hi16(raw[i]); }
#pragma unroll
    for (int pass = 0; pass < 2; ++pass) {
      float sm = 0.f;
#pragma unroll
      for (int i = 0; i < 18; ++i) sm += v[i];
      const float mean = sum32(sm) * (1.0f / 576.0f);
      float sq = 0.f;
#pragma unroll
      for (int i = 0; i < 18; ++i) { float dlt = v[i] - mean; sq += dlt * dlt; }
      const float var = sum32(sq) * (1.0f / 576.0f);
      const float mb = rbf(mean);
      const float sd = rbf(__builtin_sqrtf(rbf(rbf(var) + 1e-5f)));
      const float rs = 1.0f / sd;
      char* dst = lds + pass * (8 * RS) + rr * RS + l32 * 4;
#pragma unroll
      for (int i = 0; i < 9; ++i) {
        unsigned p = pack_bf16(v[2 * i] - mb, v[2 * i + 1] - mb);
        p = pack_bf16(lo16(p) * rs, hi16(p) * rs);
        v[2 * i] = lo16(p); v[2 * i + 1] = hi16(p);
        *(unsigned*)(dst + i * 128) = pass == 0 ? pack_f16(v[2 * i], v[2 * i + 1]) : p;
      }
    }
    __syncthreads();
    for (int cb = wave; cb < 9; cb += 4) {
      const int gg = lane >> 4, q = (lane & 15) >> 2, pp = lane & 3;
      const char* src = lds + K1_HS + q * RS + (cb * 64 + gg * 16 + pp * 4) * 2;
      const u32x4 val = tr_frag(src, src + 4 * RS);
      const int df = cb * 2 + (gg >> 1), i = 8 * q + 4 * (gg & 1) + pp;
      *(u32x4*)(ws + OFF_H + ((size_t)df * 128 + 2 * s2 + t) * 1024 + (h * 32 + i) * 16) = val;
    }
    {
      const int hh = lane >> 5, blk = (lane >> 4) & 1, q = (lane & 15) >> 2, p = lane & 3;
      const char* xa = lds + K1_XS + (2 * wave) * RS + blk * 32 + p * 8;
      const char* cf = lds + K1_C1 + lane * 16;
      f32x16 re = {}, im = {};
      f32x4 tw[8];
      tw_load(tw, lds + K1_TW + lane * 16);
#pragma unroll
      for (int ks = 0; ks < 3; ++ks) {
        const char* a0 = ks < 2 ? xa + ks * RS + (4 * hh + q) * 64 : xa + (q >> 1) * RS + (16 + (q & 1)) * 64;
        const u32x4 a = tr_frag(a0, ks < 2 ? a0 + 512 : a0);
        re = mfma32<true>(a, *(const u32x4*)(cf + (2 * ks) * 1024), re);
        im = mfma32<true>(a, *(const u32x4*)(cf + (2 * ks + 1) * 1024), im);
      }
      tw_apply(re, im, tw);
      f32x16 xr_ = {}, xi_ = {};
#pragma unroll
      for (int u = 0; u < 2; ++u) {
        const u32x4 ar = pack8_f16(re, u), ai = pack8_f16(im, u);
        const char* ef = lds + K1_E + u * 3072 + lane * 16;
        const u32x4 er = *(const u32x4*)ef, ei = *(const u32x4*)(ef + 1024), en = *(const u32x4*)(ef + 2048);
        xr_ = mfma32<true>(er, ar, xr_); xi_ = mfma32<true>(ei, ar, xi_);
        xr_ = mfma32<true>(en, ai, xr_); xi_ = mfma32<true>(er, ai, xi_);
      }
      const int col = lane & 31, k1 = col & 15, row2 = 2 * (2 * wave + (col >> 4));
      if (k1 < 10) {
        char* o1 = lds + K1_O + 32 * k1 + 576 * 4 * hh + row2;
        char* o2 = lds + K1_O + 32 * (576 - k1) - 576 * (4 * hh + 27) + row2;
#pragma unroll
        for (int rho = 0; rho < 8; ++rho) {
          const unsigned pk = pack_f16(xr_[rho] * (1.0f / 32.0f), xi_[rho] * (1.0f / 32.0f));
          char* d = o1 + 576 * ((rho & 3) + 8 * (rho >> 2));
          *(u16*)d = (u16)pk;
          if (rho != 0 || k1 != 0 || hh != 0) *(u16*)(d + 16) = (u16)(pk >> 16);
        }
        if (k1 >= 1 && k1 <= 8) {
#pragma unroll
          for (int rho = 8; rho < 16; ++rho) {
            const unsigned pk = pack_f16(xr_[rho] * (1.0f / 32.0f), xi_[rho] * (-1.0f / 32.0f));
            char* d = o2 + 576 * (27 - ((rho & 3) + 8 * (rho >> 2)));
            *(u16*)d = (u16)pk;
            *(u16*)(d + 16) = (u16)(pk >> 16);
          }
        } else if (k1 == 0 && hh == 0) {
          *(u16*)(lds + K1_O + 16 + row2) = (u16)pack_f16(xr_[8] * (1.0f / 32.0f), 0.f);
        }
      }
    }
    __syncthreads();
    const int P = ((s2 & 1) * 2 + t) * 64 + h * 32 + (s2 >> 1);
#pragma unroll
    for (int it = 0; it < 3; ++it) {
      const int p = tid + it * 256;
      if (p < 576) *(u32x4*)(ws + OFF_X1 + (size_t)P * 9216 + p * 16) = *(const u32x4*)(lds + K1_O + p * 16);
    }
  } else if (b < 512) {
    const int ab_ = b - 256;
    const int rg = ab_ >> 2, cg = ab_ & 3;
    const int c0 = cg * 72 + (cg > 0), nc = cg == 0 ? 73 : 72;
    const int kq = rg >> 2, ko = rg & 3;
    unsigned rv[10], iv[10];
#pragma unroll
    for (int it = 0; it < 10; ++it) {
      const int idx = tid + it * 256, row = idx / 73, col = idx - row * 73;
      const bool ok = idx < 32 * 73 && col < nc;
      const int ks = 8 * ko + (row & 7) + 32 * (4 * kq + (row >> 3));
      const int off = ok ? ks * NC_ + c0 + col : 0;
      rv[it] = wr[off]; iv[it] = wi[off];
    }
    unsigned mx = 0;
#pragma unroll
    for (int it = 0; it < 10; ++it) {
      const int idx = tid + it * 256, row = idx / 73, col = idx - row * 73;
      if (idx < 32 * 73 && col < nc) {
        mx = max(mx, max(rv[it] & 0x7fffu, iv[it] & 0x7fffu));
        *(unsigned*)(lds + col * 144 + (row & 7) * 16 + (row >> 3) * 4) = rv[it] | (iv[it] << 16);
      }
    }
#pragma unroll
    for (int o = 32; o; o >>= 1) mx = max(mx, (unsigned)__shfl_xor((int)mx, o));
    unsigned* red = (unsigned*)(lds + 16384);
    if ((tid & 63) == 0) red[tid >> 6] = mx;
    __syncthreads();
    if (tid == 0) ((unsigned*)(ws + OFF_WMAX))[ab_] = max(max(red[0], red[1]), max(red[2], red[3]));
    const int U = kq >> 3, h = kq & 1, rq = (kq >> 1) & 3;
#pragma unroll
    for (int it = 0; it < 3; ++it) {
      const int p = tid + it * 256;
      if (p < nc * 8) {
        const int col = p >> 3, a = p & 7;
        u32x4 val = *(const u32x4*)(lds + col * 144 + a * 16);
        *(u32x4*)(ws + OFF_WT + (size_t)(c0 + col) * 8192 + ((U * 4 + rq) * 64 + h * 32 + 8 * ko + a) * 16) = val;
      }
    }
  } else if (b < 528) {
    const int PP = (b - 512) * 256 + tid;
    if (b >= 520) *(u32x4*)(ws + OFF_SYNC + ((b - 520) * 256 + tid) * 16) = u32x4{0, 0, 0, 0};
    if (PP < TAB_BYTES / 16) {
      u32x4 val;
      if (PP < 384) val = e32_frag(PP >> 6, PP & 63);
      else if (PP < 1152) {
        const int lane = PP & 63, r = lane & 31, h = lane >> 5, frag = (PP >> 6) - 6;
        const bool inv = frag >= 6;
        const int f2 = inv ? frag - 6 : frag, u = f2 / 3, which = f2 - 3 * u;
        float o[8];
#pragma unroll
        for (int j = 0; j < 8; ++j) {
          const int kap = 16 * u + 8 * (j >> 2) + 4 * h + (j & 3);
          o[j] = cs(which, (float)((inv ? kap * (2 * r + 1) : r * (2 * kap + 1)) & 63) * (1.0f / 64.0f));
        }
        val = u32x4{pack_f16(o[0], o[1]), pack_f16(o[2], o[3]), pack_f16(o[4], o[5]), pack_f16(o[6], o[7])};
      } else {
        const int Q0 = PP - 1152;
        const bool inv = Q0 >= 1024;
        const int Q = inv ? Q0 - 1024 : Q0;
        const int lane = Q & 63, p = (Q >> 6) & 7, T = Q >> 9, r = lane & 31, h = lane >> 5;
#pragma unroll
        for (int z = 0; z < 2; ++z) {
          const int rho = 2 * p + z, cr = (rho & 3) + 8 * (rho >> 2) + 4 * h;
          const int n = inv ? (2 * r + T) * cr : (2 * cr + T) * r;
          const float a = (float)(n & 2047) * (1.0f / 2048.0f);
          val[z] = __float_as_uint(__builtin_amdgcn_cosf(a) * (inv ? 0.125f : 1.0f));
          val[2 + z] = __float_as_uint(inv ? __builtin_amdgcn_sinf(a) * 0.125f : -__builtin_amdgcn_sinf(a));
        }
      }
      *(u32x4*)(ws + OFF_TAB + (size_t)PP * 16) = val;
    }
    if (PP < 1024) {
      const int fr = PP >> 6, lane = PP & 63, r = lane & 31, hh = lane >> 5;
      u32x4 val;
      if (fr < 2) {
        float o[8];
#pragma unroll
        for (int j = 0; j < 8; ++j) {
          const int k1 = 8 * (j >> 2) + 4 * hh + (j & 3);
          const float v = cs(fr, (float)((r * k1) % 18) * (1.0f / 18.0f)) * ((k1 == 0 || k1 == 9) ? 1.f : 2.f);
          o[j] = (k1 < 10 && r < 18) ? v : 0.f;
        }
        val = u32x4{pack_f16(o[0], o[1]), pack_f16(o[2], o[3]), pack_f16(o[4], o[5]), pack_f16(o[6], o[7])};
      } else if (fr < 8) {
        val = e32_frag(fr - 2, lane);
      } else {
#pragma unroll
        for (int y = 0; y < 2; ++y) {
          const int rho = 2 * (fr - 8) + y, k1 = ((rho & 3) + 8 * (rho >> 2) + 4 * hh) & 15;
          const float a = (float)(k1 * r) * (1.0f / 576.0f);
          val[y] = __float_as_uint(__builtin_amdgcn_cosf(a) * 0.125f); val[2 + y] = __float_as_uint(__builtin_amdgcn_sinf(a) * 0.125f);
        }
      }
      *(u32x4*)(ws + OFF_RT + (size_t)PP * 16) = val;
    }
  } else {
    const int pb = b - 528;
    const int g = b & 7, wave = __builtin_amdgcn_readfirstlane(tid >> 6);
    const int ft0 = (g * 27) / 6, ft1 = (g * 27 + 26) / 6;
    warm_rows<NT1>(w1, 18874368u, F_ * 2, 0, PF1_ROWS, ft0 * 256u, (ft1 - ft0 + 1) * 2, 1, (pb >> 3) * 4 + wave, (PF1_BLOCKS >> 3) * 4, tid & 63,
              (unsigned)(size_t)(LAS char*)lds + wave * 256);
  }
  STAMP_END();
}

enum { LIN = 0, TR128 = 1, TR64 = 2 };

template <int V> struct IC { static constexpr int value = V; };
template <int NA_W, int NB_W, int WA, int WB, int AK, int BK, int NL, int NS, int DPM, int DEPTH>
struct GC {
  static constexpr int NC = WA * WB, NFA = NA_W * WA, NFB = NB_W * WB;
  static constexpr int A_BYTES = NFA * 4096, B_BYTES = NFB * 4096, STAGE = A_BYTES + B_BYTES;
  static constexpr int IA = NFA * 4, IB = NFB * 4, NI = IA + IB, G = NI / NL;
  static constexpr int LDS_BYTES = NS * STAGE;
  static_assert(NI % NL == 0, "");
  static_assert(AK == LIN || NFA == (AK == TR128 ? 4 : 2), "");
  static_assert(BK == LIN || NFB == (BK == TR128 ? 4 : 2), "");

  template <int K> static DI unsigned voff(int lane, unsigned rstride) {
    if constexpr (K == LIN) return lane * 16;
    else if constexpr (K == TR128) return (unsigned)(lane >> 4) * rstride + (unsigned)(((lane & 15) ^ ((lane >> 4) << 2)) * 16);
    else return (unsigned)(lane >> 3) * rstride + (unsigned)(((lane & 7) ^ (((lane >> 4) & 1) << 2)) * 16);
  }
  template <int K, int NF> static DI void frag_addr(unsigned (&addr)[K == LIN ? 1 : NF], int opoff, int wpos, int lane) {
    if constexpr (K == LIN) addr[0] = opoff + wpos * NF * 4096 + lane * 16;
    else {
      const int h = lane >> 5, blk = (lane >> 4) & 1, q = (lane & 15) >> 2, p = lane & 3;
#pragma unroll
      for (int i = 0; i < NF; ++i) {
        const int ch = 4 * (wpos * NF + i) + 2 * blk + (p >> 1);
        if constexpr (K == TR128) addr[i] = opoff + (4 * h + q) * 256 + ((ch ^ (q << 2)) * 16) + 8 * (p & 1);
        else addr[i] = opoff + (4 * h + q) * 128 + ((ch ^ (((q >> 1) & 1) << 2)) * 16) + 8 * (p & 1);
      }
    }
  }
  template <int K, int NF> static DI void rd(u32x4 (&f)[NF], const char* smem, const unsigned (&addr)[K == LIN ? 1 : NF], int sb, int hh) {
#pragma unroll
    for (int i = 0; i < NF; ++i) {
      if constexpr (K == LIN) f[i] = *(const u32x4*)(smem + addr[0] + sb + (i * 4 + hh) * 1024);
      else {
        constexpr int RB = K == TR128 ? 256 : 128;
        const char* p = smem + addr[i] + sb + hh * 16 * RB;
        s16x4 lo = __builtin_amdgcn_ds_read_tr16_b64_v4i16((LAS s16x4*)p);
        s16x4 hi = __builtin_amdgcn_ds_read_tr16_b64_v4i16((LAS s16x4*)(p + 8 * RB));
        u32x2 a = __builtin_bit_cast(u32x2, lo), c = __builtin_bit_cast(u32x2, hi);
        f[i] = u32x4{a[0], a[1], c[0], c[1]};
      }
    }
  }
  static DI void ilv() {
    constexpr int NR = NA_W * (AK == LIN ? 1 : 2) + NB_W * (BK == LIN ? 1 : 2), NM = NA_W * NB_W;
    constexpr int NG = (NR + DPM - 1) / DPM < NM ? (NR + DPM - 1) / DPM : NM;
#pragma unroll
    for (int i = 0; i < NG; ++i) {
      __builtin_amdgcn_sched_group_barrier(0x8, 1, 0);
      if (i + 1 < NG) __builtin_amdgcn_sched_group_barrier(0x100, DPM, 0);
      else __builtin_amdgcn_sched_group_barrier(0x100, NR - DPM * (NG - 1), 0);
    }
    if constexpr (NM > NG) __builtin_amdgcn_sched_group_barrier(0x8, NM - NG, 0);
    SCHED0();
  }
  template <class SOFF, class HOOK>
  static DI void load(char* smem, int lw, int nt, const char* baseA, unsigned bytesA, unsigned vA, const char* baseB, unsigned bytesB,
                      unsigned vB, SOFF soff, int ntfrom, HOOK hook) {
    const unsigned ldsbase = (unsigned)(size_t)(LAS char*)smem + lw * G * 1024;
    unsigned vo[G]; u32x4 rs[G];
#pragma unroll
    for (int m = 0; m < G; ++m) { const bool isA = (lw * G + m) < IA; vo[m] = isA ? vA : vB; rs[m] = mk_rsrc(isA ? baseA : baseB, isA ? bytesA : bytesB); }
    auto issue = [&](int kt, int so) {
#pragma unroll
      for (int m = 0; m < G; ++m) {
        const bool isW = ((lw * G + m) < IA) == (AK != LIN);
        if (isW && kt >= ntfrom) dma16nt(rs[m], ldsbase + so + m * 1024, vo[m], soff(kt, lw * G + m));
        else dma16(rs[m], ldsbase + so + m * 1024, vo[m], soff(kt, lw * G + m));
      }
    };
#pragma unroll
    for (int p = 0; p < NS - 2; ++p) issue(p, p * STAGE);
    hook();
    int wslot = NS - 2;
    for (int tau = 0; tau < nt - (NS - 2); ++tau) {
      WAIT_VM((NS - 3) * G);
      BAR();
      issue(tau + NS - 2, wslot * STAGE);
      wslot = (wslot + 1 == NS) ? 0 : wslot + 1;
    }
    if constexpr (NS >= 5) { WAIT_VM(2 * G); BAR(); }
    if constexpr (NS >= 4) { WAIT_VM(G); BAR(); }
    WAIT_VM(0); BAR();
    static_assert(NS >= 3 && NS <= 5, "");
  }
  template <bool F16>
  static DI void consume(const char* smem, int wave, int lane, int n, int& rslot, f32x16 (&acc)[NA_W][NB_W]) {
    const int wa = wave / WB, wb = wave - wa * WB;
    unsigned addrA[AK == LIN ? 1 : NA_W], addrB[BK == LIN ? 1 : NB_W];
    frag_addr<AK, NA_W>(addrA, 0, wa, lane);
    frag_addr<BK, NB_W>(addrB, A_BYTES, wb, lane);
    u32x4 fa[DEPTH][NA_W], fb[DEPTH][NB_W];
#pragma unroll
    for (int d = 1; d < DEPTH; ++d) {
#pragma unroll
      for (int i = 0; i < NA_W; ++i) fa[d][i] = u32x4{0, 0, 0, 0};
#pragma unroll
      for (int i = 0; i < NB_W; ++i) fb[d][i] = u32x4{0, 0, 0, 0};
    }
    auto mm = [&](const u32x4 (&a)[NA_W], const u32x4 (&b)[NB_W]) {
#pragma unroll
      for (int i = 0; i < NA_W; ++i)
#pragma unroll
        for (int j = 0; j < NB_W; ++j) acc[i][j] = mfma32<F16>(a[i], b[j], acc[i][j]);
    };
    auto step = [&](auto Z0) {
      constexpr int z0 = decltype(Z0)::value;
      BAR();
      SCHED0();
      const int sb = rslot * STAGE;
#pragma unroll
      for (int hh = 0; hh < 4; ++hh) {
        rd<AK, NA_W>(fa[(z0 + hh) % DEPTH], smem, addrA, sb, hh);
        rd<BK, NB_W>(fb[(z0 + hh) % DEPTH], smem, addrB, sb, hh);
        mm(fa[(z0 + hh + 1) % DEPTH], fb[(z0 + hh + 1) % DEPTH]);
        ilv();
      }
      rslot = (rslot + 1 == NS) ? 0 : rslot + 1;
    };
    if constexpr (DEPTH == 2) {
      for (int tau = 0; tau < n; ++tau) step(IC<0>{});
      mm(fa[1], fb[1]);
    } else {
      int tau = 0;
      for (; tau + 3 <= n; tau += 3) { step(IC<0>{}); step(IC<1>{}); step(IC<2>{}); }
      if (n - tau == 2) { step(IC<0>{}); step(IC<1>{}); mm(fa[0], fb[0]); mm(fa[1], fb[1]); }
      else if (n - tau == 1) { step(IC<0>{}); mm(fa[2], fb[2]); mm(fa[0], fb[0]); }
      else { mm(fa[1], fb[1]); mm(fa[2], fb[2]); }
    }
  }
};

DI float gelu_tanh(float u) {
  const float z = u * (1.0f + 0.044715f * u * u) * (-2.0f * 0.7978845608028654f * 1.4426950408889634f);
  return u * __builtin_amdgcn_rcpf(1.0f + __builtin_amdgcn_exp2f(z));
}

template <int NV> DI void mv_ilv() {
#pragma unroll
  for (int i = 0; i < 8; ++i) { __builtin_amdgcn_sched_group_barrier(0x8, 1, 0); __builtin_amdgcn_sched_group_barrier(0x2, NV, 0); }
  SCHED0();
}
struct EF { u32x4 r, i, n; };
DI EF ef_load(const char* p) { EF e; e.r = *(const u32x4*)p; e.i = *(const u32x4*)(p + 1024); e.n = *(const u32x4*)(p + 2048); return e; }
#define MF(a, b, c) mfma32<true>(a, b, c)
DI void cm_dA(f32x16& re, f32x16& im, u32x4 dr, u32x4 di, const EF& e) { re = MF(dr, e.r, re); im = MF(dr, e.i, im); re = MF(di, e.n, re); im = MF(di, e.r, im); }
DI void cm_dB(f32x16& re, f32x16& im, u32x4 dr, u32x4 di, const EF& e) { re = MF(e.r, dr, re); im = MF(e.i, dr, im); re = MF(e.n, di, re); im = MF(e.r, di, im); }
DI void cm_dAc(f32x16& re, f32x16& im, u32x4 dr, u32x4 di, const EF& e) { re = MF(dr, e.r, re); im = MF(dr, e.n, im); re = MF(di, e.i, re); im = MF(di, e.r, im); }
DI void wmul(f32x16& re, f32x16& im, const u32x4 (&wv)[4], float wsc) {
#pragma unroll
  for (int p = 0; p < 8; ++p) {
    const unsigned w0 = wv[p >> 1][(2 * p) & 3], w1 = wv[p >> 1][(2 * p + 1) & 3];
    tw_piece(re, im, p, f32x4{lo16(w0), lo16(w1), hi16(w0), hi16(w1)} * wsc);
  }
}
DI void bfly(f32x16& a, f32x16& b) {
  const f32x16 x = a, y = b; a = x + y; b = x - y;
}
DI void colfft_wave(const char* tab  , const char* ws, int c, int lane, float wsc, const u32x4 (&zr)[2][2], const u32x4 (&zi)[2][2],
                    char* img, int slot_re, int slot_im) {
  f32x4 tw[8];
  f32x16 r0 = {}, i0 = {}, r1 = {}, i1 = {};
  u32x4 ar[2], ai[2];
  EF ea = ef_load(tab + TAB_E1), eb = ef_load(tab + TAB_E1 + 3072);
  tw_load(tw, tab + TAB_TW1);
  SCHED0();
  cm_dA(r0, i0, zr[0][0], zi[0][0], ea); cm_dA(r0, i0, zr[0][1], zi[0][1], eb);
  SCHED0();
  cm_dA(r1, i1, zr[1][0], zi[1][0], ea); cm_dA(r1, i1, zr[1][1], zi[1][1], eb);
#pragma unroll
  for (int p = 0; p < 8; ++p) { tw_piece(r0, i0, p, tw[p]); tw[p] = *(const f32x4*)(tab + TAB_TW1 + 8192 + p * 1024); }
  ar[0] = pack8_f16(r0, 0); ar[1] = pack8_f16(r0, 1); ai[0] = pack8_f16(i0, 0); ai[1] = pack8_f16(i0, 1);
  mv_ilv<PKMATH ? 6 : 10>();
  u32x4 wv0[4], wv1[4];
#pragma unroll
  for (int i = 0; i < 4; ++i) wv0[i] = *(const u32x4*)(ws + OFF_WT + (size_t)c * 8192 + i * 1024 + lane * 16);
  r0 = f32x16{}; i0 = f32x16{};
  cm_dB(r0, i0, ar[0], ai[0], ea); ea = ef_load(tab + TAB_EO);
  cm_dB(r0, i0, ar[1], ai[1], eb); eb = ef_load(tab + TAB_EO + 3072);
  tw_apply(r1, i1, tw);
  ar[0] = pack8_f16(r1, 0); ar[1] = pack8_f16(r1, 1); ai[0] = pack8_f16(i1, 0); ai[1] = pack8_f16(i1, 1);
  mv_ilv<PKMATH ? 6 : 10>();
#pragma unroll
  for (int i = 0; i < 4; ++i) wv1[i] = *(const u32x4*)(ws + OFF_WT + (size_t)c * 8192 + (4 + i) * 1024 + lane * 16);
  r1 = f32x16{}; i1 = f32x16{};
  cm_dB(r1, i1, ar[0], ai[0], ea); ea = ef_load(tab + TAB_E1);
  cm_dB(r1, i1, ar[1], ai[1], eb); eb = ef_load(tab + TAB_E1 + 3072);
  SCHED0();
  bfly(r0, r1); bfly(i0, i1);
  wmul(r0, i0, wv0, wsc);
  wmul(r1, i1, wv1, wsc);
  bfly(r0, r1); bfly(i0, i1);
  u32x4 dr[2], di[2];
  ar[0] = pack8_f16(r0, 0); ar[1] = pack8_f16(r0, 1); ai[0] = pack8_f16(i0, 0); ai[1] = pack8_f16(i0, 1);
  SCHED0();
  tw_load(tw, tab + TAB_TW2);
  r0 = f32x16{}; i0 = f32x16{};
  cm_dAc(r0, i0, ar[0], ai[0], ea); ea = ef_load(tab + TAB_EOI);
  cm_dAc(r0, i0, ar[1], ai[1], eb); eb = ef_load(tab + TAB_EOI + 3072);
  dr[0] = pack8_f16(r1, 0); dr[1] = pack8_f16(r1, 1); di[0] = pack8_f16(i1, 0); di[1] = pack8_f16(i1, 1);
  mv_ilv<2>();
  r1 = f32x16{}; i1 = f32x16{};
  cm_dAc(r1, i1, dr[0], di[0], ea); ea = ef_load(tab + TAB_E1);
  cm_dAc(r1, i1, dr[1], di[1], eb); eb = ef_load(tab + TAB_E1 + 3072);
#pragma unroll
  for (int p = 0; p < 8; ++p) { tw_piece(r0, i0, p, tw[p]); tw[p] = *(const f32x4*)(tab + TAB_TW2 + 8192 + p * 1024); }
  ar[0] = pack8_f16(r0, 0); ar[1] = pack8_f16(r0, 1); ai[0] = pack8_f16(i0, 0); ai[1] = pack8_f16(i0, 1);
  mv_ilv<PKMATH ? 6 : 10>();
  r0 = f32x16{}; i0 = f32x16{};
  cm_dAc(r0, i0, ar[0], ai[0], ea); cm_dAc(r0, i0, ar[1], ai[1], eb);
  tw_apply(r1, i1, tw);
  dr[0] = pack8_f16(r1, 0); dr[1] = pack8_f16(r1, 1); di[0] = pack8_f16(i1, 0); di[1] = pack8_f16(i1, 1);
  mv_ilv<PKMATH ? 6 : 10>();
  r1 = f32x16{}; i1 = f32x16{};
  cm_dAc(r1, i1, dr[0], di[0], ea); cm_dAc(r1, i1, dr[1], di[1], eb);
  SCHED0();
#pragma unroll
  for (int g = 0; g < 4; ++g) {
    const int Q = (lane & 31) * 8 + 2 * g + (lane >> 5), sw = lane & 15, r = 4 * g;
    if (slot_im == -2) {
      *(u32x4*)(img + Q * 16) = u32x4{pack_f16(r0[r], r1[r]), pack_f16(r0[r + 1], r1[r + 1]), pack_f16(r0[r + 2], r1[r + 2]), pack_f16(r0[r + 3], r1[r + 3])};
      continue;
    }
#pragma unroll
    for (int z = 0; z < 2; ++z) {
      const int q = r + 2 * z;
      *(u32x4*)(img + Q * 256 + (((slot_re + z) ^ sw) * 16)) =
          u32x4{pack_f16(r0[q], i0[q]), pack_f16(r1[q], i1[q]), pack_f16(r0[q + 1], i0[q + 1]), pack_f16(r1[q + 1], i1[q + 1])};
    }
  }
}
#undef MF
DI unsigned wmax_exp(const char* ws, int lane) {
  const u32x4 mv = *(const u32x4*)(ws + OFF_WMAX + lane * 16);
  unsigned mx = max(max(mv[0], mv[1]), max(mv[2], mv[3]));
#pragma unroll
  for (int o = 32; o; o >>= 1) mx = max(mx, (unsigned)__shfl_xor((int)mx, o));
  return min(mx >> 7, 252u);
}

template <int NWV>
DI void colfft_block(char* smem, char* ws, int me, int wave, int lane) {
  const unsigned ldsb = (unsigned)(size_t)(LAS char*)smem;
  const int cbk = me == 36 ? 0 : me;
  char* img = smem + 65536;
  {
    const u32x4 r = mk_rsrc(ws + OFF_X1, 2359296u);
    for (int i = wave; i < 64; i += NWV) {
      const int P = 4 * i + (lane >> 4);
      dma16(r, ldsb + 65536 + i * 1024, (unsigned)P * 9216u + (unsigned)(((lane & 15) ^ (P & 15)) * 16), cbk * 256u);
    }
    const u32x4 r2 = mk_rsrc(ws + OFF_TAB, TAB_BYTES);
    for (int i = wave; i < TAB_BYTES / 1024; i += NWV) dma16(r2, ldsb + i * 1024, lane * 16, i * 1024u);
  }
  const bool act = me == 36 ? wave == 0 : wave < 8;
  const int c = me == 36 ? 288 : me * 8 + wave;
  const unsigned E = wmax_exp(ws, lane);
  const float wsc = __uint_as_float((253u - E) << 23) * (1.0f / 64.0f);
  WAIT_VM(0);
  BAR();
  const int slot_re = me == 36 ? 1 : 2 * wave, slot_im = c == 288 ? -2 : (c == 0 ? -1 : 2 * wave + 1);
  u32x4 zr[2][2], zi[2][2];
  if (act) {
#pragma unroll
    for (int z = 0; z < 4; ++z) {
      const char* p = img + (z * 64 + lane) * 256;
      zr[z >> 1][z & 1] = *(const u32x4*)(p + ((slot_re ^ (lane & 15)) * 16));
      zi[z >> 1][z & 1] = slot_im >= 0 ? *(const u32x4*)(p + ((slot_im ^ (lane & 15)) * 16)) : u32x4{0, 0, 0, 0};
    }
  }
  WAIT_LGKM0();
  BAR();
  if (act) colfft_wave(smem + lane * 16, ws, c, lane, wsc, zr, zi, img, slot_re, slot_im);
  WAIT_LGKM0();
  BAR();
  if (me == 36) {
    if (wave < 4) *(u32x4*)(ws + OFF_Y288 + wave * 1024 + lane * 16) = *(const u32x4*)(img + wave * 1024 + lane * 16);
    return;
  }
  for (int i = wave; i < 64; i += NWV) {
    const int Qy = 4 * i + (lane >> 4);
    const u32x4 v = *(const u32x4*)(img + Qy * 256 + (((lane & 15) ^ ((Qy >> 3) & 15)) * 16));
    *(u32x4*)(ws + OFF_Y1 + (size_t)Qy * 9216 + cbk * 256 + (lane & 15) * 16) = v;
  }
}
#define K2_NS 5
using CG1 = GC<1, 3, 4, 1, TR128, LIN, 7, K2_NS, 2, 3>;

__global__ __launch_bounds__(704, 3) void k_g1(const char* __restrict__ w1, const u16* __restrict__ b1, const char* __restrict__ w2,
                                               char* __restrict__ ws) {
  extern __shared__ __attribute__((aligned(16))) char smem[];
  const int lane = threadIdx.x & 63, wave = __builtin_amdgcn_readfirstlane(threadIdx.x >> 6);
  const int g = blockIdx.x & 7, idx = blockIdx.x >> 3;
  STAMP_BEGIN(1)
  if (idx < 27) {
    const int tile = g * 27 + idx, ft = tile / 6, dt = tile - ft * 6;
    char* ebuf = smem + (29 % K2_NS) * CG1::STAGE;
    constexpr int NE = PF2T > 0 ? 11 - PF2W : 11;
    u32x2 bv[3][2];
    auto load_bias = [&] {
#pragma unroll
      for (int it = 0; it < 3; ++it) {
        const u32x4* bp = (const u32x4*)(b1 + ft * 128 + (min(wave + NE * it, 23) / 3) * 16);
        const u32x4 b0 = bp[0], b1v = bp[1];
        const bool hi = lane >= 32;
        bv[it][0] = u32x2{hi ? b0[2] : b0[0], hi ? b0[3] : b0[1]}; bv[it][1] = u32x2{hi ? b1v[2] : b1v[0], hi ? b1v[3] : b1v[1]};
        asm volatile("" : "+v"(bv[it][0]), "+v"(bv[it][1]));
      }
    };
    if (wave >= 4) {
      constexpr unsigned ld = F_ * 2;
      CG1::load(smem, wave - 4, 32, w1, 18874368u, CG1::voff<TR128>(lane, 64 * ld), ws + OFF_H, 2359296u, CG1::voff<LIN>(lane, 0),
                [&](int kt, int a) -> unsigned {
                  return a < 16 ? (unsigned)(((4 * a) & 31) * 64 + 2 * kt + (a >> 3)) * ld + ft * 256u
                                : (unsigned)((dt * 3 + ((a - 16) >> 2)) * 128 + kt * 4 + ((a - 16) & 3)) * 1024u;
                }, NTF1, load_bias);
      if (PF2T > 0 && wave >= 11 - PF2W) {
        const u32x4 r = mk_rsrc(w2, 18874368u);
#pragma unroll
        for (int i = 0; i < PF2T; ++i) {
          const int line = ((i * 27 + idx) * PF2W + (wave - (11 - PF2W))) * 64 + lane;
          dma4<false>(r, (unsigned)(size_t)(LAS char*)smem + (29 % K2_NS) * CG1::STAGE + 24576 + (wave - 4) * 256,
                      (unsigned)((g >> 1) * 1152 + (line >> 4)) * 4096u + (g & 1) * 2048u + (line & 15) * 128u, 0u);
        }
      }
    } else {
      f32x16 acc[1][3] = {};
      int rslot = 0;
      load_bias();
      CG1::consume<false>(smem, wave, lane, 32, rslot, acc);
#pragma unroll
      for (int j = 0; j < 3; ++j)
#pragma unroll
        for (int s = 0; s < 2; ++s) {
          u32x4 val;
#pragma unroll
          for (int e = 0; e < 4; ++e) val[e] = pack_bf16(acc[0][j][8 * s + 2 * e], acc[0][j][8 * s + 2 * e + 1]);
          *(u32x4*)(ebuf + ((wave * 2 + s) * 3 + j) * 1024 + lane * 16) = val;
        }
    }
    WAIT_LGKM0();
    BAR();
#pragma unroll
    for (int it = 0; it < 3; ++it) {
      const int i = wave + NE * it;
      if (i < 24 && wave < NE) {
        const int z = i / 3, j = i - 3 * z;
        const u32x4 a = *(const u32x4*)(ebuf + i * 1024 + lane * 16);
        u32x4 val;
#pragma unroll
        for (int e = 0; e < 4; ++e) {
          const unsigned bb = bv[it][e >> 1][e & 1];
          const unsigned pk = pack_bf16(lo16(a[e]) + lo16(bb), hi16(a[e]) + hi16(bb));
          val[e] = pack_bf16(gelu_tanh(lo16(pk)), gelu_tanh(hi16(pk)));
        }
        *(u32x4*)(ws + OFF_U + ((size_t)(dt * 3 + j) * 288 + ft * 8 + z) * 1024 + lane * 16) = val;
      }
    }
  } else {
    const int worker = (idx - 27) * 11 + wave, me = (idx - 27) * 8 + g;
    const unsigned dummy = (unsigned)(size_t)(LAS char*)smem + 131072 + wave * 256;
    const int ft0 = (g * 27) / 6, ft1 = (g * 27 + 26) / 6;
    warm_rows<NT2>(w1, 18874368u, F_ * 2, PF1_ROWS, PF1_END, ft0 * 256u, (ft1 - ft0 + 1) * 2, 1, worker, 55, lane, dummy);
    if (me < 37) colfft_block<11>(smem, ws, me, wave, lane);
    if (PF2_ROWS > 0) warm_rows(w2, 18874368u, S_ * 2, (g >> 1) * 1152, (g >> 1) * 1152 + PF2_ROWS, (g & 1) * 2048u, 16, 0, worker, 55, lane, dummy);
  }
  STAMP_END();
}

constexpr int K3_NS = 4;
#define G2_DEPTH 2
using CG2 = GC<3, 2, 2, 2, LIN, TR128, 8, K3_NS, 2, G2_DEPTH>;

typedef __attribute__((address_space(1))) unsigned gu32;
#define RLX_AGENT __ATOMIC_RELAXED, __HIP_MEMORY_SCOPE_AGENT
DI void wait_count(char* ws, int off, unsigned want) {
  gu32* w = (gu32*)(ws + OFF_SYNC + off); gu32* tmo = (gu32*)(ws + OFF_SYNC + 32640);
  for (unsigned spins = 0;; ++spins) {
    const unsigned v = (unsigned)__builtin_amdgcn_readfirstlane(__hip_atomic_load(w, RLX_AGENT));
    if (v >= want) break;
    if (v == 0) __builtin_amdgcn_s_sleep(SLEEP_SLOW); else __builtin_amdgcn_s_sleep(SLEEP_FAST);
    if (spins > (1u << 16)) { __hip_atomic_store(tmo, 1u, RLX_AGENT); break; }
    if ((spins & 255u) == 255u && __builtin_amdgcn_readfirstlane(__hip_atomic_load(tmo, RLX_AGENT))) break;
  }
  __builtin_amdgcn_fence(__ATOMIC_ACQUIRE, "wavefront");
}
DI u32x4 load_dev(const char* base, unsigned bytes, unsigned off) {
  return __builtin_amdgcn_raw_buffer_load_b128(__builtin_amdgcn_make_buffer_rsrc((void*)base, 0, bytes, 0x00020000), off, 0, 16);
}
DI void arrive(char* ws, int off, int lane) {
  asm volatile("s_waitcnt vmcnt(0)" ::: "memory");
  if (lane == 0) __hip_atomic_fetch_add((gu32*)(ws + OFF_SYNC + off), 1u, RLX_AGENT);
}
DI void store_wt(char* base, unsigned bytes, unsigned off, u32x4 v) {
  __builtin_amdgcn_raw_buffer_store_b128(v, __builtin_amdgcn_make_buffer_rsrc((void*)base, 0, bytes, 0x00020000), off, 0, 16);
}
constexpr int FIN_OS = 2336;
DI void irfft_wave(const char* rec, const char* y288, int w, int lane, char* orow, const char* kt) {
  const int row = lane & 31, hh = lane >> 5, k1 = row & 15, rr = 2 * w + (row >> 4);
  const bool ok = k1 < 10;
  const int k1c = ok ? k1 : 0;
  const char* pd = rec + rr * 4 + 32 * (k1c + 72 * hh);
  const char* pm = rec + rr * 4 + 32 * (576 - k1c - 72 * hh - 18 * 27);
  unsigned v[2][8];
#pragma unroll
  for (int j = 0; j < 8; ++j) {
    v[0][j] = *(const unsigned*)(pd + 576 * (8 * (j >> 2) + (j & 3)));
    v[1][j] = *(const unsigned*)(pm + 576 * (27 - (16 + 8 * (j >> 2) + (j & 3))));
  }
  const unsigned v288 = *(const u16*)(y288 + rr * 2);
  f32x4 tw[8];
  tw_load(tw, kt + 8192);
  if (k1 == 0 && hh == 0) { v[0][0] &= 0xffffu; v[1][0] = v288; }
  f32x16 re = {}, im = {};
#pragma unroll
  for (int u = 0; u < 2; ++u) {
    u32x4 a_r, a_i;
#pragma unroll
    for (int e = 0; e < 4; ++e) {
      a_r[e] = ok ? __builtin_amdgcn_perm(v[u][2 * e + 1], v[u][2 * e], 0x05040100u) : 0u;
      a_i[e] = ok ? (__builtin_amdgcn_perm(v[u][2 * e + 1], v[u][2 * e], 0x07060302u) ^ (u ? 0x80008000u : 0u)) : 0u;
    }
    cm_dAc(re, im, a_r, a_i, ef_load(kt + 2048 + u * 3072));
  }
  const u32x4 cr = *(const u32x4*)kt, ci = *(const u32x4*)(kt + 1024);
  tw_apply(re, im, tw);
#pragma unroll
  for (int u = 0; u < 2; ++u) {
    f32x16 y = {};
    y = mfma32<true>(cr, pack8_f16(re, u), y);
    y = mfma32<true>(ci, pack8_f16(im, u), y);
    float* o = (float*)(orow + u * FIN_OS) + (lane & 31) + 128 * hh;
#pragma unroll
    for (int rho = 0; rho < 8; ++rho) o[32 * ((rho & 3) + 8 * (rho >> 2))] = y[rho];
    if (hh == 0) { o[32 * 16] = y[8]; o[32 * 17] = y[9]; }
  }
}

__global__ __launch_bounds__(768, 3) void k_g2(const char* __restrict__ w2, const u16* __restrict__ x, const u16* __restrict__ b2,
                                               char* __restrict__ ws, u16* __restrict__ out) {
  extern __shared__ __attribute__((aligned(16))) char smem[];
  const int lane = threadIdx.x & 63, wave = __builtin_amdgcn_readfirstlane(threadIdx.x >> 6);
  const int g = blockIdx.x & 7, idx = blockIdx.x >> 3;
  STAMP_BEGIN(2)
  if (idx < 24) {
    const int ks = g >> 1, dt = idx % 3, st = (g & 1) * 8 + idx / 3;
    if (wave >= 4) {
      constexpr unsigned ld = S_ * 2;
      CG2::load(smem, wave - 4, 18, ws + OFF_U, 5308416u, lane * 16, w2, 18874368u, CG2::voff<TR128>(lane, ld),
                [&](int kt, int a) -> unsigned {
                  return a < 24 ? (unsigned)((dt * 6 + (a >> 2)) * 288 + (ks * 18 + kt) * 4 + (a & 3)) * 1024u
                                : (unsigned)((ks * 18 + kt) * 64 + 4 * (a - 24)) * ld + st * 256u;
                }, NTF2, [] {});
      if (wave == 11) wait_count(ws, ((st * 4 + ks) * 3 + dt) * 128, 18u);
      BAR();
      return;
    }
    f32x16 acc[3][2] = {};
    int rslot = 0;
    CG2::consume<false>(smem, wave, lane, 18, rslot, acc);
    const int wa = wave >> 1, wb = wave & 1;
    const bool own0 = wb * 2 == ks, own1 = wb * 2 + 1 == ks;
#pragma unroll
    for (int j = 0; j < 2; ++j) {
      if (j ? own1 : own0) continue;
#pragma unroll
      for (int i = 0; i < 3; ++i) {
        const unsigned o = (unsigned)((ks * 64 + st * 4 + wb * 2 + j) * 18 + dt * 6 + wa * 3 + i) * 2048u + lane * 16;
#pragma unroll
        for (int u = 0; u < 2; ++u) {
          u32x4 val;
#pragma unroll
          for (int e = 0; e < 4; ++e) val[e] = pack_bf16(acc[i][j][8 * u + 2 * e], acc[i][j][8 * u + 2 * e + 1]);
          store_wt(ws + OFF_SLAB, 9437184u, o + u * 1024, val);
        }
      }
    }
    asm volatile("s_waitcnt vmcnt(0)" ::: "memory");
    if (lane < 2 && !(lane ? own1 : own0))
      __hip_atomic_fetch_add((gu32*)(ws + OFF_SYNC + ((st * 4 + wb * 2 + lane) * 3 + dt) * 128), 1u, RLX_AGENT);
    BAR();
    if (!own0 && !own1) return;
    const int sf = st * 4 + ks, s = sf * 32 + (lane & 31);
    auto finish = [&](auto J) {
      constexpr int j = decltype(J)::value;
      u32x4 pv[3][2][4];
#pragma unroll
      for (int i = 0; i < 3; ++i)
#pragma unroll
        for (int u = 0; u < 2; ++u) {
          const unsigned o = (unsigned)(sf * 18 + dt * 6 + wa * 3 + i) * 2048u + u * 1024 + lane * 16;
#pragma unroll
          for (int q = 0; q < 3; ++q) pv[i][u][q] = load_dev(ws + OFF_SLAB, 9437184u, o + ((ks + 1 + q) & 3) * 2359296u);
          pv[i][u][3] = load_dev(ws + OFF_R, 2359296u, o);
        }
#pragma unroll
      for (int i = 0; i < 3; ++i)
#pragma unroll
        for (int u = 0; u < 2; ++u) {
          u32x4 o;
#pragma unroll
          for (int e = 0; e < 4; ++e)
            o[e] = pack_bf16(acc[i][j][8 * u + 2 * e] + (lo16(pv[i][u][0][e]) + lo16(pv[i][u][1][e])) + (lo16(pv[i][u][2][e]) + lo16(pv[i][u][3][e])),
                             acc[i][j][8 * u + 2 * e + 1] + (hi16(pv[i][u][0][e]) + hi16(pv[i][u][1][e])) + (hi16(pv[i][u][2][e]) + hi16(pv[i][u][3][e])));
          *(u32x4*)(out + (size_t)s * D_ + (dt * 6 + wa * 3 + i) * 32 + (lane >> 5) * 16 + u * 8) = o;
        }
    };
    if (own0) finish(IC<0>{}); else finish(IC<1>{});
  } else {
    const int me = (idx - 24) * 8 + g;
    const unsigned ldsb = (unsigned)(size_t)(LAS char*)smem;
    const unsigned E = wmax_exp(ws, lane);
    if (PF2_END > PF2_ROWS)
      warm_rows<NT3>(w2, 18874368u, S_ * 2, (g >> 1) * 1152 + PF2_ROWS, (g >> 1) * 1152 + PF2_END, (g & 1) * 2048u, 16, 0,
                     (idx - 24) * 12 + wave, 96, lane, ldsb + 131072 + wave * 256);
    char* rec = smem; char* kt = smem + 36864 + 1024; char* yb = kt + 17408;
    {
      const u32x4 r = mk_rsrc(ws + OFF_Y1, 2359296u);
#pragma unroll
      for (int it = 0; it < 3; ++it) dma16(r, ldsb + (it * 12 + wave) * 1024, lane * 16, me * 36864u + (it * 12 + wave) * 1024u);
      const u32x4 r2 = mk_rsrc(ws + OFF_RT, 16384u + 4096u);
      dma16(r2, ldsb + 37888 + wave * 1024, lane * 16, wave * 1024u);
      if (wave < 4) dma16(r2, ldsb + 37888 + (12 + wave) * 1024, lane * 16, (12 + wave) * 1024u);
      if (wave == 4) dma16(r2, ldsb + 37888 + 16384, (lane & 3) * 16, 16384u + me * 64u);
    }
    u32x4 xv[3];
    const int sr = lane & 31;
#pragma unroll
    for (int it = 0; it < 3; ++it) xv[it] = *(const u32x4*)(x + (size_t)(me * 32 + sr) * D_ + ((it * 12 + wave) * 2 + (lane >> 5)) * 8);
    const float bias = lo16(b2[me * 32 + sr]);
    const float fs = __uint_as_float((E + 1u) << 23) * (1.0f / 9.0f);
    WAIT_VM(0);
    BAR();
    for (int task = wave; task < 16; task += 12)
      irfft_wave(rec + (task >> 2) * 9216, kt + 16384 + (task >> 2) * 16, task & 3, lane, yb + task * 2 * FIN_OS, kt + lane * 16);
    WAIT_LGKM0();
    BAR();
#pragma unroll
    for (int it = 0; it < 3; ++it) {
      const int d2 = it * 12 + wave, dp = d2 * 2 + (lane >> 5);
      const char* yp = yb + sr * FIN_OS + dp * 32;
      const f32x4 y0 = *(const f32x4*)yp, y1 = *(const f32x4*)(yp + 16);
      u32x4 o;
#pragma unroll
      for (int e = 0; e < 4; ++e) {
        const float ya = e < 2 ? y0[2 * e] : y1[2 * e - 4], yc = e < 2 ? y0[2 * e + 1] : y1[2 * e - 3];
        o[e] = pack_bf16(lo16(xv[it][e]) + bias + ya * fs, hi16(xv[it][e]) + bias + yc * fs);
      }
      store_wt(ws + OFF_R, 2359296u, (unsigned)(me * 18 + (dp >> 2)) * 2048u + (dp & 1) * 1024 + (((dp >> 1) & 1) * 32 + sr) * 16, o);
    }
    asm volatile("s_waitcnt vmcnt(0)" ::: "memory");
    if (lane < 3) __hip_atomic_fetch_add((gu32*)(ws + OFF_SYNC + (me * 3 + lane) * 128), 1u, RLX_AGENT);
  }
  STAMP_END();
}

extern "C" void kernel_launch(void* const* d_in, const int* in_sizes, int n_in, void* d_out, int out_size, void* d_ws, size_t ws_size,
                              hipStream_t stream) {
  const u16* x = (const u16*)d_in[0];
  const u16* wr = (const u16*)d_in[1];
  const u16* wi = (const u16*)d_in[2];
  const char* w1 = (const char*)d_in[4];
  const u16* b1 = (const u16*)d_in[5];
  const char* w2 = (const char*)d_in[6];
  const u16* b2 = (const u16*)d_in[7];
  char* ws = (char*)d_ws;
  (void)hipFuncSetAttribute((const void*)k_g1, hipFuncAttributeMaxDynamicSharedMemorySize, CG1::LDS_BYTES);
  (void)hipFuncSetAttribute((const void*)k_g2, hipFuncAttributeMaxDynamicSharedMemorySize, CG2::LDS_BYTES);
  k_prep<<<528 + PF1_BLOCKS, 256, 0, stream>>>(x, wr, wi, w1, ws);
  k_g1<<<256, 704, CG1::LDS_BYTES, stream>>>(w1, b1, w2, ws);
  k_g2<<<256, 768, CG2::LDS_BYTES, stream>>>(w2, x, b2, ws, (u16*)d_out);
}
```

```cpp
#define PF2T 3
#define NTF2 5
#include <hip/hip_runtime.h>
#include <hip/hip_bf16.h>
#include <stdint.h>

#define DI __device__ __forceinline__
#define LAS __attribute__((address_space(3)))
typedef unsigned short u16;
typedef unsigned long long u64;
typedef __attribute__((ext_vector_type(2))) float f32x2;
typedef __attribute__((ext_vector_type(4))) float f32x4;
typedef __attribute__((ext_vector_type(16))) float f32x16;
typedef __attribute__((ext_vector_type(2))) _Float16 f16x2;
typedef __attribute__((ext_vector_type(8))) _Float16 f16x8;
typedef __attribute__((ext_vector_type(2))) __bf16 bf16x2;
typedef __attribute__((ext_vector_type(8))) __bf16 bf16x8;
typedef __attribute__((ext_vector_type(4))) short s16x4;
typedef __attribute__((ext_vector_type(2))) unsigned u32x2;
typedef __attribute__((ext_vector_type(4))) unsigned u32x4;
typedef __attribute__((address_space(1))) unsigned gu32;
typedef __attribute__((address_space(1))) u64 gu64;
#define RLX_AGENT __ATOMIC_RELAXED, __HIP_MEMORY_SCOPE_AGENT

#define PF1_I 1
#define POLL_SLEEP 4

constexpr int S_ = 2048, D_ = 576, F_ = 4608, NC_ = 289;

constexpr unsigned OFF_H    = 0;
constexpr unsigned OFF_WT   = OFF_H + 2359296;
constexpr unsigned OFF_TAB  = OFF_WT + 2367488;
constexpr unsigned OFF_RT   = OFF_TAB + 65536;
constexpr unsigned OFF_Y288 = OFF_RT + 16384;
constexpr unsigned OFF_WMAX = OFF_Y288 + 4096;
constexpr unsigned OFF_X1   = OFF_WMAX + 4096;
constexpr unsigned OFF_Y1   = OFF_X1 + 2359296;
constexpr unsigned OFF_U    = OFF_Y1 + 2359296;
constexpr unsigned OFF_SLAB = OFF_U + 5308416;
constexpr unsigned OFF_R    = OFF_SLAB + 9437184;
constexpr unsigned OFF_SYNC = OFF_R + 2359296;
constexpr unsigned SY_EPOCH = OFF_SYNC, SY_TMO = OFF_SYNC + 256, SY_H = OFF_SYNC + 2048, SY_A = OFF_SYNC + 4096, SY_U = OFF_SYNC + 6144,
                   SY_Y = OFF_SYNC + 8192, SY_T = OFF_SYNC + 10240;
constexpr unsigned WS_END   = SY_T + 192 * 64;
constexpr int TAB_E1 = 0, TAB_EO = 6144, TAB_EOI = 12288, TAB_TW1 = 18432, TAB_TW2 = 34816, TAB_BYTES = 51200;

DI unsigned pack_bf16(float a, float b) { f32x2 v = {a, b}; return __builtin_bit_cast(unsigned, __builtin_convertvector(v, bf16x2)); }
DI unsigned pack_f16(float a, float b) { f32x2 v = {a, b}; return __builtin_bit_cast(unsigned, __builtin_convertvector(v, f16x2)); }
DI float lo16(unsigned p) { return __uint_as_float(p << 16); }
DI float hi16(unsigned p) { return __uint_as_float(p & 0xffff0000u); }
DI float rbf(float a) { return lo16(pack_bf16(a, a)); }

template <int CTRL> DI float dpp_add(float v) {
  return v + __int_as_float(__builtin_amdgcn_update_dpp(0, __float_as_int(v), CTRL, 0xf, 0xf, true));
}
DI float sum32(float v) {
  v = dpp_add<0xB1>(v); v = dpp_add<0x4E>(v); v = dpp_add<0x141>(v); v = dpp_add<0x140>(v);
  auto r = __builtin_amdgcn_permlane16_swap(__float_as_uint(v), __float_as_uint(v), false, false);
  return __uint_as_float(r[0]) + __uint_as_float(r[1]);
}

#define DBG(i)
#define WAIT_VM(n) asm volatile("s_waitcnt vmcnt(%0)" ::"n"(n) : "memory")
#define WAIT_LGKM0() asm volatile("s_waitcnt lgkmcnt(0)" ::: "memory")
#define BAR() do { asm volatile("" ::: "memory"); __builtin_amdgcn_s_barrier(); asm volatile("" ::: "memory"); } while (0)
#define SCHED0() __builtin_amdgcn_sched_barrier(0)

DI u32x4 mk_rsrc(const void* p, unsigned bytes) {
  const u64 a = (u64)p;
  return u32x4{(unsigned)a, (unsigned)(a >> 32) & 0xffffu, bytes, 0x00020000u};
}
template <int POL> DI void dma16(u32x4 rsrc, unsigned lds_off, unsigned voff, unsigned soff) {
  if constexpr (POL == 0) asm volatile("s_mov_b32 m0, %0\n\ts_nop 0\n\tbuffer_load_dwordx4 %1, %2, %3 offen lds" ::"s"(lds_off), "v"(voff), "s"(rsrc), "s"(soff) : "memory");
  if constexpr (POL == 1) asm volatile("s_mov_b32 m0, %0\n\ts_nop 0\n\tbuffer_load_dwordx4 %1, %2, %3 offen nt lds" ::"s"(lds_off), "v"(voff), "s"(rsrc), "s"(soff) : "memory");
  if constexpr (POL == 2) asm volatile("s_mov_b32 m0, %0\n\ts_nop 0\n\tbuffer_load_dwordx4 %1, %2, %3 offen sc1 lds" ::"s"(lds_off), "v"(voff), "s"(rsrc), "s"(soff) : "memory");
}
DI void dma4(u32x4 rsrc, unsigned lds_off, unsigned voff) {
  asm volatile("s_mov_b32 m0, %0\n\ts_nop 0\n\tbuffer_load_dword %1, %2, 0 offen lds" ::"s"(lds_off), "v"(voff), "s"(rsrc) : "memory");
}
DI void store_dev(char* ws, unsigned off, u32x4 v) {
  __builtin_amdgcn_raw_buffer_store_b128(v, __builtin_amdgcn_make_buffer_rsrc((void*)ws, 0, WS_END, 0x00020000), off, 0, 16);
}
DI u32x4 load_dev(const char* ws, unsigned off) {
  return __builtin_amdgcn_raw_buffer_load_b128(__builtin_amdgcn_make_buffer_rsrc((void*)ws, 0, WS_END, 0x00020000), off, 0, 16);
}
DI void set_flag(char* ws, unsigned off, u64 tag) { __hip_atomic_store((gu64*)(ws + off), tag, RLX_AGENT); }
template <int N> DI void poll_flags(char* ws, unsigned off, bool active, u64 tag) {
  for (unsigned spins = 0;; ++spins) {
    bool ok = true;
#pragma unroll
    for (int i = 0; i < N; ++i) { const u64 v = __hip_atomic_load((gu64*)(ws + off + i * 512), RLX_AGENT); ok &= (v == tag) || !active; }
    if (__all(ok)) break;
    __builtin_amdgcn_s_sleep(POLL_SLEEP);
    if (spins > (1u << 17)) { set_flag(ws, SY_TMO, tag); break; }
    if ((spins & 63u) == 63u && __all(__hip_atomic_load((gu64*)(ws + SY_TMO), RLX_AGENT) == tag)) break;
  }
  __builtin_amdgcn_fence(__ATOMIC_ACQUIRE, "wavefront");
}
DI unsigned w1_line(int i, int total, int r0, int nl, unsigned c0) {
  const int rr = i / nl, l = i - rr * nl, row = r0 + rr;
  return i < total ? (unsigned)(64 * (row & 31) + (row >> 5)) * (F_ * 2) + c0 + (unsigned)l * 128u : 0xfffffff0u;
}

template <bool F16> DI f32x16 mfma32(u32x4 a, u32x4 b, f32x16 c) {
  if constexpr (F16) return __builtin_amdgcn_mfma_f32_32x32x16_f16(__builtin_bit_cast(f16x8, a), __builtin_bit_cast(f16x8, b), c, 0, 0, 0);
  else return __builtin_amdgcn_mfma_f32_32x32x16_bf16(__builtin_bit_cast(bf16x8, a), __builtin_bit_cast(bf16x8, b), c, 0, 0, 0);
}
DI u32x4 pack8_f16(const f32x16& v, int u) {
  return u32x4{pack_f16(v[8 * u], v[8 * u + 1]), pack_f16(v[8 * u + 2], v[8 * u + 3]), pack_f16(v[8 * u + 4], v[8 * u + 5]), pack_f16(v[8 * u + 6], v[8 * u + 7])};
}
DI u32x4 tr_frag(const char* p0, const char* p1) {
  s16x4 lo = __builtin_amdgcn_ds_read_tr16_b64_v4i16((LAS s16x4*)p0);
  s16x4 hi = __builtin_amdgcn_ds_read_tr16_b64_v4i16((LAS s16x4*)p1);
  u32x2 a = __builtin_bit_cast(u32x2, lo), c = __builtin_bit_cast(u32x2, hi);
  return u32x4{a[0], a[1], c[0], c[1]};
}
DI void tw_piece(f32x16& re, f32x16& im, int p, const f32x4 w) {
  const f32x2 r = {re[2 * p], re[2 * p + 1]}, i = {im[2 * p], im[2 * p + 1]}, wr = {w[0], w[1]}, wi = {w[2], w[3]};
  const f32x2 a = __builtin_elementwise_fma(r, wr, -(i * wi)), b = __builtin_elementwise_fma(r, wi, i * wr);
  re[2 * p] = a[0]; re[2 * p + 1] = a[1]; im[2 * p] = b[0]; im[2 * p + 1] = b[1];
}
DI void tw_load(f32x4 (&t)[8], const char* tw) {
#pragma unroll
  for (int p = 0; p < 8; ++p) t[p] = *(const f32x4*)(tw + p * 1024);
}
DI void tw_apply(f32x16& re, f32x16& im, const f32x4 (&t)[8]) {
#pragma unroll
  for (int p = 0; p < 8; ++p) tw_piece(re, im, p, t[p]);
}
DI float cs(int which, float rev) {
  return which == 0 ? __builtin_amdgcn_cosf(rev) : (which == 1 ? -__builtin_amdgcn_sinf(rev) : __builtin_amdgcn_sinf(rev));
}
DI u32x4 e32_frag(int frag, int lane) {
  const int t = frag / 3, which = frag - 3 * t, r = lane & 31, h = lane >> 5;
  float o[8];
#pragma unroll
  for (int j = 0; j < 8; ++j) o[j] = cs(which, (float)(((16 * t + 8 * (j >> 2) + 4 * h + (j & 3)) * r) & 31) * (1.0f / 32.0f));
  return u32x4{pack_f16(o[0], o[1]), pack_f16(o[2], o[3]), pack_f16(o[4], o[5]), pack_f16(o[6], o[7])};
}

enum { LIN = 0, TR128 = 1, TR64 = 2 };

template <int V> struct IC { static constexpr int value = V; };
template <int NA_W, int NB_W, int WA, int WB, int AK, int BK, int NS, int DPM, int DEPTH>
struct GC {
  static constexpr int NC = WA * WB, NFA = NA_W * WA, NFB = NB_W * WB;
  static constexpr int A_BYTES = NFA * 4096, B_BYTES = NFB * 4096, STAGE = A_BYTES + B_BYTES;
  static constexpr int LDS_BYTES = NS * STAGE;
  static_assert(AK == LIN || NFA == (AK == TR128 ? 4 : 2), "");
  static_assert(BK == LIN || NFB == (BK == TR128 ? 4 : 2), "");

  template <int K> static DI unsigned voff(int lane, unsigned rstride) {
    if constexpr (K == LIN) return lane * 16;
    else if constexpr (K == TR128) return (unsigned)(lane >> 4) * rstride + (unsigned)(((lane & 15) ^ ((lane >> 4) << 2)) * 16);
    else return (unsigned)(lane >> 3) * rstride + (unsigned)(((lane & 7) ^ (((lane >> 4) & 1) << 2)) * 16);
  }
  template <int K, int NF> static DI void frag_addr(unsigned (&addr)[K == LIN ? 1 : NF], int opoff, int wpos, int lane) {
    if constexpr (K == LIN) addr[0] = opoff + wpos * NF * 4096 + lane * 16;
    else {
      const int h = lane >> 5, blk = (lane >> 4) & 1, q = (lane & 15) >> 2, p = lane & 3;
#pragma unroll
      for (int i = 0; i < NF; ++i) {
        const int ch = 4 * (wpos * NF + i) + 2 * blk + (p >> 1);
        if constexpr (K == TR128) addr[i] = opoff + (4 * h + q) * 256 + ((ch ^ (q << 2)) * 16) + 8 * (p & 1);
        else addr[i] = opoff + (4 * h + q) * 128 + ((ch ^ (((q >> 1) & 1) << 2)) * 16) + 8 * (p & 1);
      }
    }
  }
  template <int K, int NF> static DI void rd(u32x4 (&f)[NF], const char* smem, const unsigned (&addr)[K == LIN ? 1 : NF], int sb, int hh) {
#pragma unroll
    for (int i = 0; i < NF; ++i) {
      if constexpr (K == LIN) f[i] = *(const u32x4*)(smem + addr[0] + sb + (i * 4 + hh) * 1024);
      else {
        constexpr int RB = K == TR128 ? 256 : 128;
        const char* p = smem + addr[i] + sb + hh * 16 * RB;
        s16x4 lo = __builtin_amdgcn_ds_read_tr16_b64_v4i16((LAS s16x4*)p);
        s16x4 hi = __builtin_amdgcn_ds_read_tr16_b64_v4i16((LAS s16x4*)(p + 8 * RB));
        u32x2 a = __builtin_bit_cast(u32x2, lo), c = __builtin_bit_cast(u32x2, hi);
        f[i] = u32x4{a[0], a[1], c[0], c[1]};
      }
    }
  }
  static DI void ilv() {
    constexpr int NR = NA_W * (AK == LIN ? 1 : 2) + NB_W * (BK == LIN ? 1 : 2), NM = NA_W * NB_W;
    constexpr int NG = (NR + DPM - 1) / DPM < NM ? (NR + DPM - 1) / DPM : NM;
#pragma unroll
    for (int i = 0; i < NG; ++i) {
      __builtin_amdgcn_sched_group_barrier(0x8, 1, 0);
      if (i + 1 < NG) __builtin_amdgcn_sched_group_barrier(0x100, DPM, 0);
      else __builtin_amdgcn_sched_group_barrier(0x100, NR - DPM * (NG - 1), 0);
    }
    if constexpr (NM > NG) __builtin_amdgcn_sched_group_barrier(0x8, NM - NG, 0);
    SCHED0();
  }
  template <int G, class ISSUE> static DI void ring(int nt, ISSUE issue) {
    int wslot = NS - 2;
    for (int tau = 0; tau < nt - (NS - 2); ++tau) {
      WAIT_VM((NS - 3) * G);
      BAR();
      issue(tau + NS - 2, wslot * STAGE);
      wslot = (wslot + 1 == NS) ? 0 : wslot + 1;
    }
    if constexpr (NS >= 5) { WAIT_VM(2 * G); BAR(); }
    if constexpr (NS >= 4) { WAIT_VM(G); BAR(); }
    WAIT_VM(0); BAR();
    static_assert(NS >= 3 && NS <= 5, "");
  }
  template <bool F16>
  static DI void consume(const char* smem, int wave, int lane, int n, int& rslot, f32x16 (&acc)[NA_W][NB_W]) {
    const int wa = wave / WB, wb = wave - wa * WB;
    unsigned addrA[AK == LIN ? 1 : NA_W], addrB[BK == LIN ? 1 : NB_W];
    frag_addr<AK, NA_W>(addrA, 0, wa, lane);
    frag_addr<BK, NB_W>(addrB, A_BYTES, wb, lane);
    u32x4 fa[DEPTH][NA_W], fb[DEPTH][NB_W];
#pragma unroll
    for (int d = 1; d < DEPTH; ++d) {
#pragma unroll
      for (int i = 0; i < NA_W; ++i) fa[d][i] = u32x4{0, 0, 0, 0};
#pragma unroll
      for (int i = 0; i < NB_W; ++i) fb[d][i] = u32x4{0, 0, 0, 0};
    }
    auto mm = [&](const u32x4 (&a)[NA_W], const u32x4 (&b)[NB_W]) {
#pragma unroll
      for (int i = 0; i < NA_W; ++i)
#pragma unroll
        for (int j = 0; j < NB_W; ++j) acc[i][j] = mfma32<F16>(a[i], b[j], acc[i][j]);
    };
    auto step = [&](auto Z0) {
      constexpr int z0 = decltype(Z0)::value;
      BAR();
      SCHED0();
      const int sb = rslot * STAGE;
#pragma unroll
      for (int hh = 0; hh < 4; ++hh) {
        rd<AK, NA_W>(fa[(z0 + hh) % DEPTH], smem, addrA, sb, hh);
        rd<BK, NB_W>(fb[(z0 + hh) % DEPTH], smem, addrB, sb, hh);
        mm(fa[(z0 + hh + 1) % DEPTH], fb[(z0 + hh + 1) % DEPTH]);
        ilv();
      }
      rslot = (rslot + 1 == NS) ? 0 : rslot + 1;
    };
    if constexpr (DEPTH == 2) {
      for (int tau = 0; tau < n; ++tau) step(IC<0>{});
      mm(fa[1], fb[1]);
    } else {
      int tau = 0;
      for (; tau + 3 <= n; tau += 3) { step(IC<0>{}); step(IC<1>{}); step(IC<2>{}); }
      if (n - tau == 2) { step(IC<0>{}); step(IC<1>{}); mm(fa[0], fb[0]); mm(fa[1], fb[1]); }
      else if (n - tau == 1) { step(IC<0>{}); mm(fa[2], fb[2]); mm(fa[0], fb[0]); }
      else { mm(fa[1], fb[1]); mm(fa[2], fb[2]); }
    }
  }
};

DI float gelu_tanh(float u) {
  const float z = u * (1.0f + 0.044715f * u * u) * (-2.0f * 0.7978845608028654f * 1.4426950408889634f);
  return u * __builtin_amdgcn_rcpf(1.0f + __builtin_amdgcn_exp2f(z));
}

template <int NV> DI void mv_ilv() {
#pragma unroll
  for (int i = 0; i < 8; ++i) { __builtin_amdgcn_sched_group_barrier(0x8, 1, 0); __builtin_amdgcn_sched_group_barrier(0x2, NV, 0); }
  SCHED0();
}
struct EF { u32x4 r, i, n; };
DI EF ef_load(const char* p) { EF e; e.r = *(const u32x4*)p; e.i = *(const u32x4*)(p + 1024); e.n = *(const u32x4*)(p + 2048); return e; }
#define MF(a, b, c) mfma32<true>(a, b, c)
DI void cm_dA(f32x16& re, f32x16& im, u32x4 dr, u32x4 di, const EF& e) { re = MF(dr, e.r, re); im = MF(dr, e.i, im); re = MF(di, e.n, re); im = MF(di, e.r, im); }
DI void cm_dB(f32x16& re, f32x16& im, u32x4 dr, u32x4 di, const EF& e) { re = MF(e.r, dr, re); im = MF(e.i, dr, im); re = MF(e.n, di, re); im = MF(e.r, di, im); }
DI void cm_dAc(f32x16& re, f32x16& im, u32x4 dr, u32x4 di, const EF& e) { re = MF(dr, e.r, re); im = MF(dr, e.n, im); re = MF(di, e.i, re); im = MF(di, e.r, im); }
DI void wmul(f32x16& re, f32x16& im, const u32x4 (&wv)[4], float wsc) {
#pragma unroll
  for (int p = 0; p < 8; ++p) {
    const unsigned w0 = wv[p >> 1][(2 * p) & 3], w1 = wv[p >> 1][(2 * p + 1) & 3];
    tw_piece(re, im, p, f32x4{lo16(w0), lo16(w1), hi16(w0), hi16(w1)} * wsc);
  }
}
DI void bfly(f32x16& a, f32x16& b) {
  const f32x16 x = a, y = b; a = x + y; b = x - y;
}
DI void colfft_wave(const char* tab  , const char* ws, int c, int lane, float wsc, const u32x4 (&zr)[2][2], const u32x4 (&zi)[2][2],
                    char* img, int slot_re, int slot_im) {
  f32x4 tw[8];
  f32x16 r0 = {}, i0 = {}, r1 = {}, i1 = {};
  u32x4 ar[2], ai[2];
  EF ea = ef_load(tab + TAB_E1), eb = ef_load(tab + TAB_E1 + 3072);
  tw_load(tw, tab + TAB_TW1);
  SCHED0();
  cm_dA(r0, i0, zr[0][0], zi[0][0], ea); cm_dA(r0, i0, zr[0][1], zi[0][1], eb);
  SCHED0();
  cm_dA(r1, i1, zr[1][0], zi[1][0], ea); cm_dA(r1, i1, zr[1][1], zi[1][1], eb);
#pragma unroll
  for (int p = 0; p < 8; ++p) { tw_piece(r0, i0, p, tw[p]); tw[p] = *(const f32x4*)(tab + TAB_TW1 + 8192 + p * 1024); }
  ar[0] = pack8_f16(r0, 0); ar[1] = pack8_f16(r0, 1); ai[0] = pack8_f16(i0, 0); ai[1] = pack8_f16(i0, 1);
  mv_ilv<6>();
  u32x4 wv0[4], wv1[4];
#pragma unroll
  for (int i = 0; i < 4; ++i) wv0[i] = load_dev(ws, OFF_WT + c * 8192u + i * 1024 + lane * 16);
  r0 = f32x16{}; i0 = f32x16{};
  cm_dB(r0, i0, ar[0], ai[0], ea); ea = ef_load(tab + TAB_EO);
  cm_dB(r0, i0, ar[1], ai[1], eb); eb = ef_load(tab + TAB_EO + 3072);
  tw_apply(r1, i1, tw);
  ar[0] = pack8_f16(r1, 0); ar[1] = pack8_f16(r1, 1); ai[0] = pack8_f16(i1, 0); ai[1] = pack8_f16(i1, 1);
  mv_ilv<6>();
#pragma unroll
  for (int i = 0; i < 4; ++i) wv1[i] = load_dev(ws, OFF_WT + c * 8192u + (4 + i) * 1024 + lane * 16);
  r1 = f32x16{}; i1 = f32x16{};
  cm_dB(r1, i1, ar[0], ai[0], ea); ea = ef_load(tab + TAB_E1);
  cm_dB(r1, i1, ar[1], ai[1], eb); eb = ef_load(tab + TAB_E1 + 3072);
  SCHED0();
  bfly(r0, r1); bfly(i0, i1);
  wmul(r0, i0, wv0, wsc);
  wmul(r1, i1, wv1, wsc);
  bfly(r0, r1); bfly(i0, i1);
  u32x4 dr[2], di[2];
  ar[0] = pack8_f16(r0, 0); ar[1] = pack8_f16(r0, 1); ai[0] = pack8_f16(i0, 0); ai[1] = pack8_f16(i0, 1);
  SCHED0();
  tw_load(tw, tab + TAB_TW2);
  r0 = f32x16{}; i0 = f32x16{};
  cm_dAc(r0, i0, ar[0], ai[0], ea); ea = ef_load(tab + TAB_EOI);
  cm_dAc(r0, i0, ar[1], ai[1], eb); eb = ef_load(tab + TAB_EOI + 3072);
  dr[0] = pack8_f16(r1, 0); dr[1] = pack8_f16(r1, 1); di[0] = pack8_f16(i1, 0); di[1] = pack8_f16(i1, 1);
  mv_ilv<2>();
  r1 = f32x16{}; i1 = f32x16{};
  cm_dAc(r1, i1, dr[0], di[0], ea); ea = ef_load(tab + TAB_E1);
  cm_dAc(r1, i1, dr[1], di[1], eb); eb = ef_load(tab + TAB_E1 + 3072);
#pragma unroll
  for (int p = 0; p < 8; ++p) { tw_piece(r0, i0, p, tw[p]); tw[p] = *(const f32x4*)(tab + TAB_TW2 + 8192 + p * 1024); }
  ar[0] = pack8_f16(r0, 0); ar[1] = pack8_f16(r0, 1); ai[0] = pack8_f16(i0, 0); ai[1] = pack8_f16(i0, 1);
  mv_ilv<6>();
  r0 = f32x16{}; i0 = f32x16{};
  cm_dAc(r0, i0, ar[0], ai[0], ea); cm_dAc(r0, i0, ar[1], ai[1], eb);
  tw_apply(r1, i1, tw);
  dr[0] = pack8_f16(r1, 0); dr[1] = pack8_f16(r1, 1); di[0] = pack8_f16(i1, 0); di[1] = pack8_f16(i1, 1);
  mv_ilv<6>();
  r1 = f32x16{}; i1 = f32x16{};
  cm_dAc(r1, i1, dr[0], di[0], ea); cm_dAc(r1, i1, dr[1], di[1], eb);
  SCHED0();
#pragma unroll
  for (int g = 0; g < 4; ++g) {
    const int Q = (lane & 31) * 8 + 2 * g + (lane >> 5), sw = lane & 15, r = 4 * g;
    if (slot_im == -2) {
      *(u32x4*)(img + Q * 16) = u32x4{pack_f16(r0[r], r1[r]), pack_f16(r0[r + 1], r1[r + 1]), pack_f16(r0[r + 2], r1[r + 2]), pack_f16(r0[r + 3], r1[r + 3])};
      continue;
    }
#pragma unroll
    for (int z = 0; z < 2; ++z) {
      const int q = r + 2 * z;
      *(u32x4*)(img + Q * 256 + (((slot_re + z) ^ sw) * 16)) =
          u32x4{pack_f16(r0[q], i0[q]), pack_f16(r1[q], i1[q]), pack_f16(r0[q + 1], i0[q + 1]), pack_f16(r1[q + 1], i1[q + 1])};
    }
  }
}
#undef MF
constexpr int FIN_OS = 2336;
DI void irfft_wave(const char* rec, const char* y288, int w, int lane, char* orow, const char* kt) {
  const int row = lane & 31, hh = lane >> 5, k1 = row & 15, rr = 2 * w + (row >> 4);
  const bool ok = k1 < 10;
  const int k1c = ok ? k1 : 0;
  const char* pd = rec + rr * 4 + 32 * (k1c + 72 * hh);
  const char* pm = rec + rr * 4 + 32 * (576 - k1c - 72 * hh - 18 * 27);
  unsigned v[2][8];
#pragma unroll
  for (int j = 0; j < 8; ++j) {
    v[0][j] = *(const unsigned*)(pd + 576 * (8 * (j >> 2) + (j & 3)));
    v[1][j] = *(const unsigned*)(pm + 576 * (27 - (16 + 8 * (j >> 2) + (j & 3))));
  }
  const unsigned v288 = *(const u16*)(y288 + rr * 2);
  f32x4 tw[8];
  tw_load(tw, kt + 8192);
  if (k1 == 0 && hh == 0) { v[0][0] &= 0xffffu; v[1][0] = v288; }
  f32x16 re = {}, im = {};
#pragma unroll
  for (int u = 0; u < 2; ++u) {
    u32x4 a_r, a_i;
#pragma unroll
    for (int e = 0; e < 4; ++e) {
      a_r[e] = ok ? __builtin_amdgcn_perm(v[u][2 * e + 1], v[u][2 * e], 0x05040100u) : 0u;
      a_i[e] = ok ? (__builtin_amdgcn_perm(v[u][2 * e + 1], v[u][2 * e], 0x07060302u) ^ (u ? 0x80008000u : 0u)) : 0u;
    }
    cm_dAc(re, im, a_r, a_i, ef_load(kt + 2048 + u * 3072));
  }
  const u32x4 cr = *(const u32x4*)kt, ci = *(const u32x4*)(kt + 1024);
  tw_apply(re, im, tw);
#pragma unroll
  for (int u = 0; u < 2; ++u) {
    f32x16 y = {};
    y = mfma32<true>(cr, pack8_f16(re, u), y);
    y = mfma32<true>(ci, pack8_f16(im, u), y);
    float* o = (float*)(orow + u * FIN_OS) + (lane & 31) + 128 * hh;
#pragma unroll
    for (int rho = 0; rho < 8; ++rho) o[32 * ((rho & 3) + 8 * (rho >> 2))] = y[rho];
    if (hh == 0) { o[32 * 16] = y[8]; o[32 * 17] = y[9]; }
  }
}

DI unsigned wmax_exp(const char* ws, int lane) {
  const u32x4 mv = load_dev(ws, OFF_WMAX + lane * 16);
  unsigned mx = max(max(mv[0], mv[1]), max(mv[2], mv[3]));
#pragma unroll
  for (int o = 32; o; o >>= 1) mx = max(mx, (unsigned)__shfl_xor((int)mx, o));
  return min(mx >> 7, 252u);
}

using CG1 = GC<1, 3, 4, 1, TR128, LIN, 5, 2, 3>;
using CG2 = GC<3, 2, 2, 2, LIN, TR128, 4, 2, 2>;
constexpr int P1_RS = 1216, P1_XS = 3 * CG1::STAGE, P1_HS = P1_XS + 8 * P1_RS, P1_C1 = P1_XS + 16 * P1_RS, P1_E = P1_C1 + 6144,
              P1_TW = P1_E + 6144, P1_O = P1_TW + 8192, P1_WT = P1_O + 9216, P1_RED = P1_WT + 10512, P1_DUMMY = 147456;
static_assert(P1_RED + 16 <= P1_DUMMY && P1_DUMMY >= CG1::LDS_BYTES, "");

__global__ __launch_bounds__(768, 3) void k_all(const u16* __restrict__ x, const u16* __restrict__ wr, const u16* __restrict__ wi,
                                                const char* __restrict__ w1, const u16* __restrict__ b1, const char* __restrict__ w2,
                                                const u16* __restrict__ b2, char* __restrict__ ws, u16* __restrict__ out) {
  extern __shared__ __attribute__((aligned(16))) char smem[];
  const int lane = threadIdx.x & 63, wave = __builtin_amdgcn_readfirstlane(threadIdx.x >> 6);
  const int b = blockIdx.x, g = b & 7, idx = b >> 3;
  const unsigned ldsb = (unsigned)(size_t)(LAS char*)smem;
  const u32x4 rs_ws = mk_rsrc(ws, WS_END), rs_w1 = mk_rsrc(w1, 18874368u), rs_w2 = mk_rsrc(w2, 18874368u);
  if (wave == 0) DBG(0);
  const int tile = g * 27 + idx, ft = tile / 6, dt1 = tile - ft * 6;
  const int ft0 = (g * 27) / 6, nl1 = ((g * 27 + 26) / 6 - ft0 + 1) * 2;
  constexpr int PF1_ROWS = (PF1_I * 128 * 64) / 10;
  auto issue_w1 = [&](int kt, int so) {
    constexpr unsigned ld = F_ * 2;
    const unsigned vo = CG1::voff<TR128>(lane, 64 * ld);
#pragma unroll
    for (int m = 0; m < 4; ++m) {
      const int a = (wave - 4) * 4 + m;
      dma16<0>(rs_w1, ldsb + so + a * 1024, vo, (unsigned)(((4 * a) & 31) * 64 + 2 * kt + (a >> 3)) * ld + ft * 256u);
    }
  };
  u64 tag;
  if (wave < 4) {
    const int tid = threadIdx.x, s2 = b >> 2, t = (b >> 1) & 1, h = b & 1;
    const int rr = tid >> 5, l32 = tid & 31;
    const int s1 = 16 * t + 8 * (rr >> 2) + 4 * h + (rr & 3);
    const unsigned* xr = (const unsigned*)(x + (size_t)(64 * s1 + s2) * D_);
    unsigned raw[9];
#pragma unroll
    for (int i = 0; i < 9; ++i) raw[i] = xr[32 * i + l32];
    const u64 ep = __hip_atomic_load((gu64*)(ws + SY_EPOCH), RLX_AGENT);
    float v[18];
#pragma unroll
    for (int i = 0; i < 9; ++i) { v[2 * i] = lo16(raw[i]); v[2 * i + 1] = hi16(raw[i]); }
#pragma unroll
    for (int pass = 0; pass < 2; ++pass) {
      float sm = 0.f;
#pragma unroll
      for (int i = 0; i < 18; ++i) sm += v[i];
      const float mean = sum32(sm) * (1.0f / 576.0f);
      float sq = 0.f;
#pragma unroll
      for (int i = 0; i < 18; ++i) { float dlt = v[i] - mean; sq += dlt * dlt; }
      const float var = sum32(sq) * (1.0f / 576.0f);
      const float mb = rbf(mean);
      const float sd = rbf(__builtin_sqrtf(rbf(rbf(var) + 1e-5f)));
      const float rs = 1.0f / sd;
      char* dst = smem + P1_XS + pass * (8 * P1_RS) + rr * P1_RS + l32 * 4;
#pragma unroll
      for (int i = 0; i < 9; ++i) {
        unsigned p = pack_bf16(v[2 * i] - mb, v[2 * i + 1] - mb);
        p = pack_bf16(lo16(p) * rs, hi16(p) * rs);
        v[2 * i] = lo16(p); v[2 * i + 1] = hi16(p);
        *(unsigned*)(dst + i * 128) = pass == 0 ? pack_f16(v[2 * i], v[2 * i + 1]) : p;
      }
    }
    tag = ((u64)__builtin_amdgcn_readfirstlane((unsigned)(ep >> 32)) << 32 | (unsigned)__builtin_amdgcn_readfirstlane((unsigned)ep)) *
              6364136223846793005ull + 1442695040888963407ull;
    WAIT_LGKM0();
    BAR();
    for (int cb = wave; cb < 9; cb += 4) {
      const int gg = lane >> 4, q = (lane & 15) >> 2, pp = lane & 3;
      const char* src = smem + P1_HS + q * P1_RS + (cb * 64 + gg * 16 + pp * 4) * 2;
      const int df = cb * 2 + (gg >> 1), i = 8 * q + 4 * (gg & 1) + pp;
      store_dev(ws, OFF_H + (unsigned)(df * 128 + 2 * s2 + t) * 1024u + (h * 32 + i) * 16, tr_frag(src, src + 4 * P1_RS));
    }
    {
      const int hh = lane >> 5, blk = (lane >> 4) & 1, q = (lane & 15) >> 2, p = lane & 3;
      const char* xa = smem + P1_XS + (2 * wave) * P1_RS + blk * 32 + p * 8;
      const char* cf = smem + P1_C1 + lane * 16;
      f32x16 re = {}, im = {};
      f32x4 tw[8];
      tw_load(tw, smem + P1_TW + lane * 16);
#pragma unroll
      for (int ks = 0; ks < 3; ++ks) {
        const char* a0 = ks < 2 ? xa + ks * P1_RS + (4 * hh + q) * 64 : xa + (q >> 1) * P1_RS + (16 + (q & 1)) * 64;
        const u32x4 a = tr_frag(a0, ks < 2 ? a0 + 512 : a0);
        re = mfma32<true>(a, *(const u32x4*)(cf + (2 * ks) * 1024), re);
        im = mfma32<true>(a, *(const u32x4*)(cf + (2 * ks + 1) * 1024), im);
      }
      tw_apply(re, im, tw);
      f32x16 xr_ = {}, xi_ = {};
#pragma unroll
      for (int u = 0; u < 2; ++u) cm_dB(xr_, xi_, pack8_f16(re, u), pack8_f16(im, u), ef_load(smem + P1_E + u * 3072 + lane * 16));
      const int col = lane & 31, k1 = col & 15, row2 = 2 * (2 * wave + (col >> 4));
      if (k1 < 10) {
        char* o1 = smem + P1_O + 32 * k1 + 576 * 4 * hh + row2;
        char* o2 = smem + P1_O + 32 * (576 - k1) - 576 * (4 * hh + 27) + row2;
#pragma unroll
        for (int rho = 0; rho < 8; ++rho) {
          const unsigned pk = pack_f16(xr_[rho] * (1.0f / 32.0f), xi_[rho] * (1.0f / 32.0f));
          char* d = o1 + 576 * ((rho & 3) + 8 * (rho >> 2));
          *(u16*)d = (u16)pk;
          if (rho != 0 || k1 != 0 || hh != 0) *(u16*)(d + 16) = (u16)(pk >> 16);
        }
        if (k1 >= 1 && k1 <= 8) {
#pragma unroll
          for (int rho = 8; rho < 16; ++rho) {
            const unsigned pk = pack_f16(xr_[rho] * (1.0f / 32.0f), xi_[rho] * (-1.0f / 32.0f));
            char* d = o2 + 576 * (27 - ((rho & 3) + 8 * (rho >> 2)));
            *(u16*)d = (u16)pk;
            *(u16*)(d + 16) = (u16)(pk >> 16);
          }
        } else if (k1 == 0 && hh == 0) {
          *(u16*)(smem + P1_O + 16 + row2) = (u16)pack_f16(xr_[8] * (1.0f / 32.0f), 0.f);
        }
      }
    }
    WAIT_VM(0); WAIT_LGKM0();
    BAR();
    if (tid == 0) set_flag(ws, SY_H + b * 8, tag);
    if (wave == 0) DBG(1);
    const unsigned P = ((s2 & 1) * 2 + t) * 64 + h * 32 + (s2 >> 1);
#pragma unroll
    for (int it = 0; it < 3; ++it) {
      const int p = tid + it * 256;
      if (p < 576) store_dev(ws, OFF_X1 + P * 9216u + p * 16, *(const u32x4*)(smem + P1_O + p * 16));
    }
    WAIT_VM(0);
    BAR();
    if (tid == 0) set_flag(ws, SY_A + b * 8, tag);
  } else if (wave < 8) {
    const int tid = threadIdx.x - 256;
    const int rg = b >> 2, cg = b & 3, c0 = cg * 72 + (cg > 0), nc = cg == 0 ? 73 : 72, kq = rg >> 2, ko = rg & 3;
    unsigned rv[10], iv[10];
#pragma unroll
    for (int it = 0; it < 10; ++it) {
      const int i = tid + it * 256, row = i / 73, col = i - row * 73;
      const bool ok = i < 32 * 73 && col < nc;
      const int off = ok ? (8 * ko + (row & 7) + 32 * (4 * kq + (row >> 3))) * NC_ + c0 + col : 0;
      rv[it] = wr[off]; iv[it] = wi[off];
    }
    const u64 ep = __hip_atomic_load((gu64*)(ws + SY_EPOCH), RLX_AGENT);
    unsigned mx = 0;
#pragma unroll
    for (int it = 0; it < 10; ++it) {
      const int i = tid + it * 256, row = i / 73, col = i - row * 73;
      if (i < 32 * 73 && col < nc) {
        mx = max(mx, max(rv[it] & 0x7fffu, iv[it] & 0x7fffu));
        *(unsigned*)(smem + P1_WT + col * 144 + (row & 7) * 16 + (row >> 3) * 4) = rv[it] | (iv[it] << 16);
      }
    }
#pragma unroll
    for (int o = 32; o; o >>= 1) mx = max(mx, (unsigned)__shfl_xor((int)mx, o));
    unsigned* red = (unsigned*)(smem + P1_RED);
    if (lane == 0) red[wave - 4] = mx;
    tag = ((u64)__builtin_amdgcn_readfirstlane((unsigned)(ep >> 32)) << 32 | (unsigned)__builtin_amdgcn_readfirstlane((unsigned)ep)) *
              6364136223846793005ull + 1442695040888963407ull;
    if (idx < 27) { issue_w1(0, 0); issue_w1(1, CG1::STAGE); issue_w1(2, 2 * CG1::STAGE); }
    WAIT_LGKM0();
    BAR();
    if (tid == 0) __hip_atomic_store((gu32*)(ws + OFF_WMAX + b * 4), max(max(red[0], red[1]), max(red[2], red[3])), RLX_AGENT);
    const int U = kq >> 3, h = kq & 1, rq = (kq >> 1) & 3;
#pragma unroll
    for (int it = 0; it < 3; ++it) {
      const int p = tid + it * 256;
      if (p < nc * 8) {
        const int col = p >> 3, a = p & 7;
        store_dev(ws, OFF_WT + (unsigned)(c0 + col) * 8192u + ((U * 4 + rq) * 64 + h * 32 + 8 * ko + a) * 16,
                  *(const u32x4*)(smem + P1_WT + col * 144 + a * 16));
      }
    }
    BAR();
    WAIT_VM(0);
    BAR();
  } else {
    const u64 ep = __hip_atomic_load((gu64*)(ws + SY_EPOCH), RLX_AGENT);
#pragma unroll
    for (int z = 0; z < 5; ++z) {
      const int fr = (wave - 8) + 4 * z;
      u32x4 val;
      const int col = lane & 31, hh = lane >> 5, r = col >> 4, k1 = col & 15;
      if (fr < 6) {
        const int ks = fr >> 1, which = fr & 1;
        float o[8];
#pragma unroll
        for (int j = 0; j < 8; ++j) {
          int row, d1;
          if (ks < 2) { row = ks; d1 = 8 * (j >> 2) + 4 * hh + (j & 3); }
          else { row = (hh == 0 && j < 4) ? (j >> 1) : 2; d1 = 16 + (j & 1); }
          const float v = cs(which, (float)((d1 * k1) % 18) * (1.0f / 18.0f));
          o[j] = (row == r && k1 < 10) ? v : 0.f;
        }
        val = u32x4{pack_f16(o[0], o[1]), pack_f16(o[2], o[3]), pack_f16(o[4], o[5]), pack_f16(o[6], o[7])};
      } else if (fr < 12) {
        val = e32_frag(fr - 6, lane);
      } else {
#pragma unroll
        for (int y = 0; y < 2; ++y) {
          const int rho = 2 * (fr - 12) + y, d2 = (rho & 3) + 8 * (rho >> 2) + 4 * hh;
          const float a = (float)(d2 * k1) * (1.0f / 576.0f);
          val[y] = __float_as_uint(__builtin_amdgcn_cosf(a)); val[2 + y] = __float_as_uint(-__builtin_amdgcn_sinf(a));
        }
      }
      *(u32x4*)(smem + P1_C1 + fr * 1024 + lane * 16) = val;
    }
    tag = ((u64)__builtin_amdgcn_readfirstlane((unsigned)(ep >> 32)) << 32 | (unsigned)__builtin_amdgcn_readfirstlane((unsigned)ep)) *
              6364136223846793005ull + 1442695040888963407ull;
    if (wave == 8 && b < 66) {
      const int r = lane & 31, h = lane >> 5;
      u32x4 val;
      if (b < 6) val = e32_frag(b, lane);
      else if (b < 18) {
        const bool inv = b >= 12;
        const int f2 = inv ? b - 12 : b - 6, u = f2 / 3, which = f2 - 3 * u;
        float o[8];
#pragma unroll
        for (int j = 0; j < 8; ++j) {
          const int kap = 16 * u + 8 * (j >> 2) + 4 * h + (j & 3);
          o[j] = cs(which, (float)((inv ? kap * (2 * r + 1) : r * (2 * kap + 1)) & 63) * (1.0f / 64.0f));
        }
        val = u32x4{pack_f16(o[0], o[1]), pack_f16(o[2], o[3]), pack_f16(o[4], o[5]), pack_f16(o[6], o[7])};
      } else if (b < 50) {
        const bool inv = b >= 34;
        const int q = inv ? b - 34 : b - 18, p = q & 7, T = q >> 3;
#pragma unroll
        for (int z = 0; z < 2; ++z) {
          const int rho = 2 * p + z, cr = (rho & 3) + 8 * (rho >> 2) + 4 * h;
          const int n = inv ? (2 * r + T) * cr : (2 * cr + T) * r;
          const float a = (float)(n & 2047) * (1.0f / 2048.0f);
          val[z] = __float_as_uint(__builtin_amdgcn_cosf(a) * (inv ? 0.125f : 1.0f));
          val[2 + z] = __float_as_uint(inv ? __builtin_amdgcn_sinf(a) * 0.125f : -__builtin_amdgcn_sinf(a));
        }
      } else if (b < 52) {
        float o[8];
#pragma unroll
        for (int j = 0; j < 8; ++j) {
          const int k1 = 8 * (j >> 2) + 4 * h + (j & 3);
          const float v = cs(b - 50, (float)((r * k1) % 18) * (1.0f / 18.0f)) * ((k1 == 0 || k1 == 9) ? 1.f : 2.f);
          o[j] = (k1 < 10 && r < 18) ? v : 0.f;
        }
        val = u32x4{pack_f16(o[0], o[1]), pack_f16(o[2], o[3]), pack_f16(o[4], o[5]), pack_f16(o[6], o[7])};
      } else if (b < 58) {
        val = e32_frag(b - 52, lane);
      } else {
#pragma unroll
        for (int y = 0; y < 2; ++y) {
          const int rho = 2 * (b - 58) + y, k1 = ((rho & 3) + 8 * (rho >> 2) + 4 * h) & 15;
          const float a = (float)(k1 * r) * (1.0f / 576.0f);
          val[y] = __float_as_uint(__builtin_amdgcn_cosf(a) * 0.125f); val[2 + y] = __float_as_uint(__builtin_amdgcn_sinf(a) * 0.125f);
        }
      }
      store_dev(ws, (b < 50 ? OFF_TAB + b * 1024u : OFF_RT + (b - 50) * 1024u) + lane * 16, val);
      WAIT_VM(0);
    }
#pragma unroll
    for (int i = 0; i < PF1_I; ++i)
      dma4(rs_w1, ldsb + P1_DUMMY + wave * 256, w1_line(((i * 32 + idx) * 4 + (wave - 8)) * 64 + lane, PF1_ROWS * nl1, 0, nl1, ft0 * 256u));
    WAIT_LGKM0();
    BAR();
    BAR();
    BAR();
  }
  if (wave == 0) DBG(2);
  if (idx < 27) {
    char* ebuf = smem + (29 % 5) * CG1::STAGE;
    u32x2 bv[3][2];
#pragma unroll
    for (int it = 0; it < 3; ++it) {
      const u32x4* bp = (const u32x4*)(b1 + ft * 128 + (min(wave + 11 * it, 23) / 3) * 16);
      const u32x4 b0 = bp[0], b1v = bp[1];
      const bool hi = lane >= 32;
      bv[it][0] = u32x2{hi ? b0[2] : b0[0], hi ? b0[3] : b0[1]}; bv[it][1] = u32x2{hi ? b1v[2] : b1v[0], hi ? b1v[3] : b1v[1]};
      asm volatile("" : "+v"(bv[it][0]), "+v"(bv[it][1]));
    }
    if (wave == 8) {
      poll_flags<4>(ws, SY_H + lane * 8, true, tag);
      if (b == 0 && lane == 0) set_flag(ws, SY_EPOCH, tag);
      DBG(3);
    }
    BAR();
    if (wave >= 8) {
      auto issue_h = [&](int kt, int so) {
#pragma unroll
        for (int m = 0; m < 3; ++m) {
          const int a = (wave - 8) * 3 + m;
          dma16<2>(rs_ws, ldsb + so + CG1::A_BYTES + a * 1024, lane * 16, OFF_H + (unsigned)((dt1 * 3 + (a >> 2)) * 128 + kt * 4 + (a & 3)) * 1024u);
        }
      };
      issue_h(0, 0); issue_h(1, CG1::STAGE); issue_h(2, 2 * CG1::STAGE);
      CG1::ring<3>(32, issue_h);
      if (wave == 11) {
#pragma unroll
        for (int i = 0; i < PF2T; ++i) {
          const int line = (i * 27 + idx) * 64 + lane;
          dma4(rs_w2, ldsb + P1_DUMMY + wave * 256, (unsigned)((g >> 1) * 1152 + (line >> 4)) * 4096u + (g & 1) * 2048u + (line & 15) * 128u);
        }
      }
    } else if (wave >= 4) {
      CG1::ring<4>(32, issue_w1);
    } else {
      f32x16 acc[1][3] = {};
      int rslot = 0;
      CG1::consume<false>(smem, wave, lane, 32, rslot, acc);
      if (wave == 0) DBG(4);
#pragma unroll
      for (int j = 0; j < 3; ++j)
#pragma unroll
        for (int s = 0; s < 2; ++s) {
          u32x4 val;
#pragma unroll
          for (int e = 0; e < 4; ++e) val[e] = pack_bf16(acc[0][j][8 * s + 2 * e], acc[0][j][8 * s + 2 * e + 1]);
          *(u32x4*)(ebuf + ((wave * 2 + s) * 3 + j) * 1024 + lane * 16) = val;
        }
    }
    WAIT_LGKM0();
    BAR();
    if (wave < 11) {
#pragma unroll
      for (int it = 0; it < 3; ++it) {
        const int i = wave + 11 * it;
        if (i < 24) {
          const int z = i / 3, j = i - 3 * z;
          const u32x4 a = *(const u32x4*)(ebuf + i * 1024 + lane * 16);
          u32x4 val;
#pragma unroll
          for (int e = 0; e < 4; ++e) {
            const unsigned bb = bv[it][e >> 1][e & 1];
            const unsigned pk = pack_bf16(lo16(a[e]) + lo16(bb), hi16(a[e]) + hi16(bb));
            val[e] = pack_bf16(gelu_tanh(lo16(pk)), gelu_tanh(hi16(pk)));
          }
          store_dev(ws, OFF_U + (unsigned)((dt1 * 3 + j) * 288 + ft * 8 + z) * 1024u + lane * 16, val);
        }
      }
      WAIT_VM(0);
    }
    BAR();
    if (threadIdx.x == 0) set_flag(ws, SY_U + tile * 8, tag);
  } else {
    const int me = (idx - 27) * 8 + g;
    {
      const int total = (2048 - PF1_ROWS) * nl1;
      for (int i0 = ((idx - 27) * 12 + wave) * 64; i0 < total; i0 += 60 * 64) {
        dma4(rs_w1, ldsb + P1_DUMMY + wave * 256, w1_line(i0 + lane, total, PF1_ROWS, nl1, ft0 * 256u));
        WAIT_VM(7);
      }
    }
    if (wave == 0) DBG(3);
    if (me < 37) {
      if (wave == 8) poll_flags<4>(ws, SY_A + lane * 8, true, tag);
      BAR();
      const int cbk = me == 36 ? 0 : me;
      char* img = smem + 65536;
      for (int i = wave; i < 64; i += 12) {
        const int P = 4 * i + (lane >> 4);
        dma16<2>(rs_ws, ldsb + 65536 + i * 1024, (unsigned)P * 9216u + (unsigned)(((lane & 15) ^ (P & 15)) * 16), OFF_X1 + cbk * 256u);
      }
      for (int i = wave; i < TAB_BYTES / 1024; i += 12) dma16<2>(rs_ws, ldsb + i * 1024, lane * 16, OFF_TAB + i * 1024u);
      const bool act = me == 36 ? wave == 0 : wave < 8;
      const int c = me == 36 ? 288 : me * 8 + wave;
      const float wsc = __uint_as_float((253u - wmax_exp(ws, lane)) << 23) * (1.0f / 64.0f);
      WAIT_VM(0);
      BAR();
      const int slot_re = me == 36 ? 1 : 2 * wave, slot_im = c == 288 ? -2 : (c == 0 ? -1 : 2 * wave + 1);
      u32x4 zr[2][2], zi[2][2];
      if (act) {
#pragma unroll
        for (int z = 0; z < 4; ++z) {
          const char* p = img + (z * 64 + lane) * 256;
          zr[z >> 1][z & 1] = *(const u32x4*)(p + ((slot_re ^ (lane & 15)) * 16));
          zi[z >> 1][z & 1] = slot_im >= 0 ? *(const u32x4*)(p + ((slot_im ^ (lane & 15)) * 16)) : u32x4{0, 0, 0, 0};
        }
      }
      WAIT_LGKM0();
      BAR();
      if (act) colfft_wave(smem + lane * 16, ws, c, lane, wsc, zr, zi, img, slot_re, slot_im);
      WAIT_LGKM0();
      BAR();
      if (me == 36) {
        if (wave < 4) store_dev(ws, OFF_Y288 + wave * 1024 + lane * 16, *(const u32x4*)(img + wave * 1024 + lane * 16));
      } else {
        for (int i = wave; i < 64; i += 12) {
          const int Qy = 4 * i + (lane >> 4);
          store_dev(ws, OFF_Y1 + (unsigned)Qy * 9216u + cbk * 256 + (lane & 15) * 16,
                    *(const u32x4*)(img + Qy * 256 + (((lane & 15) ^ ((Qy >> 3) & 15)) * 16)));
        }
      }
      WAIT_VM(0);
      BAR();
      if (threadIdx.x == 0) set_flag(ws, SY_Y + me * 8, tag);
    }
  }
  if (wave == 0) DBG(5);
  if (idx < 24) {
    const int ks = g >> 1, dt = idx % 3, st = (g & 1) * 8 + idx / 3;
    const unsigned myflags = SY_T + (unsigned)((st * 4 + ks) * 3 + dt) * 64u;
    if (wave >= 8) {
      if (wave == 8) { poll_flags<1>(ws, SY_U + (unsigned)((9 * ks + (lane >> 1)) * 6 + 2 * dt + (lane & 1)) * 8u, lane < 18, tag); DBG(6); }
      BAR();
      auto issue_u = [&](int kt, int so) {
#pragma unroll
        for (int m = 0; m < 6; ++m) {
          const int a = (wave - 8) * 6 + m;
          dma16<2>(rs_ws, ldsb + so + a * 1024, lane * 16, OFF_U + (unsigned)((dt * 6 + (a >> 2)) * 288 + (ks * 18 + kt) * 4 + (a & 3)) * 1024u);
        }
      };
      issue_u(0, 0); issue_u(1, CG2::STAGE);
      CG2::ring<6>(18, issue_u);
      BAR();
      if (wave == 8) poll_flags<1>(ws, myflags + (lane & 3) * 8, true, tag);
      BAR();
      return;
    }
    if (wave >= 4) {
      auto issue_w2 = [&](int kt, int so) {
        constexpr unsigned ld = S_ * 2;
        const unsigned vo = CG2::voff<TR128>(lane, ld);
#pragma unroll
        for (int m = 0; m < 4; ++m) {
          const int a = (wave - 4) * 4 + m;
          const unsigned sof = (unsigned)((ks * 18 + kt) * 64 + 4 * a) * ld + st * 256u;
          if (kt >= NTF2) dma16<1>(rs_w2, ldsb + so + CG2::A_BYTES + a * 1024, vo, sof);
          else dma16<0>(rs_w2, ldsb + so + CG2::A_BYTES + a * 1024, vo, sof);
        }
      };
      issue_w2(0, 0); issue_w2(1, CG2::STAGE);
      BAR();
      CG2::ring<4>(18, issue_w2);
      BAR();
      BAR();
      return;
    }
    BAR();
    f32x16 acc[3][2] = {};
    int rslot = 0;
    CG2::consume<false>(smem, wave, lane, 18, rslot, acc);
    if (wave == 0) DBG(7);
    const int wa = wave >> 1, wb = wave & 1;
    const bool own0 = wb * 2 == ks, own1 = wb * 2 + 1 == ks;
#pragma unroll
    for (int j = 0; j < 2; ++j) {
      if (j ? own1 : own0) continue;
#pragma unroll
      for (int i = 0; i < 3; ++i) {
        const unsigned o = OFF_SLAB + (unsigned)((ks * 64 + st * 4 + wb * 2 + j) * 18 + dt * 6 + wa * 3 + i) * 2048u + lane * 16;
#pragma unroll
        for (int u = 0; u < 2; ++u) {
          u32x4 val;
#pragma unroll
          for (int e = 0; e < 4; ++e) val[e] = pack_bf16(acc[i][j][8 * u + 2 * e], acc[i][j][8 * u + 2 * e + 1]);
          store_dev(ws, o + u * 1024, val);
        }
      }
    }
    WAIT_VM(0);
    BAR();
    if (wave == 0 && lane < 4 && lane != ks) set_flag(ws, SY_T + (unsigned)((st * 4 + lane) * 3 + dt) * 64u + ks * 8, tag);
    BAR();
    if (wave == 0) DBG(8);
    if (!own0 && !own1) return;
    const int sf = st * 4 + ks, s = sf * 32 + (lane & 31);
    auto finish = [&](auto J) {
      constexpr int j = decltype(J)::value;
      u32x4 pv[3][2][4];
#pragma unroll
      for (int i = 0; i < 3; ++i)
#pragma unroll
        for (int u = 0; u < 2; ++u) {
          const unsigned o = (unsigned)(sf * 18 + dt * 6 + wa * 3 + i) * 2048u + u * 1024 + lane * 16;
#pragma unroll
          for (int q = 0; q < 3; ++q) pv[i][u][q] = load_dev(ws, OFF_SLAB + o + ((ks + 1 + q) & 3) * 2359296u);
          pv[i][u][3] = load_dev(ws, OFF_R + o);
        }
#pragma unroll
      for (int i = 0; i < 3; ++i)
#pragma unroll
        for (int u = 0; u < 2; ++u) {
          u32x4 o;
#pragma unroll
          for (int e = 0; e < 4; ++e)
            o[e] = pack_bf16(acc[i][j][8 * u + 2 * e] + (lo16(pv[i][u][0][e]) + lo16(pv[i][u][1][e])) + (lo16(pv[i][u][2][e]) + lo16(pv[i][u][3][e])),
                             acc[i][j][8 * u + 2 * e + 1] + (hi16(pv[i][u][0][e]) + hi16(pv[i][u][1][e])) + (hi16(pv[i][u][2][e]) + hi16(pv[i][u][3][e])));
          *(u32x4*)(out + (size_t)s * D_ + (dt * 6 + wa * 3 + i) * 32 + (lane >> 5) * 16 + u * 8) = o;
        }
    };
    if (own0) finish(IC<0>{}); else finish(IC<1>{});
  } else {
    const int me = (idx - 24) * 8 + g, sr = lane & 31;
    char* kt = smem + 37888; char* yb = kt + 17408;
    u32x4 xv[3];
#pragma unroll
    for (int it = 0; it < 3; ++it) xv[it] = *(const u32x4*)(x + (size_t)(me * 32 + sr) * D_ + ((it * 12 + wave) * 2 + (lane >> 5)) * 8);
    const float bias = lo16(b2[me * 32 + sr]);
    if (wave == 8) { poll_flags<1>(ws, SY_Y + min(lane, 36) * 8, true, tag); DBG(6); }
    BAR();
#pragma unroll
    for (int it = 0; it < 3; ++it) dma16<2>(rs_ws, ldsb + (it * 12 + wave) * 1024, lane * 16, OFF_Y1 + me * 36864u + (it * 12 + wave) * 1024u);
    dma16<2>(rs_ws, ldsb + 37888 + wave * 1024, lane * 16, OFF_RT + wave * 1024u);
    if (wave < 4) dma16<2>(rs_ws, ldsb + 37888 + (12 + wave) * 1024, lane * 16, OFF_RT + (12 + wave) * 1024u);
    if (wave == 4) dma16<2>(rs_ws, ldsb + 37888 + 16384, (lane & 3) * 16, OFF_Y288 + me * 64u);
    const float fs = __uint_as_float((wmax_exp(ws, lane) + 1u) << 23) * (1.0f / 9.0f);
    WAIT_VM(0);
    BAR();
    for (int task = wave; task < 16; task += 12)
      irfft_wave(smem + (task >> 2) * 9216, kt + 16384 + (task >> 2) * 16, task & 3, lane, yb + task * 2 * FIN_OS, kt + lane * 16);
    WAIT_LGKM0();
    BAR();
#pragma unroll
    for (int it = 0; it < 3; ++it) {
      const int dp = (it * 12 + wave) * 2 + (lane >> 5);
      const char* yp = yb + sr * FIN_OS + dp * 32;
      const f32x4 y0 = *(const f32x4*)yp, y1 = *(const f32x4*)(yp + 16);
      u32x4 o;
#pragma unroll
      for (int e = 0; e < 4; ++e) {
        const float ya = e < 2 ? y0[2 * e] : y1[2 * e - 4], yc = e < 2 ? y0[2 * e + 1] : y1[2 * e - 3];
        o[e] = pack_bf16(lo16(xv[it][e]) + bias + ya * fs, hi16(xv[it][e]) + bias + yc * fs);
      }
      store_dev(ws, OFF_R + (unsigned)(me * 18 + (dp >> 2)) * 2048u + (dp & 1) * 1024 + (((dp >> 1) & 1) * 32 + sr) * 16, o);
    }
    WAIT_VM(0);
    BAR();
    if (wave == 0 && lane < 3) set_flag(ws, SY_T + (unsigned)(me * 3 + lane) * 64u + (me & 3) * 8, tag);
    if (wave == 0) DBG(9);
  }
}

extern "C" void kernel_launch(void* const* d_in, const int* in_sizes, int n_in, void* d_out, int out_size, void* d_ws, size_t ws_size,
                              hipStream_t stream) {
  (void)hipFuncSetAttribute((const void*)k_all, hipFuncAttributeMaxDynamicSharedMemorySize, 163840);
  k_all<<<256, 768, 163840, stream>>>((const u16*)d_in[0], (const u16*)d_in[1], (const u16*)d_in[2], (const char*)d_in[4], (const u16*)d_in[5],
                                      (const char*)d_in[6], (const u16*)d_in[7], (char*)d_ws, (u16*)d_out);
}
```

```cpp
#define W1_Z 3
#define PF2T 2
#define NTF2 3
#include <hip/hip_runtime.h>
#include <hip/hip_bf16.h>
#include <stdint.h>

#define DI __device__ __forceinline__
#define LAS __attribute__((address_space(3)))
typedef unsigned short u16;
typedef unsigned long long u64;
typedef __attribute__((ext_vector_type(2))) float f32x2;
typedef __attribute__((ext_vector_type(4))) float f32x4;
typedef __attribute__((ext_vector_type(16))) float f32x16;
typedef __attribute__((ext_vector_type(2))) _Float16 f16x2;
typedef __attribute__((ext_vector_type(8))) _Float16 f16x8;
typedef __attribute__((ext_vector_type(2))) __bf16 bf16x2;
typedef __attribute__((ext_vector_type(8))) __bf16 bf16x8;
typedef __attribute__((ext_vector_type(4))) short s16x4;
typedef __attribute__((ext_vector_type(2))) unsigned u32x2;
typedef __attribute__((ext_vector_type(4))) unsigned u32x4;
typedef __attribute__((address_space(1))) unsigned gu32;
typedef __attribute__((address_space(1))) u64 gu64;
#define RLX_AGENT __ATOMIC_RELAXED, __HIP_MEMORY_SCOPE_AGENT

#define PF1_I 1
#define PF2_TAU 29
#define PF2_ROWS 0
#define POLL_SLEEP 4

constexpr int S_ = 2048, D_ = 576, F_ = 4608, NC_ = 289;

constexpr unsigned OFF_H    = 0;
constexpr unsigned OFF_WT   = OFF_H + 2359296;
constexpr unsigned OFF_TAB  = OFF_WT + 2367488;
constexpr unsigned OFF_RT   = OFF_TAB + 65536;
constexpr unsigned OFF_Y288 = OFF_RT + 16384;
constexpr unsigned OFF_WMAX = OFF_Y288 + 4096;
constexpr unsigned OFF_X1   = OFF_WMAX + 4096;
constexpr unsigned OFF_Y1   = OFF_X1 + 2359296;
constexpr unsigned OFF_U    = OFF_Y1 + 2359296;
constexpr unsigned OFF_SLAB = OFF_U + 5308416;
constexpr unsigned OFF_R    = OFF_SLAB + 9437184;
constexpr unsigned OFF_SYNC = OFF_R + 2359296;
constexpr unsigned SY_EPOCH = OFF_SYNC, SY_TMO = OFF_SYNC + 256, SY_A = OFF_SYNC + 2048, SY_Y = OFF_SYNC + 4096, SY_U = OFF_SYNC + 6144,
                   SY_H = SY_U + 2 * 2048, SY_T = SY_H + 8 * 2048;
constexpr unsigned WS_END   = SY_T + 192 * 64;
constexpr int TAB_E1 = 0, TAB_EO = 6144, TAB_EOI = 12288, TAB_TW1 = 18432, TAB_TW2 = 34816, TAB_BYTES = 51200;

DI unsigned pack_bf16(float a, float b) { f32x2 v = {a, b}; return __builtin_bit_cast(unsigned, __builtin_convertvector(v, bf16x2)); }
DI unsigned pack_f16(float a, float b) { f32x2 v = {a, b}; return __builtin_bit_cast(unsigned, __builtin_convertvector(v, f16x2)); }
DI float lo16(unsigned p) { return __uint_as_float(p << 16); }
DI float hi16(unsigned p) { return __uint_as_float(p & 0xffff0000u); }
DI float rbf(float a) { return lo16(pack_bf16(a, a)); }

template <int CTRL> DI float dpp_add(float v) {
  return v + __int_as_float(__builtin_amdgcn_update_dpp(0, __float_as_int(v), CTRL, 0xf, 0xf, true));
}
DI float sum32(float v) {
  v = dpp_add<0xB1>(v); v = dpp_add<0x4E>(v); v = dpp_add<0x141>(v); v = dpp_add<0x140>(v);
  auto r = __builtin_amdgcn_permlane16_swap(__float_as_uint(v), __float_as_uint(v), false, false);
  return __uint_as_float(r[0]) + __uint_as_float(r[1]);
}

#define DBG(i)
#define WAIT_VM(n) asm volatile("s_waitcnt vmcnt(%0)" ::"n"(n) : "memory")
#define WAIT_LGKM0() asm volatile("s_waitcnt lgkmcnt(0)" ::: "memory")
#define BAR() do { asm volatile("" ::: "memory"); __builtin_amdgcn_s_barrier(); asm volatile("" ::: "memory"); } while (0)
#define SCHED0() __builtin_amdgcn_sched_barrier(0)

DI u32x4 mk_rsrc(const void* p, unsigned bytes) {
  const u64 a = (u64)p;
  return u32x4{(unsigned)a, (unsigned)(a >> 32) & 0xffffu, bytes, 0x00020000u};
}
template <int POL> DI void dma16(u32x4 rsrc, unsigned lds_off, unsigned voff, unsigned soff) {
  if constexpr (POL == 0) asm volatile("s_mov_b32 m0, %0\n\ts_nop 0\n\tbuffer_load_dwordx4 %1, %2, %3 offen lds" ::"s"(lds_off), "v"(voff), "s"(rsrc), "s"(soff) : "memory");
  if constexpr (POL == 1) asm volatile("s_mov_b32 m0, %0\n\ts_nop 0\n\tbuffer_load_dwordx4 %1, %2, %3 offen nt lds" ::"s"(lds_off), "v"(voff), "s"(rsrc), "s"(soff) : "memory");
  if constexpr (POL == 2) asm volatile("s_mov_b32 m0, %0\n\ts_nop 0\n\tbuffer_load_dwordx4 %1, %2, %3 offen sc1 lds" ::"s"(lds_off), "v"(voff), "s"(rsrc), "s"(soff) : "memory");
}
DI void dma4(u32x4 rsrc, unsigned lds_off, unsigned voff) {
  asm volatile("s_mov_b32 m0, %0\n\ts_nop 0\n\tbuffer_load_dword %1, %2, 0 offen lds" ::"s"(lds_off), "v"(voff), "s"(rsrc) : "memory");
}
DI void store_dev(char* ws, unsigned off, u32x4 v) {
  __builtin_amdgcn_raw_buffer_store_b128(v, __builtin_amdgcn_make_buffer_rsrc((void*)ws, 0, WS_END, 0x00020000), off, 0, 16);
}
DI u32x4 load_dev(const char* ws, unsigned off) {
  return __builtin_amdgcn_raw_buffer_load_b128(__builtin_amdgcn_make_buffer_rsrc((void*)ws, 0, WS_END, 0x00020000), off, 0, 16);
}
DI void set_flag(char* ws, unsigned off, u64 tag) { __hip_atomic_store((gu64*)(ws + off), tag, RLX_AGENT); }
template <int N, int SLP = POLL_SLEEP> DI void poll_flags(char* ws, unsigned off, bool active, u64 tag) {
  for (unsigned spins = 0;; ++spins) {
    bool ok = true;
#pragma unroll
    for (int i = 0; i < N; ++i) { const u64 v = __hip_atomic_load((gu64*)(ws + off + i * 512), RLX_AGENT); ok &= (v == tag) || !active; }
    if (__all(ok)) break;
    __builtin_amdgcn_s_sleep(SLP);
    if (spins > (1u << 17)) { set_flag(ws, SY_TMO, tag); break; }
    if ((spins & 63u) == 63u && __all(__hip_atomic_load((gu64*)(ws + SY_TMO), RLX_AGENT) == tag)) break;
  }
  __builtin_amdgcn_fence(__ATOMIC_ACQUIRE, "wavefront");
}
DI unsigned w1_line(int i, int total, int r0, int nl, unsigned c0) {
  const int rr = i / nl, l = i - rr * nl, row = r0 + rr;
  return i < total ? (unsigned)(64 * (row & 31) + (row >> 5)) * (F_ * 2) + c0 + (unsigned)l * 128u : 0xfffffff0u;
}

template <bool F16> DI f32x16 mfma32(u32x4 a, u32x4 b, f32x16 c) {
  if constexpr (F16) return __builtin_amdgcn_mfma_f32_32x32x16_f16(__builtin_bit_cast(f16x8, a), __builtin_bit_cast(f16x8, b), c, 0, 0, 0);
  else return __builtin_amdgcn_mfma_f32_32x32x16_bf16(__builtin_bit_cast(bf16x8, a), __builtin_bit_cast(bf16x8, b), c, 0, 0, 0);
}
DI u32x4 pack8_f16(const f32x16& v, int u) {
  return u32x4{pack_f16(v[8 * u], v[8 * u + 1]), pack_f16(v[8 * u + 2], v[8 * u + 3]), pack_f16(v[8 * u + 4], v[8 * u + 5]), pack_f16(v[8 * u + 6], v[8 * u + 7])};
}
DI u32x4 tr_frag(const char* p0, const char* p1) {
  s16x4 lo = __builtin_amdgcn_ds_read_tr16_b64_v4i16((LAS s16x4*)p0);
  s16x4 hi = __builtin_amdgcn_ds_read_tr16_b64_v4i16((LAS s16x4*)p1);
  u32x2 a = __builtin_bit_cast(u32x2, lo), c = __builtin_bit_cast(u32x2, hi);
  return u32x4{a[0], a[1], c[0], c[1]};
}
DI void tw_piece(f32x16& re, f32x16& im, int p, const f32x4 w) {
  const f32x2 r = {re[2 * p], re[2 * p + 1]}, i = {im[2 * p], im[2 * p + 1]}, wr = {w[0], w[1]}, wi = {w[2], w[3]};
  const f32x2 a = __builtin_elementwise_fma(r, wr, -(i * wi)), b = __builtin_elementwise_fma(r, wi, i * wr);
  re[2 * p] = a[0]; re[2 * p + 1] = a[1]; im[2 * p] = b[0]; im[2 * p + 1] = b[1];
}
DI void tw_load(f32x4 (&t)[8], const char* tw) {
#pragma unroll
  for (int p = 0; p < 8; ++p) t[p] = *(const f32x4*)(tw + p * 1024);
}
DI void tw_apply(f32x16& re, f32x16& im, const f32x4 (&t)[8]) {
#pragma unroll
  for (int p = 0; p < 8; ++p) tw_piece(re, im, p, t[p]);
}
DI float cs(int which, float rev) {
  return which == 0 ? __builtin_amdgcn_cosf(rev) : (which == 1 ? -__builtin_amdgcn_sinf(rev) : __builtin_amdgcn_sinf(rev));
}
DI u32x4 e32_frag(int frag, int lane) {
  const int t = frag / 3, which = frag - 3 * t, r = lane & 31, h = lane >> 5;
  float o[8];
#pragma unroll
  for (int j = 0; j < 8; ++j) o[j] = cs(which, (float)(((16 * t + 8 * (j >> 2) + 4 * h + (j & 3)) * r) & 31) * (1.0f / 32.0f));
  return u32x4{pack_f16(o[0], o[1]), pack_f16(o[2], o[3]), pack_f16(o[4], o[5]), pack_f16(o[6], o[7])};
}

enum { LIN = 0, TR128 = 1, TR64 = 2 };

template <int V> struct IC { static constexpr int value = V; };
template <int NA_W, int NB_W, int WA, int WB, int AK, int BK, int NS, int DPM, int DEPTH>
struct GC {
  static constexpr int NC = WA * WB, NFA = NA_W * WA, NFB = NB_W * WB;
  static constexpr int A_BYTES = NFA * 4096, B_BYTES = NFB * 4096, STAGE = A_BYTES + B_BYTES;
  static constexpr int LDS_BYTES = NS * STAGE;
  static_assert(AK == LIN || NFA == (AK == TR128 ? 4 : 2), "");
  static_assert(BK == LIN || NFB == (BK == TR128 ? 4 : 2), "");

  template <int K> static DI unsigned voff(int lane, unsigned rstride) {
    if constexpr (K == LIN) return lane * 16;
    else if constexpr (K == TR128) return (unsigned)(lane >> 4) * rstride + (unsigned)(((lane & 15) ^ ((lane >> 4) << 2)) * 16);
    else return (unsigned)(lane >> 3) * rstride + (unsigned)(((lane & 7) ^ (((lane >> 4) & 1) << 2)) * 16);
  }
  template <int K, int NF> static DI void frag_addr(unsigned (&addr)[K == LIN ? 1 : NF], int opoff, int wpos, int lane) {
    if constexpr (K == LIN) addr[0] = opoff + wpos * NF * 4096 + lane * 16;
    else {
      const int h = lane >> 5, blk = (lane >> 4) & 1, q = (lane & 15) >> 2, p = lane & 3;
#pragma unroll
      for (int i = 0; i < NF; ++i) {
        const int ch = 4 * (wpos * NF + i) + 2 * blk + (p >> 1);
        if constexpr (K == TR128) addr[i] = opoff + (4 * h + q) * 256 + ((ch ^ (q << 2)) * 16) + 8 * (p & 1);
        else addr[i] = opoff + (4 * h + q) * 128 + ((ch ^ (((q >> 1) & 1) << 2)) * 16) + 8 * (p & 1);
      }
    }
  }
  template <int K, int NF> static DI void rd(u32x4 (&f)[NF], const char* smem, const unsigned (&addr)[K == LIN ? 1 : NF], int sb, int hh) {
#pragma unroll
    for (int i = 0; i < NF; ++i) {
      if constexpr (K == LIN) f[i] = *(const u32x4*)(smem + addr[0] + sb + (i * 4 + hh) * 1024);
      else {
        constexpr int RB = K == TR128 ? 256 : 128;
        const char* p = smem + addr[i] + sb + hh * 16 * RB;
        s16x4 lo = __builtin_amdgcn_ds_read_tr16_b64_v4i16((LAS s16x4*)p);
        s16x4 hi = __builtin_amdgcn_ds_read_tr16_b64_v4i16((LAS s16x4*)(p + 8 * RB));
        u32x2 a = __builtin_bit_cast(u32x2, lo), c = __builtin_bit_cast(u32x2, hi);
        f[i] = u32x4{a[0], a[1], c[0], c[1]};
      }
    }
  }
  static DI void ilv() {
    constexpr int NR = NA_W * (AK == LIN ? 1 : 2) + NB_W * (BK == LIN ? 1 : 2), NM = NA_W * NB_W;
    constexpr int NG = (NR + DPM - 1) / DPM < NM ? (NR + DPM - 1) / DPM : NM;
#pragma unroll
    for (int i = 0; i < NG; ++i) {
      __builtin_amdgcn_sched_group_barrier(0x8, 1, 0);
      if (i + 1 < NG) __builtin_amdgcn_sched_group_barrier(0x100, DPM, 0);
      else __builtin_amdgcn_sched_group_barrier(0x100, NR - DPM * (NG - 1), 0);
    }
    if constexpr (NM > NG) __builtin_amdgcn_sched_group_barrier(0x8, NM - NG, 0);
    SCHED0();
  }
  template <int G, class ISSUE> static DI void ring(int nt, ISSUE issue) {
    int wslot = NS - 2;
    for (int tau = 0; tau < nt - (NS - 2); ++tau) {
      WAIT_VM((NS - 3) * G);
      BAR();
      issue(tau + NS - 2, wslot * STAGE);
      wslot = (wslot + 1 == NS) ? 0 : wslot + 1;
    }
    if constexpr (NS >= 5) { WAIT_VM(2 * G); BAR(); }
    if constexpr (NS >= 4) { WAIT_VM(G); BAR(); }
    WAIT_VM(0); BAR();
    static_assert(NS >= 3 && NS <= 5, "");
  }
  template <bool F16>
  static DI void consume(const char* smem, int wave, int lane, int n, int& rslot, f32x16 (&acc)[NA_W][NB_W]) {
    const int wa = wave / WB, wb = wave - wa * WB;
    unsigned addrA[AK == LIN ? 1 : NA_W], addrB[BK == LIN ? 1 : NB_W];
    frag_addr<AK, NA_W>(addrA, 0, wa, lane);
    frag_addr<BK, NB_W>(addrB, A_BYTES, wb, lane);
    u32x4 fa[DEPTH][NA_W], fb[DEPTH][NB_W];
#pragma unroll
    for (int d = 1; d < DEPTH; ++d) {
#pragma unroll
      for (int i = 0; i < NA_W; ++i) fa[d][i] = u32x4{0, 0, 0, 0};
#pragma unroll
      for (int i = 0; i < NB_W; ++i) fb[d][i] = u32x4{0, 0, 0, 0};
    }
    auto mm = [&](const u32x4 (&a)[NA_W], const u32x4 (&b)[NB_W]) {
#pragma unroll
      for (int i = 0; i < NA_W; ++i)
#pragma unroll
        for (int j = 0; j < NB_W; ++j) acc[i][j] = mfma32<F16>(a[i], b[j], acc[i][j]);
    };
    auto step = [&](auto Z0) {
      constexpr int z0 = decltype(Z0)::value;
      BAR();
      SCHED0();
      const int sb = rslot * STAGE;
#pragma unroll
      for (int hh = 0; hh < 4; ++hh) {
        rd<AK, NA_W>(fa[(z0 + hh) % DEPTH], smem, addrA, sb, hh);
        rd<BK, NB_W>(fb[(z0 + hh) % DEPTH], smem, addrB, sb, hh);
        mm(fa[(z0 + hh + 1) % DEPTH], fb[(z0 + hh + 1) % DEPTH]);
        ilv();
      }
      rslot = (rslot + 1 == NS) ? 0 : rslot + 1;
    };
    if constexpr (DEPTH == 2) {
      for (int tau = 0; tau < n; ++tau) step(IC<0>{});
      mm(fa[1], fb[1]);
    } else {
      int tau = 0;
      for (; tau + 3 <= n; tau += 3) { step(IC<0>{}); step(IC<1>{}); step(IC<2>{}); }
      if (n - tau == 2) { step(IC<0>{}); step(IC<1>{}); mm(fa[0], fb[0]); mm(fa[1], fb[1]); }
      else if (n - tau == 1) { step(IC<0>{}); mm(fa[2], fb[2]); mm(fa[0], fb[0]); }
      else { mm(fa[1], fb[1]); mm(fa[2], fb[2]); }
    }
  }
};

DI float gelu_tanh(float u) {
  const float z = u * (1.0f + 0.044715f * u * u) * (-2.0f * 0.7978845608028654f * 1.4426950408889634f);
  return u * __builtin_amdgcn_rcpf(1.0f + __builtin_amdgcn_exp2f(z));
}

template <int NV> DI void mv_ilv() {
#pragma unroll
  for (int i = 0; i < 8; ++i) { __builtin_amdgcn_sched_group_barrier(0x8, 1, 0); __builtin_amdgcn_sched_group_barrier(0x2, NV, 0); }
  SCHED0();
}
struct EF { u32x4 r, i, n; };
DI EF ef_load(const char* p) { EF e; e.r = *(const u32x4*)p; e.i = *(const u32x4*)(p + 1024); e.n = *(const u32x4*)(p + 2048); return e; }
#define MF(a, b, c) mfma32<true>(a, b, c)
DI void cm_dA(f32x16& re, f32x16& im, u32x4 dr, u32x4 di, const EF& e) { re = MF(dr, e.r, re); im = MF(dr, e.i, im); re = MF(di, e.n, re); im = MF(di, e.r, im); }
DI void cm_dB(f32x16& re, f32x16& im, u32x4 dr, u32x4 di, const EF& e) { re = MF(e.r, dr, re); im = MF(e.i, dr, im); re = MF(e.n, di, re); im = MF(e.r, di, im); }
DI void cm_dAc(f32x16& re, f32x16& im, u32x4 dr, u32x4 di, const EF& e) { re = MF(dr, e.r, re); im = MF(dr, e.n, im); re = MF(di, e.i, re); im = MF(di, e.r, im); }
DI void wmul(f32x16& re, f32x16& im, const u32x4 (&wv)[4], float wsc) {
#pragma unroll
  for (int p = 0; p < 8; ++p) {
    const unsigned w0 = wv[p >> 1][(2 * p) & 3], w1 = wv[p >> 1][(2 * p + 1) & 3];
    tw_piece(re, im, p, f32x4{lo16(w0), lo16(w1), hi16(w0), hi16(w1)} * wsc);
  }
}
DI void bfly(f32x16& a, f32x16& b) {
  const f32x16 x = a, y = b; a = x + y; b = x - y;
}
DI void colfft_wave(const char* tab  , const char* ws, int c, int lane, float wsc, const u32x4 (&zr)[2][2], const u32x4 (&zi)[2][2],
                    char* img, int slot_re, int slot_im) {
  f32x4 tw[8];
  f32x16 r0 = {}, i0 = {}, r1 = {}, i1 = {};
  u32x4 ar[2], ai[2];
  EF ea = ef_load(tab + TAB_E1), eb = ef_load(tab + TAB_E1 + 3072);
  tw_load(tw, tab + TAB_TW1);
  SCHED0();
  cm_dA(r0, i0, zr[0][0], zi[0][0], ea); cm_dA(r0, i0, zr[0][1], zi[0][1], eb);
  SCHED0();
  cm_dA(r1, i1, zr[1][0], zi[1][0], ea); cm_dA(r1, i1, zr[1][1], zi[1][1], eb);
#pragma unroll
  for (int p = 0; p < 8; ++p) { tw_piece(r0, i0, p, tw[p]); tw[p] = *(const f32x4*)(tab + TAB_TW1 + 8192 + p * 1024); }
  ar[0] = pack8_f16(r0, 0); ar[1] = pack8_f16(r0, 1); ai[0] = pack8_f16(i0, 0); ai[1] = pack8_f16(i0, 1);
  mv_ilv<6>();
  u32x4 wv0[4], wv1[4];
#pragma unroll
  for (int i = 0; i < 4; ++i) wv0[i] = load_dev(ws, OFF_WT + c * 8192u + i * 1024 + lane * 16);
  r0 = f32x16{}; i0 = f32x16{};
  cm_dB(r0, i0, ar[0], ai[0], ea); ea = ef_load(tab + TAB_EO);
  cm_dB(r0, i0, ar[1], ai[1], eb); eb = ef_load(tab + TAB_EO + 3072);
  tw_apply(r1, i1, tw);
  ar[0] = pack8_f16(r1, 0); ar[1] = pack8_f16(r1, 1); ai[0] = pack8_f16(i1, 0); ai[1] = pack8_f16(i1, 1);
  mv_ilv<6>();
#pragma unroll
  for (int i = 0; i < 4; ++i) wv1[i] = load_dev(ws, OFF_WT + c * 8192u + (4 + i) * 1024 + lane * 16);
  r1 = f32x16{}; i1 = f32x16{};
  cm_dB(r1, i1, ar[0], ai[0], ea); ea = ef_load(tab + TAB_E1);
  cm_dB(r1, i1, ar[1], ai[1], eb); eb = ef_load(tab + TAB_E1 + 3072);
  SCHED0();
  bfly(r0, r1); bfly(i0, i1);
  wmul(r0, i0, wv0, wsc);
  wmul(r1, i1, wv1, wsc);
  bfly(r0, r1); bfly(i0, i1);
  u32x4 dr[2], di[2];
  ar[0] = pack8_f16(r0, 0); ar[1] = pack8_f16(r0, 1); ai[0] = pack8_f16(i0, 0); ai[1] = pack8_f16(i0, 1);
  SCHED0();
  tw_load(tw, tab + TAB_TW2);
  r0 = f32x16{}; i0 = f32x16{};
  cm_dAc(r0, i0, ar[0], ai[0], ea); ea = ef_load(tab + TAB_EOI);
  cm_dAc(r0, i0, ar[1], ai[1], eb); eb = ef_load(tab + TAB_EOI + 3072);
  dr[0] = pack8_f16(r1, 0); dr[1] = pack8_f16(r1, 1); di[0] = pack8_f16(i1, 0); di[1] = pack8_f16(i1, 1);
  mv_ilv<2>();
  r1 = f32x16{}; i1 = f32x16{};
  cm_dAc(r1, i1, dr[0], di[0], ea); ea = ef_load(tab + TAB_E1);
  cm_dAc(r1, i1, dr[1], di[1], eb); eb = ef_load(tab + TAB_E1 + 3072);
#pragma unroll
  for (int p = 0; p < 8; ++p) { tw_piece(r0, i0, p, tw[p]); tw[p] = *(const f32x4*)(tab + TAB_TW2 + 8192 + p * 1024); }
  ar[0] = pack8_f16(r0, 0); ar[1] = pack8_f16(r0, 1); ai[0] = pack8_f16(i0, 0); ai[1] = pack8_f16(i0, 1);
  mv_ilv<6>();
  r0 = f32x16{}; i0 = f32x16{};
  cm_dAc(r0, i0, ar[0], ai[0], ea); cm_dAc(r0, i0, ar[1], ai[1], eb);
  tw_apply(r1, i1, tw);
  dr[0] = pack8_f16(r1, 0); dr[1] = pack8_f16(r1, 1); di[0] = pack8_f16(i1, 0); di[1] = pack8_f16(i1, 1);
  mv_ilv<6>();
  r1 = f32x16{}; i1 = f32x16{};
  cm_dAc(r1, i1, dr[0], di[0], ea); cm_dAc(r1, i1, dr[1], di[1], eb);
  SCHED0();
#pragma unroll
  for (int g = 0; g < 4; ++g) {
    const int Q = (lane & 31) * 8 + 2 * g + (lane >> 5), sw = lane & 15, r = 4 * g;
    if (slot_im == -2) {
      *(u32x4*)(img + Q * 16) = u32x4{pack_f16(r0[r], r1[r]), pack_f16(r0[r + 1], r1[r + 1]), pack_f16(r0[r + 2], r1[r + 2]), pack_f16(r0[r + 3], r1[r + 3])};
      continue;
    }
#pragma unroll
    for (int z = 0; z < 2; ++z) {
      const int q = r + 2 * z;
      *(u32x4*)(img + Q * 256 + (((slot_re + z) ^ sw) * 16)) =
          u32x4{pack_f16(r0[q], i0[q]), pack_f16(r1[q], i1[q]), pack_f16(r0[q + 1], i0[q + 1]), pack_f16(r1[q + 1], i1[q + 1])};
    }
  }
}
#undef MF
constexpr int FIN_OS = 2336;
DI void irfft_wave(const char* rec, const char* y288, int w, int lane, char* orow, const char* kt) {
  const int row = lane & 31, hh = lane >> 5, k1 = row & 15, rr = 2 * w + (row >> 4);
  const bool ok = k1 < 10;
  const int k1c = ok ? k1 : 0;
  const char* pd = rec + rr * 4 + 32 * (k1c + 72 * hh);
  const char* pm = rec + rr * 4 + 32 * (576 - k1c - 72 * hh - 18 * 27);
  unsigned v[2][8];
#pragma unroll
  for (int j = 0; j < 8; ++j) {
    v[0][j] = *(const unsigned*)(pd + 576 * (8 * (j >> 2) + (j & 3)));
    v[1][j] = *(const unsigned*)(pm + 576 * (27 - (16 + 8 * (j >> 2) + (j & 3))));
  }
  const unsigned v288 = *(const u16*)(y288 + rr * 2);
  f32x4 tw[8];
  tw_load(tw, kt + 8192);
  if (k1 == 0 && hh == 0) { v[0][0] &= 0xffffu; v[1][0] = v288; }
  f32x16 re = {}, im = {};
#pragma unroll
  for (int u = 0; u < 2; ++u) {
    u32x4 a_r, a_i;
#pragma unroll
    for (int e = 0; e < 4; ++e) {
      a_r[e] = ok ? __builtin_amdgcn_perm(v[u][2 * e + 1], v[u][2 * e], 0x05040100u) : 0u;
      a_i[e] = ok ? (__builtin_amdgcn_perm(v[u][2 * e + 1], v[u][2 * e], 0x07060302u) ^ (u ? 0x80008000u : 0u)) : 0u;
    }
    cm_dAc(re, im, a_r, a_i, ef_load(kt + 2048 + u * 3072));
  }
  const u32x4 cr = *(const u32x4*)kt, ci = *(const u32x4*)(kt + 1024);
  tw_apply(re, im, tw);
#pragma unroll
  for (int u = 0; u < 2; ++u) {
    f32x16 y = {};
    y = mfma32<true>(cr, pack8_f16(re, u), y);
    y = mfma32<true>(ci, pack8_f16(im, u), y);
    float* o = (float*)(orow + u * FIN_OS) + (lane & 31) + 128 * hh;
#pragma unroll
    for (int rho = 0; rho < 8; ++rho) o[32 * ((rho & 3) + 8 * (rho >> 2))] = y[rho];
    if (hh == 0) { o[32 * 16] = y[8]; o[32 * 17] = y[9]; }
  }
}

DI unsigned wmax_exp(const char* ws, int lane) {
  const u32x4 mv = load_dev(ws, OFF_WMAX + lane * 16);
  unsigned mx = max(max(mv[0], mv[1]), max(mv[2], mv[3]));
#pragma unroll
  for (int o = 32; o; o >>= 1) mx = max(mx, (unsigned)__shfl_xor((int)mx, o));
  return min(mx >> 7, 252u);
}

DI unsigned lds_arrive(char* p) { return __hip_atomic_fetch_add((LAS unsigned*)p, 1u, __ATOMIC_RELAXED, __HIP_MEMORY_SCOPE_WORKGROUP); }
DI void lds_arrive_wait(char* p, unsigned n, int lane) {
  if (lane == 0) lds_arrive(p);
  while (__hip_atomic_load((LAS unsigned*)p, __ATOMIC_RELAXED, __HIP_MEMORY_SCOPE_WORKGROUP) < n) __builtin_amdgcn_s_sleep(1);
  asm volatile("" ::: "memory");
}
DI void lds_wait(const char* p) {
  while (__hip_atomic_load((const LAS unsigned*)p, __ATOMIC_RELAXED, __HIP_MEMORY_SCOPE_WORKGROUP) == 0u) __builtin_amdgcn_s_sleep(1);
  asm volatile("" ::: "memory");
}
using CG1 = GC<1, 3, 4, 1, TR128, LIN, 5, 2, 3>;
using CG2 = GC<3, 2, 2, 2, LIN, TR128, 4, 2, 2>;
constexpr int P1_RS = 1216, P1_XS = 3 * CG1::STAGE, P1_HS = P1_XS + 8 * P1_RS, P1_C1 = P1_XS + 16 * P1_RS, P1_E = P1_C1 + 6144,
              P1_TW = P1_E + 6144, P1_O = P1_TW + 8192, P1_WT = P1_O + 9216, P1_RED = P1_WT + 10512, P1_DUMMY = 147456;
static_assert(P1_RED + 48 <= P1_DUMMY && P1_DUMMY >= CG1::LDS_BYTES, "");

__global__ __launch_bounds__(768, 3) void k_all(const u16* __restrict__ x, const u16* __restrict__ wr, const u16* __restrict__ wi,
                                                const char* __restrict__ w1, const u16* __restrict__ b1, const char* __restrict__ w2,
                                                const u16* __restrict__ b2, char* __restrict__ ws, u16* __restrict__ out) {
  extern __shared__ __attribute__((aligned(16))) char smem[];
  const int lane = threadIdx.x & 63, wave = __builtin_amdgcn_readfirstlane(threadIdx.x >> 6);
  const int b = blockIdx.x, g = b & 7, idx = b >> 3;
  const unsigned ldsb = (unsigned)(size_t)(LAS char*)smem;
  const u32x4 rs_ws = mk_rsrc(ws, WS_END), rs_w1 = mk_rsrc(w1, 18874368u), rs_w2 = mk_rsrc(w2, 18874368u);
  if (wave == 0) DBG(0);
  const int tile = g * 27 + idx, ft = tile / 6, dt1 = tile - ft * 6;
  const int ft0 = (g * 27) / 6, nl1 = ((g * 27 + 26) / 6 - ft0 + 1) * 2;
  constexpr int PF1_ROWS = 192 + (PF1_I * 128 * 64) / 10;
  auto issue_w1x = [&](int lw, int kt, int so) {
    constexpr unsigned ld = F_ * 2;
    const unsigned vo = CG1::voff<TR128>(lane, 64 * ld);
#pragma unroll
    for (int m = 0; m < 4; ++m) {
      const int a = lw * 4 + m;
      dma16<0>(rs_w1, ldsb + so + a * 1024, vo, (unsigned)(((4 * a) & 31) * 64 + 2 * kt + (a >> 3)) * ld + ft * 256u);
    }
  };
  const int tb = (g >> 2) * 128 + idx * 4 + (g & 3);
  u64 tag;
  if (wave < 4) {
    const int tid = threadIdx.x, s2 = tb >> 2, t = (tb >> 1) & 1, h = tb & 1;
    const int rr = tid >> 5, l32 = tid & 31;
    const int s1 = 16 * t + 8 * (rr >> 2) + 4 * h + (rr & 3);
    const unsigned* xr = (const unsigned*)(x + (size_t)(64 * s1 + s2) * D_);
    unsigned raw[9];
#pragma unroll
    for (int i = 0; i < 9; ++i) raw[i] = xr[32 * i + l32];
    const u64 ep = __hip_atomic_load((gu64*)(ws + SY_EPOCH), RLX_AGENT);
    float v[18];
#pragma unroll
    for (int i = 0; i < 9; ++i) { v[2 * i] = lo16(raw[i]); v[2 * i + 1] = hi16(raw[i]); }
#pragma unroll
    for (int pass = 0; pass < 2; ++pass) {
      float sm = 0.f;
#pragma unroll
      for (int i = 0; i < 18; ++i) sm += v[i];
      const float mean = sum32(sm) * (1.0f / 576.0f);
      float sq = 0.f;
#pragma unroll
      for (int i = 0; i < 18; ++i) { float dlt = v[i] - mean; sq += dlt * dlt; }
      const float var = sum32(sq) * (1.0f / 576.0f);
      const float mb = rbf(mean);
      const float sd = rbf(__builtin_sqrtf(rbf(rbf(var) + 1e-5f)));
      const float rs = 1.0f / sd;
      char* dst = smem + P1_XS + pass * (8 * P1_RS) + rr * P1_RS + l32 * 4;
#pragma unroll
      for (int i = 0; i < 9; ++i) {
        unsigned p = pack_bf16(v[2 * i] - mb, v[2 * i + 1] - mb);
        p = pack_bf16(lo16(p) * rs, hi16(p) * rs);
        v[2 * i] = lo16(p); v[2 * i + 1] = hi16(p);
        *(unsigned*)(dst + i * 128) = pass == 0 ? pack_f16(v[2 * i], v[2 * i + 1]) : p;
      }
    }
    tag = ((u64)__builtin_amdgcn_readfirstlane((unsigned)(ep >> 32)) << 32 | (unsigned)__builtin_amdgcn_readfirstlane((unsigned)ep)) *
              6364136223846793005ull + 1442695040888963407ull;
    if (tid == 0) *(u64*)(smem + P1_RED + 24) = tag;
    WAIT_LGKM0();
    if (wave == 0) DBG(10);
    BAR();
    if (wave == 0) DBG(13);
    {
      const int hh = lane >> 5, blk = (lane >> 4) & 1, q = (lane & 15) >> 2, p = lane & 3;
      const char* xa = smem + P1_XS + (2 * wave) * P1_RS + blk * 32 + p * 8;
      const char* cf = smem + P1_C1 + lane * 16;
      f32x16 re = {}, im = {};
      f32x4 tw[8];
      tw_load(tw, smem + P1_TW + lane * 16);
#pragma unroll
      for (int ks = 0; ks < 3; ++ks) {
        const char* a0 = ks < 2 ? xa + ks * P1_RS + (4 * hh + q) * 64 : xa + (q >> 1) * P1_RS + (16 + (q & 1)) * 64;
        const u32x4 a = tr_frag(a0, ks < 2 ? a0 + 512 : a0);
        re = mfma32<true>(a, *(const u32x4*)(cf + (2 * ks) * 1024), re);
        im = mfma32<true>(a, *(const u32x4*)(cf + (2 * ks + 1) * 1024), im);
      }
      tw_apply(re, im, tw);
      f32x16 xr_ = {}, xi_ = {};
#pragma unroll
      for (int u = 0; u < 2; ++u) cm_dB(xr_, xi_, pack8_f16(re, u), pack8_f16(im, u), ef_load(smem + P1_E + u * 3072 + lane * 16));
      const int col = lane & 31, k1 = col & 15, row2 = 2 * (2 * wave + (col >> 4));
      if (k1 < 10) {
        char* o1 = smem + P1_O + 32 * k1 + 576 * 4 * hh + row2;
        char* o2 = smem + P1_O + 32 * (576 - k1) - 576 * (4 * hh + 27) + row2;
#pragma unroll
        for (int rho = 0; rho < 8; ++rho) {
          const unsigned pk = pack_f16(xr_[rho] * (1.0f / 32.0f), xi_[rho] * (1.0f / 32.0f));
          char* d = o1 + 576 * ((rho & 3) + 8 * (rho >> 2));
          *(u16*)d = (u16)pk;
          if (rho != 0 || k1 != 0 || hh != 0) *(u16*)(d + 16) = (u16)(pk >> 16);
        }
        if (k1 >= 1 && k1 <= 8) {
#pragma unroll
          for (int rho = 8; rho < 16; ++rho) {
            const unsigned pk = pack_f16(xr_[rho] * (1.0f / 32.0f), xi_[rho] * (-1.0f / 32.0f));
            char* d = o2 + 576 * (27 - ((rho & 3) + 8 * (rho >> 2)));
            *(u16*)d = (u16)pk;
            *(u16*)(d + 16) = (u16)(pk >> 16);
          }
        } else if (k1 == 0 && hh == 0) {
          *(u16*)(smem + P1_O + 16 + row2) = (u16)pack_f16(xr_[8] * (1.0f / 32.0f), 0.f);
        }
      }
    }
    if (wave == 0) DBG(14);
    WAIT_LGKM0();
    lds_arrive_wait(smem + P1_RED + 32, 4u, lane);
    const unsigned P = ((s2 & 1) * 2 + t) * 64 + h * 32 + (s2 >> 1);
#pragma unroll
    for (int it = 0; it < 3; ++it) {
      const int p = tid + it * 256;
      if (p < 576) store_dev(ws, OFF_X1 + P * 9216u + p * 16, *(const u32x4*)(smem + P1_O + p * 16));
    }
    WAIT_VM(0);
    if (lane == 0 && lds_arrive(smem + P1_RED + 36) == 8u) set_flag(ws, SY_A + b * 8, tag);
  } else if (wave < 8) {
    const int tid = threadIdx.x - 256;
    const int rg = b >> 2, cg = b & 3, c0 = cg * 72 + (cg > 0), nc = cg == 0 ? 73 : 72, kq = rg >> 2, ko = rg & 3;
    unsigned rv[10], iv[10];
#pragma unroll
    for (int it = 0; it < 10; ++it) {
      const int i = tid + it * 256, row = i / 73, col = i - row * 73;
      const bool ok = i < 32 * 73 && col < nc;
      const int off = ok ? (8 * ko + (row & 7) + 32 * (4 * kq + (row >> 3))) * NC_ + c0 + col : 0;
      rv[it] = wr[off]; iv[it] = wi[off];
    }
    const u64 ep = __hip_atomic_load((gu64*)(ws + SY_EPOCH), RLX_AGENT);
    unsigned mx = 0;
#pragma unroll
    for (int it = 0; it < 10; ++it) {
      const int i = tid + it * 256, row = i / 73, col = i - row * 73;
      if (i < 32 * 73 && col < nc) {
        mx = max(mx, max(rv[it] & 0x7fffu, iv[it] & 0x7fffu));
        *(unsigned*)(smem + P1_WT + col * 144 + (row & 7) * 16 + (row >> 3) * 4) = rv[it] | (iv[it] << 16);
      }
    }
#pragma unroll
    for (int o = 32; o; o >>= 1) mx = max(mx, (unsigned)__shfl_xor((int)mx, o));
    unsigned* red = (unsigned*)(smem + P1_RED);
    if (lane == 0) red[wave - 4] = mx;
    tag = ((u64)__builtin_amdgcn_readfirstlane((unsigned)(ep >> 32)) << 32 | (unsigned)__builtin_amdgcn_readfirstlane((unsigned)ep)) *
              6364136223846793005ull + 1442695040888963407ull;
    WAIT_LGKM0();
    if (wave == 4) DBG(11);
    BAR();
    if (tid == 0) __hip_atomic_store((gu32*)(ws + OFF_WMAX + b * 4), max(max(red[0], red[1]), max(red[2], red[3])), RLX_AGENT);
    const int U = kq >> 3, h = kq & 1, rq = (kq >> 1) & 3;
#pragma unroll
    for (int it = 0; it < 3; ++it) {
      const int p = tid + it * 256;
      if (p < nc * 8) {
        const int col = p >> 3, a = p & 7;
        store_dev(ws, OFF_WT + (unsigned)(c0 + col) * 8192u + ((U * 4 + rq) * 64 + h * 32 + 8 * ko + a) * 16,
                  *(const u32x4*)(smem + P1_WT + col * 144 + a * 16));
      }
    }
    WAIT_VM(0);
    if (lane == 0 && lds_arrive(smem + P1_RED + 36) == 8u) set_flag(ws, SY_A + b * 8, tag);
#pragma unroll
    for (int i = 0; i < PF1_I; ++i)
      dma4(rs_w1, ldsb + P1_DUMMY + wave * 256, w1_line(((i * 32 + idx) * 4 + (wave - 4)) * 64 + lane, (PF1_ROWS - 192) * nl1, 192, nl1, ft0 * 256u));
  } else {
#pragma unroll
    for (int z = 0; z < 5; ++z) {
      const int fr = (wave - 8) + 4 * z;
      u32x4 val;
      const int col = lane & 31, hh = lane >> 5, r = col >> 4, k1 = col & 15;
      if (fr < 6) {
        const int ks = fr >> 1, which = fr & 1;
        float o[8];
#pragma unroll
        for (int j = 0; j < 8; ++j) {
          int row, d1;
          if (ks < 2) { row = ks; d1 = 8 * (j >> 2) + 4 * hh + (j & 3); }
          else { row = (hh == 0 && j < 4) ? (j >> 1) : 2; d1 = 16 + (j & 1); }
          const float v = cs(which, (float)((d1 * k1) % 18) * (1.0f / 18.0f));
          o[j] = (row == r && k1 < 10) ? v : 0.f;
        }
        val = u32x4{pack_f16(o[0], o[1]), pack_f16(o[2], o[3]), pack_f16(o[4], o[5]), pack_f16(o[6], o[7])};
      } else if (fr < 12) {
        val = e32_frag(fr - 6, lane);
      } else {
#pragma unroll
        for (int y = 0; y < 2; ++y) {
          const int rho = 2 * (fr - 12) + y, d2 = (rho & 3) + 8 * (rho >> 2) + 4 * hh;
          const float a = (float)(d2 * k1) * (1.0f / 576.0f);
          val[y] = __float_as_uint(__builtin_amdgcn_cosf(a)); val[2 + y] = __float_as_uint(-__builtin_amdgcn_sinf(a));
        }
      }
      *(u32x4*)(smem + P1_C1 + fr * 1024 + lane * 16) = val;
      if (z == W1_Z && idx < 27) { issue_w1x(wave - 8, 0, 0); issue_w1x(wave - 8, 1, CG1::STAGE); issue_w1x(wave - 8, 2, 2 * CG1::STAGE); }
    }
    if (threadIdx.x == 512) { *(unsigned*)(smem + P1_RED + 16) = 0u; *(u64*)(smem + P1_RED + 32) = 0ull; *(unsigned*)(smem + P1_RED + 40) = 0u; }
    if (wave == 8 && b < 66) {
      const int r = lane & 31, h = lane >> 5;
      u32x4 val;
      if (b < 6) val = e32_frag(b, lane);
      else if (b < 18) {
        const bool inv = b >= 12;
        const int f2 = inv ? b - 12 : b - 6, u = f2 / 3, which = f2 - 3 * u;
        float o[8];
#pragma unroll
        for (int j = 0; j < 8; ++j) {
          const int kap = 16 * u + 8 * (j >> 2) + 4 * h + (j & 3);
          o[j] = cs(which, (float)((inv ? kap * (2 * r + 1) : r * (2 * kap + 1)) & 63) * (1.0f / 64.0f));
        }
        val = u32x4{pack_f16(o[0], o[1]), pack_f16(o[2], o[3]), pack_f16(o[4], o[5]), pack_f16(o[6], o[7])};
      } else if (b < 50) {
        const bool inv = b >= 34;
        const int q = inv ? b - 34 : b - 18, p = q & 7, T = q >> 3;
#pragma unroll
        for (int z = 0; z < 2; ++z) {
          const int rho = 2 * p + z, cr = (rho & 3) + 8 * (rho >> 2) + 4 * h;
          const int n = inv ? (2 * r + T) * cr : (2 * cr + T) * r;
          const float a = (float)(n & 2047) * (1.0f / 2048.0f);
          val[z] = __float_as_uint(__builtin_amdgcn_cosf(a) * (inv ? 0.125f : 1.0f));
          val[2 + z] = __float_as_uint(inv ? __builtin_amdgcn_sinf(a) * 0.125f : -__builtin_amdgcn_sinf(a));
        }
      } else if (b < 52) {
        float o[8];
#pragma unroll
        for (int j = 0; j < 8; ++j) {
          const int k1 = 8 * (j >> 2) + 4 * h + (j & 3);
          const float v = cs(b - 50, (float)((r * k1) % 18) * (1.0f / 18.0f)) * ((k1 == 0 || k1 == 9) ? 1.f : 2.f);
          o[j] = (k1 < 10 && r < 18) ? v : 0.f;
        }
        val = u32x4{pack_f16(o[0], o[1]), pack_f16(o[2], o[3]), pack_f16(o[4], o[5]), pack_f16(o[6], o[7])};
      } else if (b < 58) {
        val = e32_frag(b - 52, lane);
      } else {
#pragma unroll
        for (int y = 0; y < 2; ++y) {
          const int rho = 2 * (b - 58) + y, k1 = ((rho & 3) + 8 * (rho >> 2) + 4 * h) & 15;
          const float a = (float)(k1 * r) * (1.0f / 576.0f);
          val[y] = __float_as_uint(__builtin_amdgcn_cosf(a) * 0.125f); val[2 + y] = __float_as_uint(__builtin_amdgcn_sinf(a) * 0.125f);
        }
      }
      store_dev(ws, (b < 50 ? OFF_TAB + b * 1024u : OFF_RT + (b - 50) * 1024u) + lane * 16, val);
    }
    WAIT_LGKM0();
    if (wave == 8) DBG(12);
    BAR();
    { const u64 tv = *(const u64*)(smem + P1_RED + 24);
      tag = (u64)__builtin_amdgcn_readfirstlane((unsigned)(tv >> 32)) << 32 | (unsigned)__builtin_amdgcn_readfirstlane((unsigned)tv); }
    {
      const int s2 = tb >> 2, t = (tb >> 1) & 1, h = tb & 1;
      for (int cb = wave - 8; cb < 9; cb += 4) {
        const int gg = lane >> 4, q = (lane & 15) >> 2, pp = lane & 3;
        const char* src = smem + P1_HS + q * P1_RS + (cb * 64 + gg * 16 + pp * 4) * 2;
        const int df = cb * 2 + (gg >> 1), i = 8 * q + 4 * (gg & 1) + pp;
        store_dev(ws, OFF_H + (unsigned)(df * 128 + 2 * s2 + t) * 1024u + (h * 32 + i) * 16, tr_frag(src, src + 4 * P1_RS));
      }
      WAIT_VM(0);
      unsigned last = 0;
      if (lane == 0) last = lds_arrive(smem + P1_RED + 16);
      if (__builtin_amdgcn_readfirstlane(last) == 3u) { if (lane < 8) set_flag(ws, SY_H + lane * 2048 + tb * 8, tag); DBG(1); }
      if (wave == 8 && lane == 0 && lds_arrive(smem + P1_RED + 36) == 8u) set_flag(ws, SY_A + b * 8, tag);
    }
  }
  if (wave == 0) DBG(2);
  if (idx < 27) {
    char* ebuf = smem + (29 % 5) * CG1::STAGE;
    u32x2 bv[3][2];
#pragma unroll
    for (int it = 0; it < 3; ++it) {
      const u32x4* bp = (const u32x4*)(b1 + ft * 128 + (min(wave + 11 * it, 23) / 3) * 16);
      const u32x4 b0 = bp[0], b1v = bp[1];
      const bool hi = lane >= 32;
      bv[it][0] = u32x2{hi ? b0[2] : b0[0], hi ? b0[3] : b0[1]}; bv[it][1] = u32x2{hi ? b1v[2] : b1v[0], hi ? b1v[3] : b1v[1]};
      asm volatile("" : "+v"(bv[it][0]), "+v"(bv[it][1]));
    }
    if (wave == 11) {
      const unsigned fh = SY_H + g * 2048 + lane * 8;
      poll_flags<1>(ws, fh, true, tag);
      if (lane == 0) lds_arrive(smem + P1_RED + 40);
      bool ok[3];
#pragma unroll
      for (int q = 0; q < 3; ++q) ok[q] = __all(__hip_atomic_load((gu64*)(ws + fh + (q + 1) * 512), RLX_AGENT) == tag);
      for (int tau = 0; tau < 32; ++tau) {
#pragma unroll
        for (int q = 0; q < 3; ++q)
          if (tau == 8 * q + 5 && !ok[q]) poll_flags<1>(ws, fh + (q + 1) * 512, true, tag);
        if (tau == 21 && b == 0 && lane == 0) set_flag(ws, SY_EPOCH, tag);
        BAR();
        if (tau == PF2_TAU) {
#pragma unroll
          for (int i = 0; i < PF2T; ++i) {
            const int line = (i * 27 + idx) * 64 + lane;
            int row = (line >> 4) + (g & 1) * 576; if (row >= 1152) row -= 1152;
            dma4(rs_w2, ldsb + P1_DUMMY + wave * 256, (unsigned)((g >> 1) * 1152 + row) * 4096u + (g & 1) * 2048u + (line & 15) * 128u);
          }
        }
      }
    } else if (wave >= 8) {
      auto issue_h = [&](int kt, int so) {
#pragma unroll
        for (int m = 0; m < 4; ++m)
          dma16<2>(rs_ws, ldsb + so + CG1::A_BYTES + ((wave - 8) * 4 + m) * 1024, lane * 16,
                   OFF_H + (unsigned)((dt1 * 3 + (wave - 8)) * 128 + kt * 4 + m) * 1024u);
      };
      lds_wait(smem + P1_RED + 40);
      if (wave == 8) DBG(3);
      issue_h(0, 0); issue_h(1, CG1::STAGE); issue_h(2, 2 * CG1::STAGE);
      CG1::ring<4>(32, issue_h);
    } else if (wave >= 4) {
      CG1::ring<4>(32, [&](int kt, int so) { issue_w1x(wave - 4, kt, so); });
    } else {
      f32x16 acc[1][3] = {};
      int rslot = 0;
      CG1::consume<false>(smem, wave, lane, 32, rslot, acc);
      if (wave == 0) DBG(4);
#pragma unroll
      for (int j = 0; j < 3; ++j)
#pragma unroll
        for (int s = 0; s < 2; ++s) {
          u32x4 val;
#pragma unroll
          for (int e = 0; e < 4; ++e) val[e] = pack_bf16(acc[0][j][8 * s + 2 * e], acc[0][j][8 * s + 2 * e + 1]);
          *(u32x4*)(ebuf + ((wave * 2 + s) * 3 + j) * 1024 + lane * 16) = val;
        }
    }
    WAIT_LGKM0();
    BAR();
    if (wave == 0) DBG(15);
    if (wave < 11) {
#pragma unroll
      for (int it = 0; it < 3; ++it) {
        const int i = wave + 11 * it;
        if (i < 24) {
          const int z = i / 3, j = i - 3 * z;
          const u32x4 a = *(const u32x4*)(ebuf + i * 1024 + lane * 16);
          u32x4 val;
#pragma unroll
          for (int e = 0; e < 4; ++e) {
            const unsigned bb = bv[it][e >> 1][e & 1];
            const unsigned pk = pack_bf16(lo16(a[e]) + lo16(bb), hi16(a[e]) + hi16(bb));
            val[e] = pack_bf16(gelu_tanh(lo16(pk)), gelu_tanh(hi16(pk)));
          }
          store_dev(ws, OFF_U + (unsigned)((dt1 * 3 + j) * 288 + ft * 8 + z) * 1024u + lane * 16, val);
        }
      }
      WAIT_VM(0);
    } else if (lane == 0) *(unsigned*)(smem + 163832) = 0u;
    WAIT_LGKM0();
    BAR();
    if (threadIdx.x < 2) set_flag(ws, SY_U + threadIdx.x * 2048 + tile * 8, tag);
  } else {
    const int me = (idx - 27) * 8 + g;
    {
      const int total = (2048 - PF1_ROWS) * nl1;
      for (int i0 = ((idx - 27) * 12 + wave) * 64; i0 < total; i0 += 60 * 64) {
        dma4(rs_w1, ldsb + P1_DUMMY + wave * 256, w1_line(i0 + lane, total, PF1_ROWS, nl1, ft0 * 256u));
        WAIT_VM(7);
      }
    }
    for (int i0 = ((idx - 27) * 12 + wave) * 64; i0 < PF2_ROWS * 16; i0 += 60 * 64) {
      const int line = i0 + lane;
      int row = (line >> 4) + (g & 1) * 576; if (row >= 1152) row -= 1152;
      dma4(rs_w2, ldsb + P1_DUMMY + wave * 256, (unsigned)((g >> 1) * 1152 + row) * 4096u + (g & 1) * 2048u + (line & 15) * 128u);
      WAIT_VM(7);
    }
    if (wave == 0) DBG(3);
    if (me < 37) {
      if (wave == 8) poll_flags<4>(ws, SY_A + lane * 8, true, tag);
      BAR();
      const int cbk = me == 36 ? 0 : me;
      char* img = smem + 65536;
      for (int i = wave; i < 64; i += 12) {
        const int P = 4 * i + (lane >> 4);
        dma16<2>(rs_ws, ldsb + 65536 + i * 1024, (unsigned)P * 9216u + (unsigned)(((lane & 15) ^ (P & 15)) * 16), OFF_X1 + cbk * 256u);
      }
      for (int i = wave; i < TAB_BYTES / 1024; i += 12) dma16<2>(rs_ws, ldsb + i * 1024, lane * 16, OFF_TAB + i * 1024u);
      const bool act = me == 36 ? wave == 0 : wave < 8;
      const int c = me == 36 ? 288 : me * 8 + wave;
      const float wsc = __uint_as_float((253u - wmax_exp(ws, lane)) << 23) * (1.0f / 64.0f);
      WAIT_VM(0);
      BAR();
      const int slot_re = me == 36 ? 1 : 2 * wave, slot_im = c == 288 ? -2 : (c == 0 ? -1 : 2 * wave + 1);
      u32x4 zr[2][2], zi[2][2];
      if (act) {
#pragma unroll
        for (int z = 0; z < 4; ++z) {
          const char* p = img + (z * 64 + lane) * 256;
          zr[z >> 1][z & 1] = *(const u32x4*)(p + ((slot_re ^ (lane & 15)) * 16));
          zi[z >> 1][z & 1] = slot_im >= 0 ? *(const u32x4*)(p + ((slot_im ^ (lane & 15)) * 16)) : u32x4{0, 0, 0, 0};
        }
      }
      WAIT_LGKM0();
      BAR();
      if (act) colfft_wave(smem + lane * 16, ws, c, lane, wsc, zr, zi, img, slot_re, slot_im);
      WAIT_LGKM0();
      BAR();
      if (me == 36) {
        if (wave < 4) store_dev(ws, OFF_Y288 + wave * 1024 + lane * 16, *(const u32x4*)(img + wave * 1024 + lane * 16));
      } else {
        for (int i = wave; i < 64; i += 12) {
          const int Qy = 4 * i + (lane >> 4);
          store_dev(ws, OFF_Y1 + (unsigned)Qy * 9216u + cbk * 256 + (lane & 15) * 16,
                    *(const u32x4*)(img + Qy * 256 + (((lane & 15) ^ ((Qy >> 3) & 15)) * 16)));
        }
      }
      WAIT_VM(0);
      BAR();
      if (threadIdx.x == 0) set_flag(ws, SY_Y + me * 8, tag);
    }
  }
  if (wave == 0) DBG(5);
  if (idx < 24) {
    const int ks = g >> 1, dt = idx % 3, st = (g & 1) * 8 + idx / 3;
    const int rot = (g & 1) * 9;
    const unsigned myflags = SY_T + (unsigned)((st * 4 + ks) * 3 + dt) * 64u;
    if (wave >= 8) {
      auto issue_u = [&](int k0, int so) {
        const int kt = k0 + rot < 18 ? k0 + rot : k0 + rot - 18;
        if (k0 == 0 || k0 == 8) {
          const int kf = k0 == 0 ? rot : 9 - rot;
          if (wave == 8) {
            poll_flags<1>(ws, SY_U + (g & 1) * 2048 + (unsigned)((9 * ks + (kf >> 1) + (lane >> 1)) * 6 + 2 * dt + (lane & 1)) * 8u, lane < 10, tag);
            if (k0 == 0) { if (lane == 0) lds_arrive(smem + 163832); DBG(6); }
          } else if (k0 == 0) lds_wait(smem + 163832);
        }
#pragma unroll
        for (int m = 0; m < 6; ++m) {
          const int a = (wave - 8) * 6 + m;
          dma16<2>(rs_ws, ldsb + so + a * 1024, lane * 16, OFF_U + (unsigned)((dt * 6 + (a >> 2)) * 288 + (ks * 18 + kt) * 4 + (a & 3)) * 1024u);
        }
      };
      issue_u(0, 0); issue_u(1, CG2::STAGE);
      CG2::ring<6>(18, issue_u);
      BAR();
      if (wave == 8) poll_flags<1>(ws, myflags + (lane & 3) * 8, true, tag);
      BAR();
      return;
    }
    if (wave >= 4) {
      auto issue_w2 = [&](int k0, int so) {
        const int kt = k0 + rot < 18 ? k0 + rot : k0 + rot - 18;
        constexpr unsigned ld = S_ * 2;
        const unsigned vo = CG2::voff<TR128>(lane, ld);
#pragma unroll
        for (int m = 0; m < 4; ++m) {
          const int a = (wave - 4) * 4 + m;
          const unsigned sof = (unsigned)((ks * 18 + kt) * 64 + 4 * a) * ld + st * 256u;
          if (k0 >= NTF2) dma16<1>(rs_w2, ldsb + so + CG2::A_BYTES + a * 1024, vo, sof);
          else dma16<0>(rs_w2, ldsb + so + CG2::A_BYTES + a * 1024, vo, sof);
        }
      };
      issue_w2(0, 0); issue_w2(1, CG2::STAGE);
      CG2::ring<4>(18, issue_w2);
      BAR();
      BAR();
      return;
    }
    f32x16 acc[3][2] = {};
    int rslot = 0;
    CG2::consume<false>(smem, wave, lane, 18, rslot, acc);
    if (wave == 0) DBG(7);
    const int wa = wave >> 1, wb = wave & 1;
    const bool own0 = wb * 2 == ks, own1 = wb * 2 + 1 == ks;
#pragma unroll
    for (int j = 0; j < 2; ++j) {
      if (j ? own1 : own0) continue;
#pragma unroll
      for (int i = 0; i < 3; ++i) {
        const unsigned o = OFF_SLAB + (unsigned)((ks * 64 + st * 4 + wb * 2 + j) * 18 + dt * 6 + wa * 3 + i) * 2048u + lane * 16;
#pragma unroll
        for (int u = 0; u < 2; ++u) {
          u32x4 val;
#pragma unroll
          for (int e = 0; e < 4; ++e) val[e] = pack_bf16(acc[i][j][8 * u + 2 * e], acc[i][j][8 * u + 2 * e + 1]);
          store_dev(ws, o + u * 1024, val);
        }
      }
    }
    WAIT_VM(0);
    BAR();
    if (wave == 0 && lane < 4 && lane != ks) set_flag(ws, SY_T + (unsigned)((st * 4 + lane) * 3 + dt) * 64u + ks * 8, tag);
    BAR();
    if (wave == 0) DBG(8);
    if (!own0 && !own1) return;
    const int sf = st * 4 + ks, s = sf * 32 + (lane & 31);
    auto finish = [&](auto J) {
      constexpr int j = decltype(J)::value;
      u32x4 pv[3][2][4];
#pragma unroll
      for (int i = 0; i < 3; ++i)
#pragma unroll
        for (int u = 0; u < 2; ++u) {
          const unsigned o = (unsigned)(sf * 18 + dt * 6 + wa * 3 + i) * 2048u + u * 1024 + lane * 16;
#pragma unroll
          for (int q = 0; q < 3; ++q) pv[i][u][q] = load_dev(ws, OFF_SLAB + o + ((ks + 1 + q) & 3) * 2359296u);
          pv[i][u][3] = load_dev(ws, OFF_R + o);
        }
#pragma unroll
      for (int i = 0; i < 3; ++i)
#pragma unroll
        for (int u = 0; u < 2; ++u) {
          u32x4 o;
#pragma unroll
          for (int e = 0; e < 4; ++e)
            o[e] = pack_bf16(acc[i][j][8 * u + 2 * e] + (lo16(pv[i][u][0][e]) + lo16(pv[i][u][1][e])) + (lo16(pv[i][u][2][e]) + lo16(pv[i][u][3][e])),
                             acc[i][j][8 * u + 2 * e + 1] + (hi16(pv[i][u][0][e]) + hi16(pv[i][u][1][e])) + (hi16(pv[i][u][2][e]) + hi16(pv[i][u][3][e])));
          *(u32x4*)(out + (size_t)s * D_ + (dt * 6 + wa * 3 + i) * 32 + (lane >> 5) * 16 + u * 8) = o;
        }
    };
    if (own0) finish(IC<0>{}); else finish(IC<1>{});
  } else {
    const int me = (idx - 24) * 8 + g, sr = lane & 31;
    char* kt = smem + 37888; char* yb = kt + 17408;
    u32x4 xv[3];
#pragma unroll
    for (int it = 0; it < 3; ++it) xv[it] = *(const u32x4*)(x + (size_t)(me * 32 + sr) * D_ + ((it * 12 + wave) * 2 + (lane >> 5)) * 8);
    const float bias = lo16(b2[me * 32 + sr]);
    if (wave == 8) { poll_flags<1, 16>(ws, SY_Y + min(lane, 36) * 8, true, tag); DBG(6); }
    BAR();
#pragma unroll
    for (int it = 0; it < 3; ++it) dma16<2>(rs_ws, ldsb + (it * 12 + wave) * 1024, lane * 16, OFF_Y1 + me * 36864u + (it * 12 + wave) * 1024u);
    dma16<2>(rs_ws, ldsb + 37888 + wave * 1024, lane * 16, OFF_RT + wave * 1024u);
    if (wave < 4) dma16<2>(rs_ws, ldsb + 37888 + (12 + wave) * 1024, lane * 16, OFF_RT + (12 + wave) * 1024u);
    if (wave == 4) dma16<2>(rs_ws, ldsb + 37888 + 16384, (lane & 3) * 16, OFF_Y288 + me * 64u);
    const float fs = __uint_as_float((wmax_exp(ws, lane) + 1u) << 23) * (1.0f / 9.0f);
    WAIT_VM(0);
    BAR();
    for (int task = wave; task < 16; task += 12)
      irfft_wave(smem + (task >> 2) * 9216, kt + 16384 + (task >> 2) * 16, task & 3, lane, yb + task * 2 * FIN_OS, kt + lane * 16);
    WAIT_LGKM0();
    BAR();
#pragma unroll
    for (int it = 0; it < 3; ++it) {
      const int dp = (it * 12 + wave) * 2 + (lane >> 5);
      const char* yp = yb + sr * FIN_OS + dp * 32;
      const f32x4 y0 = *(const f32x4*)yp, y1 = *(const f32x4*)(yp + 16);
      u32x4 o;
#pragma unroll
      for (int e = 0; e < 4; ++e) {
        const float ya = e < 2 ? y0[2 * e] : y1[2 * e - 4], yc = e < 2 ? y0[2 * e + 1] : y1[2 * e - 3];
        o[e] = pack_bf16(lo16(xv[it][e]) + bias + ya * fs, hi16(xv[it][e]) + bias + yc * fs);
      }
      store_dev(ws, OFF_R + (unsigned)(me * 18 + (dp >> 2)) * 2048u + (dp & 1) * 1024 + (((dp >> 1) & 1) * 32 + sr) * 16, o);
    }
    WAIT_VM(0);
    BAR();
    if (wave == 0 && lane < 3) set_flag(ws, SY_T + (unsigned)(me * 3 + lane) * 64u + (me & 3) * 8, tag);
    if (wave == 0) DBG(9);
  }
}

extern "C" void kernel_launch(void* const* d_in, const int* in_sizes, int n_in, void* d_out, int out_size, void* d_ws, size_t ws_size,
                              hipStream_t stream) {
  (void)hipFuncSetAttribute((const void*)k_all, hipFuncAttributeMaxDynamicSharedMemorySize, 163840);
  k_all<<<256, 768, 163840, stream>>>((const u16*)d_in[0], (const u16*)d_in[1], (const u16*)d_in[2], (const char*)d_in[4], (const u16*)d_in[5],
                                      (const char*)d_in[6], (const u16*)d_in[7], (char*)d_ws + ((ws_size - WS_END) & ~(size_t)4095), (u16*)d_out);
}
```

```cpp
#define PF2T 2
#define PF2C_ROWS 488
#define NTF2 11
#include <hip/hip_runtime.h>
#include <hip/hip_bf16.h>
#include <stdint.h>

#define DI __device__ __forceinline__
#define LAS __attribute__((address_space(3)))
typedef unsigned short u16;
typedef unsigned long long u64;
typedef __attribute__((ext_vector_type(2))) float f32x2;
typedef __attribute__((ext_vector_type(4))) float f32x4;
typedef __attribute__((ext_vector_type(16))) float f32x16;
typedef __attribute__((ext_vector_type(2))) _Float16 f16x2;
typedef __attribute__((ext_vector_type(8))) _Float16 f16x8;
typedef __attribute__((ext_vector_type(2))) __bf16 bf16x2;
typedef __attribute__((ext_vector_type(8))) __bf16 bf16x8;
typedef __attribute__((ext_vector_type(4))) short s16x4;
typedef __attribute__((ext_vector_type(2))) unsigned u32x2;
typedef __attribute__((ext_vector_type(4))) unsigned u32x4;
typedef __attribute__((address_space(1))) unsigned gu32;
typedef __attribute__((address_space(1))) u64 gu64;
#define RLX_AGENT __ATOMIC_RELAXED, __HIP_MEMORY_SCOPE_AGENT

#define PF1_I 2
#define W1_Z 2
#define PF2_TAU 29
#define PF2_ROWS 0
#define GATE 1
#define WARM_MODE 1
#define PF1_W 1
#define PF2C_SLP 1
#define PF3_ROWS 0
#define POLL_SLEEP 4

constexpr int S_ = 2048, D_ = 576, F_ = 4608, NC_ = 289;

constexpr unsigned OFF_H    = 0;
constexpr unsigned OFF_WT   = OFF_H + 2359296;
constexpr unsigned OFF_TAB  = OFF_WT + 2367488;
constexpr unsigned OFF_RT   = OFF_TAB + 65536;
constexpr unsigned OFF_Y288 = OFF_RT + 16384;
constexpr unsigned OFF_WMAX = OFF_Y288 + 4096;
constexpr unsigned OFF_X1   = OFF_WMAX + 4096;
constexpr unsigned OFF_Y1   = OFF_X1 + 2359296;
constexpr unsigned OFF_U    = OFF_Y1 + 2359296;
constexpr unsigned OFF_SLAB = OFF_U + 5308416;
constexpr unsigned OFF_R    = OFF_SLAB + 9437184;
constexpr unsigned OFF_SYNC = OFF_R + 2359296;
constexpr unsigned SY_EPOCH = OFF_SYNC, SY_TMO = OFF_SYNC + 256, SY_A = OFF_SYNC + 2048, SY_Y = OFF_SYNC + 4096, SY_U = OFF_SYNC + 6144,
                   SY_H = SY_U + 2 * 2048, SY_T = SY_H + 8 * 2048;
constexpr unsigned WS_END   = SY_T + 192 * 64;
constexpr int TAB_E1 = 0, TAB_EO = 6144, TAB_EOI = 12288, TAB_TW1 = 18432, TAB_TW2 = 34816, TAB_BYTES = 51200;

DI unsigned pack_bf16(float a, float b) { f32x2 v = {a, b}; return __builtin_bit_cast(unsigned, __builtin_convertvector(v, bf16x2)); }
DI unsigned pack_f16(float a, float b) { f32x2 v = {a, b}; return __builtin_bit_cast(unsigned, __builtin_convertvector(v, f16x2)); }
DI float lo16(unsigned p) { return __uint_as_float(p << 16); }
DI float hi16(unsigned p) { return __uint_as_float(p & 0xffff0000u); }
DI float rbf(float a) { return lo16(pack_bf16(a, a)); }

template <int CTRL> DI float dpp_add(float v) {
  return v + __int_as_float(__builtin_amdgcn_update_dpp(0, __float_as_int(v), CTRL, 0xf, 0xf, true));
}
DI float sum32(float v) {
  v = dpp_add<0xB1>(v); v = dpp_add<0x4E>(v); v = dpp_add<0x141>(v); v = dpp_add<0x140>(v);
  auto r = __builtin_amdgcn_permlane16_swap(__float_as_uint(v), __float_as_uint(v), false, false);
  return __uint_as_float(r[0]) + __uint_as_float(r[1]);
}

#define DBG(i)
#define WAIT_VM(n) asm volatile("s_waitcnt vmcnt(%0)" ::"n"(n) : "memory")
#define WAIT_LGKM0() asm volatile("s_waitcnt lgkmcnt(0)" ::: "memory")
#define BAR() do { asm volatile("" ::: "memory"); __builtin_amdgcn_s_barrier(); asm volatile("" ::: "memory"); } while (0)
#define SCHED0() __builtin_amdgcn_sched_barrier(0)

DI u32x4 mk_rsrc(const void* p, unsigned bytes) {
  const u64 a = (u64)p;
  return u32x4{(unsigned)a, (unsigned)(a >> 32) & 0xffffu, bytes, 0x00020000u};
}
template <int POL> DI void dma16(u32x4 rsrc, unsigned lds_off, unsigned voff, unsigned soff) {
  if constexpr (POL == 0) asm volatile("s_mov_b32 m0, %0\n\ts_nop 0\n\tbuffer_load_dwordx4 %1, %2, %3 offen lds" ::"s"(lds_off), "v"(voff), "s"(rsrc), "s"(soff) : "memory");
  if constexpr (POL == 1) asm volatile("s_mov_b32 m0, %0\n\ts_nop 0\n\tbuffer_load_dwordx4 %1, %2, %3 offen nt lds" ::"s"(lds_off), "v"(voff), "s"(rsrc), "s"(soff) : "memory");
  if constexpr (POL == 2) asm volatile("s_mov_b32 m0, %0\n\ts_nop 0\n\tbuffer_load_dwordx4 %1, %2, %3 offen sc1 lds" ::"s"(lds_off), "v"(voff), "s"(rsrc), "s"(soff) : "memory");
}
DI void dma4(u32x4 rsrc, unsigned lds_off, unsigned voff) {
  asm volatile("s_mov_b32 m0, %0\n\ts_nop 0\n\tbuffer_load_dword %1, %2, 0 offen lds" ::"s"(lds_off), "v"(voff), "s"(rsrc) : "memory");
}
DI void store_dev(char* ws, unsigned off, u32x4 v) {
  __builtin_amdgcn_raw_buffer_store_b128(v, __builtin_amdgcn_make_buffer_rsrc((void*)ws, 0, WS_END, 0x00020000), off, 0, 16);
}
DI u32x4 load_dev(const char* ws, unsigned off) {
  return __builtin_amdgcn_raw_buffer_load_b128(__builtin_amdgcn_make_buffer_rsrc((void*)ws, 0, WS_END, 0x00020000), off, 0, 16);
}
DI void set_flag(char* ws, unsigned off, u64 tag) { __hip_atomic_store((gu64*)(ws + off), tag, RLX_AGENT); }
template <int N, int SLP = POLL_SLEEP> DI void poll_flags(char* ws, unsigned off, bool active, u64 tag) {
  for (unsigned spins = 0;; ++spins) {
    bool ok = true;
#pragma unroll
    for (int i = 0; i < N; ++i) { const u64 v = __hip_atomic_load((gu64*)(ws + off + i * 512), RLX_AGENT); ok &= (v == tag) || !active; }
    if (__all(ok)) break;
    __builtin_amdgcn_s_sleep(SLP);
    if (spins > (1u << 17)) { set_flag(ws, SY_TMO, tag); break; }
    if ((spins & 63u) == 63u && __all(__hip_atomic_load((gu64*)(ws + SY_TMO), RLX_AGENT) == tag)) break;
  }
  __builtin_amdgcn_fence(__ATOMIC_ACQUIRE, "wavefront");
}
DI unsigned w1_line(int i, int total, int r0, int nl, unsigned c0) {
  const int rr = i / nl, l = i - rr * nl, row = r0 + rr;
  return i < total ? (unsigned)(64 * (row & 31) + (row >> 5)) * (F_ * 2) + c0 + (unsigned)l * 128u : 0xfffffff0u;
}

template <bool F16> DI f32x16 mfma32(u32x4 a, u32x4 b, f32x16 c) {
  if constexpr (F16) return __builtin_amdgcn_mfma_f32_32x32x16_f16(__builtin_bit_cast(f16x8, a), __builtin_bit_cast(f16x8, b), c, 0, 0, 0);
  else return __builtin_amdgcn_mfma_f32_32x32x16_bf16(__builtin_bit_cast(bf16x8, a), __builtin_bit_cast(bf16x8, b), c, 0, 0, 0);
}
DI u32x4 pack8_f16(const f32x16& v, int u) {
  return u32x4{pack_f16(v[8 * u], v[8 * u + 1]), pack_f16(v[8 * u + 2], v[8 * u + 3]), pack_f16(v[8 * u + 4], v[8 * u + 5]), pack_f16(v[8 * u + 6], v[8 * u + 7])};
}
DI u32x4 tr_frag(const char* p0, const char* p1) {
  s16x4 lo = __builtin_amdgcn_ds_read_tr16_b64_v4i16((LAS s16x4*)p0);
  s16x4 hi = __builtin_amdgcn_ds_read_tr16_b64_v4i16((LAS s16x4*)p1);
  u32x2 a = __builtin_bit_cast(u32x2, lo), c = __builtin_bit_cast(u32x2, hi);
  return u32x4{a[0], a[1], c[0], c[1]};
}
DI void tw_piece(f32x16& re, f32x16& im, int p, const f32x4 w) {
  const f32x2 r = {re[2 * p], re[2 * p + 1]}, i = {im[2 * p], im[2 * p + 1]}, wr = {w[0], w[1]}, wi = {w[2], w[3]};
  const f32x2 a = __builtin_elementwise_fma(r, wr, -(i * wi)), b = __builtin_elementwise_fma(r, wi, i * wr);
  re[2 * p] = a[0]; re[2 * p + 1] = a[1]; im[2 * p] = b[0]; im[2 * p + 1] = b[1];
}
DI void tw_load(f32x4 (&t)[8], const char* tw) {
#pragma unroll
  for (int p = 0; p < 8; ++p) t[p] = *(const f32x4*)(tw + p * 1024);
}
DI void tw_apply(f32x16& re, f32x16& im, const f32x4 (&t)[8]) {
#pragma unroll
  for (int p = 0; p < 8; ++p) tw_piece(re, im, p, t[p]);
}
DI float cs(int which, float rev) {
  return which == 0 ? __builtin_amdgcn_cosf(rev) : (which == 1 ? -__builtin_amdgcn_sinf(rev) : __builtin_amdgcn_sinf(rev));
}
DI u32x4 e32_frag(int frag, int lane) {
  const int t = frag / 3, which = frag - 3 * t, r = lane & 31, h = lane >> 5;
  float o[8];
#pragma unroll
  for (int j = 0; j < 8; ++j) o[j] = cs(which, (float)(((16 * t + 8 * (j >> 2) + 4 * h + (j & 3)) * r) & 31) * (1.0f / 32.0f));
  return u32x4{pack_f16(o[0], o[1]), pack_f16(o[2], o[3]), pack_f16(o[4], o[5]), pack_f16(o[6], o[7])};
}

enum { LIN = 0, TR128 = 1, TR64 = 2 };

template <int V> struct IC { static constexpr int value = V; };
template <int NA_W, int NB_W, int WA, int WB, int AK, int BK, int NS, int DPM, int DEPTH>
struct GC {
  static constexpr int NC = WA * WB, NFA = NA_W * WA, NFB = NB_W * WB;
  static constexpr int A_BYTES = NFA * 4096, B_BYTES = NFB * 4096, STAGE = A_BYTES + B_BYTES;
  static constexpr int LDS_BYTES = NS * STAGE;
  static_assert(AK == LIN || NFA == (AK == TR128 ? 4 : 2), "");
  static_assert(BK == LIN || NFB == (BK == TR128 ? 4 : 2), "");

  template <int K> static DI unsigned voff(int lane, unsigned rstride) {
    if constexpr (K == LIN) return lane * 16;
    else if constexpr (K == TR128) return (unsigned)(lane >> 4) * rstride + (unsigned)(((lane & 15) ^ ((lane >> 4) << 2)) * 16);
    else return (unsigned)(lane >> 3) * rstride + (unsigned)(((lane & 7) ^ (((lane >> 4) & 1) << 2)) * 16);
  }
  template <int K, int NF> static DI void frag_addr(unsigned (&addr)[K == LIN ? 1 : NF], int opoff, int wpos, int lane) {
    if constexpr (K == LIN) addr[0] = opoff + wpos * NF * 4096 + lane * 16;
    else {
      const int h = lane >> 5, blk = (lane >> 4) & 1, q = (lane & 15) >> 2, p = lane & 3;
#pragma unroll
      for (int i = 0; i < NF; ++i) {
        const int ch = 4 * (wpos * NF + i) + 2 * blk + (p >> 1);
        if constexpr (K == TR128) addr[i] = opoff + (4 * h + q) * 256 + ((ch ^ (q << 2)) * 16) + 8 * (p & 1);
        else addr[i] = opoff + (4 * h + q) * 128 + ((ch ^ (((q >> 1) & 1) << 2)) * 16) + 8 * (p & 1);
      }
    }
  }
  template <int K, int NF> static DI void rd(u32x4 (&f)[NF], const char* smem, const unsigned (&addr)[K == LIN ? 1 : NF], int sb, int hh) {
#pragma unroll
    for (int i = 0; i < NF; ++i) {
      if constexpr (K == LIN) f[i] = *(const u32x4*)(smem + addr[0] + sb + (i * 4 + hh) * 1024);
      else {
        constexpr int RB = K == TR128 ? 256 : 128;
        const char* p = smem + addr[i] + sb + hh * 16 * RB;
        s16x4 lo = __builtin_amdgcn_ds_read_tr16_b64_v4i16((LAS s16x4*)p);
        s16x4 hi = __builtin_amdgcn_ds_read_tr16_b64_v4i16((LAS s16x4*)(p + 8 * RB));
        u32x2 a = __builtin_bit_cast(u32x2, lo), c = __builtin_bit_cast(u32x2, hi);
        f[i] = u32x4{a[0], a[1], c[0], c[1]};
      }
    }
  }
  static DI void ilv() {
    constexpr int NR = NA_W * (AK == LIN ? 1 : 2) + NB_W * (BK == LIN ? 1 : 2), NM = NA_W * NB_W;
    constexpr int NG = (NR + DPM - 1) / DPM < NM ? (NR + DPM - 1) / DPM : NM;
#pragma unroll
    for (int i = 0; i < NG; ++i) {
      __builtin_amdgcn_sched_group_barrier(0x8, 1, 0);
      if (i + 1 < NG) __builtin_amdgcn_sched_group_barrier(0x100, DPM, 0);
      else __builtin_amdgcn_sched_group_barrier(0x100, NR - DPM * (NG - 1), 0);
    }
    if constexpr (NM > NG) __builtin_amdgcn_sched_group_barrier(0x8, NM - NG, 0);
    SCHED0();
  }
  template <int G, class ISSUE> static DI void ring(int nt, ISSUE issue) {
    int wslot = NS - 2;
    for (int tau = 0; tau < nt - (NS - 2); ++tau) {
      WAIT_VM((NS - 3) * G);
      BAR();
      issue(tau + NS - 2, wslot * STAGE);
      wslot = (wslot + 1 == NS) ? 0 : wslot + 1;
    }
    if constexpr (NS >= 5) { WAIT_VM(2 * G); BAR(); }
    if constexpr (NS >= 4) { WAIT_VM(G); BAR(); }
    WAIT_VM(0); BAR();
    static_assert(NS >= 3 && NS <= 5, "");
  }
  template <bool F16>
  static DI void consume(const char* smem, int wave, int lane, int n, int& rslot, f32x16 (&acc)[NA_W][NB_W]) {
    const int wa = wave / WB, wb = wave - wa * WB;
    unsigned addrA[AK == LIN ? 1 : NA_W], addrB[BK == LIN ? 1 : NB_W];
    frag_addr<AK, NA_W>(addrA, 0, wa, lane);
    frag_addr<BK, NB_W>(addrB, A_BYTES, wb, lane);
    u32x4 fa[DEPTH][NA_W], fb[DEPTH][NB_W];
#pragma unroll
    for (int d = 1; d < DEPTH; ++d) {
#pragma unroll
      for (int i = 0; i < NA_W; ++i) fa[d][i] = u32x4{0, 0, 0, 0};
#pragma unroll
      for (int i = 0; i < NB_W; ++i) fb[d][i] = u32x4{0, 0, 0, 0};
    }
    auto mm = [&](const u32x4 (&a)[NA_W], const u32x4 (&b)[NB_W]) {
#pragma unroll
      for (int i = 0; i < NA_W; ++i)
#pragma unroll
        for (int j = 0; j < NB_W; ++j) acc[i][j] = mfma32<F16>(a[i], b[j], acc[i][j]);
    };
    auto step = [&](auto Z0) {
      constexpr int z0 = decltype(Z0)::value;
      BAR();
      SCHED0();
      const int sb = rslot * STAGE;
#pragma unroll
      for (int hh = 0; hh < 4; ++hh) {
        rd<AK, NA_W>(fa[(z0 + hh) % DEPTH], smem, addrA, sb, hh);
        rd<BK, NB_W>(fb[(z0 + hh) % DEPTH], smem, addrB, sb, hh);
        mm(fa[(z0 + hh + 1) % DEPTH], fb[(z0 + hh + 1) % DEPTH]);
        ilv();
      }
      rslot = (rslot + 1 == NS) ? 0 : rslot + 1;
    };
    if constexpr (DEPTH == 2) {
      for (int tau = 0; tau < n; ++tau) step(IC<0>{});
      mm(fa[1], fb[1]);
    } else {
      int tau = 0;
      for (; tau + 3 <= n; tau += 3) { step(IC<0>{}); step(IC<1>{}); step(IC<2>{}); }
      if (n - tau == 2) { step(IC<0>{}); step(IC<1>{}); mm(fa[0], fb[0]); mm(fa[1], fb[1]); }
      else if (n - tau == 1) { step(IC<0>{}); mm(fa[2], fb[2]); mm(fa[0], fb[0]); }
      else { mm(fa[1], fb[1]); mm(fa[2], fb[2]); }
    }
  }
};

DI float gelu_tanh(float u) {
  const float z = u * (1.0f + 0.044715f * u * u) * (-2.0f * 0.7978845608028654f * 1.4426950408889634f);
  return u * __builtin_amdgcn_rcpf(1.0f + __builtin_amdgcn_exp2f(z));
}

template <int NV> DI void mv_ilv() {
#pragma unroll
  for (int i = 0; i < 8; ++i) { __builtin_amdgcn_sched_group_barrier(0x8, 1, 0); __builtin_amdgcn_sched_group_barrier(0x2, NV, 0); }
  SCHED0();
}
struct EF { u32x4 r, i, n; };
DI EF ef_load(const char* p) { EF e; e.r = *(const u32x4*)p; e.i = *(const u32x4*)(p + 1024); e.n = *(const u32x4*)(p + 2048); return e; }
#define MF(a, b, c) mfma32<true>(a, b, c)
DI void cm_dA(f32x16& re, f32x16& im, u32x4 dr, u32x4 di, const EF& e) { re = MF(dr, e.r, re); im = MF(dr, e.i, im); re = MF(di, e.n, re); im = MF(di, e.r, im); }
DI void cm_dB(f32x16& re, f32x16& im, u32x4 dr, u32x4 di, const EF& e) { re = MF(e.r, dr, re); im = MF(e.i, dr, im); re = MF(e.n, di, re); im = MF(e.r, di, im); }
DI void cm_dAc(f32x16& re, f32x16& im, u32x4 dr, u32x4 di, const EF& e) { re = MF(dr, e.r, re); im = MF(dr, e.n, im); re = MF(di, e.i, re); im = MF(di, e.r, im); }
DI void wmul(f32x16& re, f32x16& im, const u32x4 (&wv)[4], float wsc) {
#pragma unroll
  for (int p = 0; p < 8; ++p) {
    const unsigned w0 = wv[p >> 1][(2 * p) & 3], w1 = wv[p >> 1][(2 * p + 1) & 3];
    tw_piece(re, im, p, f32x4{lo16(w0), lo16(w1), hi16(w0), hi16(w1)} * wsc);
  }
}
DI void bfly(f32x16& a, f32x16& b) {
  const f32x16 x = a, y = b; a = x + y; b = x - y;
}
DI void colfft_wave(const char* tab  , const char* ws, int c, int lane, float wsc, const u32x4 (&zr)[2][2], const u32x4 (&zi)[2][2],
                    char* img, int slot_re, int slot_im) {
  f32x4 tw[8];
  f32x16 r0 = {}, i0 = {}, r1 = {}, i1 = {};
  u32x4 ar[2], ai[2];
  EF ea = ef_load(tab + TAB_E1), eb = ef_load(tab + TAB_E1 + 3072);
  tw_load(tw, tab + TAB_TW1);
  SCHED0();
  cm_dA(r0, i0, zr[0][0], zi[0][0], ea); cm_dA(r0, i0, zr[0][1], zi[0][1], eb);
  SCHED0();
  cm_dA(r1, i1, zr[1][0], zi[1][0], ea); cm_dA(r1, i1, zr[1][1], zi[1][1], eb);
#pragma unroll
  for (int p = 0; p < 8; ++p) { tw_piece(r0, i0, p, tw[p]); tw[p] = *(const f32x4*)(tab + TAB_TW1 + 8192 + p * 1024); }
  ar[0] = pack8_f16(r0, 0); ar[1] = pack8_f16(r0, 1); ai[0] = pack8_f16(i0, 0); ai[1] = pack8_f16(i0, 1);
  mv_ilv<6>();
  u32x4 wv0[4], wv1[4];
#pragma unroll
  for (int i = 0; i < 4; ++i) wv0[i] = load_dev(ws, OFF_WT + c * 8192u + i * 1024 + lane * 16);
  r0 = f32x16{}; i0 = f32x16{};
  cm_dB(r0, i0, ar[0], ai[0], ea); ea = ef_load(tab + TAB_EO);
  cm_dB(r0, i0, ar[1], ai[1], eb); eb = ef_load(tab + TAB_EO + 3072);
  tw_apply(r1, i1, tw);
  ar[0] = pack8_f16(r1, 0); ar[1] = pack8_f16(r1, 1); ai[0] = pack8_f16(i1, 0); ai[1] = pack8_f16(i1, 1);
  mv_ilv<6>();
#pragma unroll
  for (int i = 0; i < 4; ++i) wv1[i] = load_dev(ws, OFF_WT + c * 8192u + (4 + i) * 1024 + lane * 16);
  r1 = f32x16{}; i1 = f32x16{};
  cm_dB(r1, i1, ar[0], ai[0], ea); ea = ef_load(tab + TAB_E1);
  cm_dB(r1, i1, ar[1], ai[1], eb); eb = ef_load(tab + TAB_E1 + 3072);
  SCHED0();
  bfly(r0, r1); bfly(i0, i1);
  wmul(r0, i0, wv0, wsc);
  wmul(r1, i1, wv1, wsc);
  bfly(r0, r1); bfly(i0, i1);
  u32x4 dr[2], di[2];
  ar[0] = pack8_f16(r0, 0); ar[1] = pack8_f16(r0, 1); ai[0] = pack8_f16(i0, 0); ai[1] = pack8_f16(i0, 1);
  SCHED0();
  tw_load(tw, tab + TAB_TW2);
  r0 = f32x16{}; i0 = f32x16{};
  cm_dAc(r0, i0, ar[0], ai[0], ea); ea = ef_load(tab + TAB_EOI);
  cm_dAc(r0, i0, ar[1], ai[1], eb); eb = ef_load(tab + TAB_EOI + 3072);
  dr[0] = pack8_f16(r1, 0); dr[1] = pack8_f16(r1, 1); di[0] = pack8_f16(i1, 0); di[1] = pack8_f16(i1, 1);
  mv_ilv<2>();
  r1 = f32x16{}; i1 = f32x16{};
  cm_dAc(r1, i1, dr[0], di[0], ea); ea = ef_load(tab + TAB_E1);
  cm_dAc(r1, i1, dr[1], di[1], eb); eb = ef_load(tab + TAB_E1 + 3072);
#pragma unroll
  for (int p = 0; p < 8; ++p) { tw_piece(r0, i0, p, tw[p]); tw[p] = *(const f32x4*)(tab + TAB_TW2 + 8192 + p * 1024); }
  ar[0] = pack8_f16(r0, 0); ar[1] = pack8_f16(r0, 1); ai[0] = pack8_f16(i0, 0); ai[1] = pack8_f16(i0, 1);
  mv_ilv<6>();
  r0 = f32x16{}; i0 = f32x16{};
  cm_dAc(r0, i0, ar[0], ai[0], ea); cm_dAc(r0, i0, ar[1], ai[1], eb);
  tw_apply(r1, i1, tw);
  dr[0] = pack8_f16(r1, 0); dr[1] = pack8_f16(r1, 1); di[0] = pack8_f16(i1, 0); di[1] = pack8_f16(i1, 1);
  mv_ilv<6>();
  r1 = f32x16{}; i1 = f32x16{};
  cm_dAc(r1, i1, dr[0], di[0], ea); cm_dAc(r1, i1, dr[1], di[1], eb);
  SCHED0();
#pragma unroll
  for (int g = 0; g < 4; ++g) {
    const int Q = (lane & 31) * 8 + 2 * g + (lane >> 5), sw = lane & 15, r = 4 * g;
    if (slot_im == -2) {
      *(u32x4*)(img + Q * 16) = u32x4{pack_f16(r0[r], r1[r]), pack_f16(r0[r + 1], r1[r + 1]), pack_f16(r0[r + 2], r1[r + 2]), pack_f16(r0[r + 3], r1[r + 3])};
      continue;
    }
#pragma unroll
    for (int z = 0; z < 2; ++z) {
      const int q = r + 2 * z;
      *(u32x4*)(img + Q * 256 + (((slot_re + z) ^ sw) * 16)) =
          u32x4{pack_f16(r0[q], i0[q]), pack_f16(r1[q], i1[q]), pack_f16(r0[q + 1], i0[q + 1]), pack_f16(r1[q + 1], i1[q + 1])};
    }
  }
}
#undef MF
constexpr int FIN_OS = 2336;
DI void irfft_wave(const char* rec, const char* y288, int w, int lane, char* orow, const char* kt) {
  const int row = lane & 31, hh = lane >> 5, k1 = row & 15, rr = 2 * w + (row >> 4);
  const bool ok = k1 < 10;
  const int k1c = ok ? k1 : 0;
  const char* pd = rec + rr * 4 + 32 * (k1c + 72 * hh);
  const char* pm = rec + rr * 4 + 32 * (576 - k1c - 72 * hh - 18 * 27);
  unsigned v[2][8];
#pragma unroll
  for (int j = 0; j < 8; ++j) {
    v[0][j] = *(const unsigned*)(pd + 576 * (8 * (j >> 2) + (j & 3)));
    v[1][j] = *(const unsigned*)(pm + 576 * (27 - (16 + 8 * (j >> 2) + (j & 3))));
  }
  const unsigned v288 = *(const u16*)(y288 + rr * 2);
  f32x4 tw[8];
  tw_load(tw, kt + 8192);
  if (k1 == 0 && hh == 0) { v[0][0] &= 0xffffu; v[1][0] = v288; }
  f32x16 re = {}, im = {};
#pragma unroll
  for (int u = 0; u < 2; ++u) {
    u32x4 a_r, a_i;
#pragma unroll
    for (int e = 0; e < 4; ++e) {
      a_r[e] = ok ? __builtin_amdgcn_perm(v[u][2 * e + 1], v[u][2 * e], 0x05040100u) : 0u;
      a_i[e] = ok ? (__builtin_amdgcn_perm(v[u][2 * e + 1], v[u][2 * e], 0x07060302u) ^ (u ? 0x80008000u : 0u)) : 0u;
    }
    cm_dAc(re, im, a_r, a_i, ef_load(kt + 2048 + u * 3072));
  }
  const u32x4 cr = *(const u32x4*)kt, ci = *(const u32x4*)(kt + 1024);
  tw_apply(re, im, tw);
#pragma unroll
  for (int u = 0; u < 2; ++u) {
    f32x16 y = {};
    y = mfma32<true>(cr, pack8_f16(re, u), y);
    y = mfma32<true>(ci, pack8_f16(im, u), y);
    float* o = (float*)(orow + u * FIN_OS) + (lane & 31) + 128 * hh;
#pragma unroll
    for (int rho = 0; rho < 8; ++rho) o[32 * ((rho & 3) + 8 * (rho >> 2))] = y[rho];
    if (hh == 0) { o[32 * 16] = y[8]; o[32 * 17] = y[9]; }
  }
}

DI unsigned wmax_exp(const char* ws, int lane) {
  const u32x4 mv = load_dev(ws, OFF_WMAX + lane * 16);
  unsigned mx = max(max(mv[0], mv[1]), max(mv[2], mv[3]));
#pragma unroll
  for (int o = 32; o; o >>= 1) mx = max(mx, (unsigned)__shfl_xor((int)mx, o));
  return min(mx >> 7, 252u);
}

DI unsigned lds_arrive(char* p) { return __hip_atomic_fetch_add((LAS unsigned*)p, 1u, __ATOMIC_RELAXED, __HIP_MEMORY_SCOPE_WORKGROUP); }
DI void lds_arrive_wait(char* p, unsigned n, int lane) {
  if (lane == 0) lds_arrive(p);
  while (__hip_atomic_load((LAS unsigned*)p, __ATOMIC_RELAXED, __HIP_MEMORY_SCOPE_WORKGROUP) < n) __builtin_amdgcn_s_sleep(1);
  asm volatile("" ::: "memory");
}
DI void lds_wait(const char* p) {
  while (__hip_atomic_load((const LAS unsigned*)p, __ATOMIC_RELAXED, __HIP_MEMORY_SCOPE_WORKGROUP) == 0u) __builtin_amdgcn_s_sleep(1);
  asm volatile("" ::: "memory");
}
using CG1 = GC<1, 3, 4, 1, TR128, LIN, 5, 2, 3>;
using CG2 = GC<3, 2, 2, 2, LIN, TR128, 4, 2, 2>;
constexpr int P1_RS = 1216, P1_XS = 3 * CG1::STAGE, P1_HS = P1_XS + 8 * P1_RS, P1_C1 = P1_XS + 16 * P1_RS, P1_E = P1_C1 + 6144,
              P1_TW = P1_E + 6144, P1_O = P1_TW + 8192, P1_WT = P1_O + 9216, P1_RED = P1_WT + 10512, P1_DUMMY = 147456;
static_assert(P1_RED + 48 <= P1_DUMMY && P1_DUMMY >= CG1::LDS_BYTES, "");

__global__ __launch_bounds__(768, 3) void k_all(const u16* __restrict__ x, const u16* __restrict__ wr, const u16* __restrict__ wi,
                                                const char* __restrict__ w1, const u16* __restrict__ b1, const char* __restrict__ w2,
                                                const u16* __restrict__ b2, char* __restrict__ ws, u16* __restrict__ out) {
  extern __shared__ __attribute__((aligned(16))) char smem[];
  const int lane = threadIdx.x & 63, wave = __builtin_amdgcn_readfirstlane(threadIdx.x >> 6);
  const int b = blockIdx.x, g = b & 7, idx = b >> 3;
  const unsigned ldsb = (unsigned)(size_t)(LAS char*)smem;
  const u32x4 rs_ws = mk_rsrc(ws, WS_END), rs_w1 = mk_rsrc(w1, 18874368u), rs_w2 = mk_rsrc(w2, 18874368u);
  if (wave == 0) DBG(0);
  const int tile = g * 27 + idx, ft = tile / 6, dt1 = tile - ft * 6;
  const int ft0 = (g * 27) / 6, nl1 = ((g * 27 + 26) / 6 - ft0 + 1) * 2;
  constexpr int PF1_ROWS = WARM_MODE == 0 ? 192 + (PF1_I * 128 * 64) / 10 : (((PF1_I - 1) * 128 + PF1_W * 32) * 64) / 10;
  auto issue_w1x = [&](int lw, int kt, int so) {
    constexpr unsigned ld = F_ * 2;
    const unsigned vo = CG1::voff<TR128>(lane, 64 * ld);
#pragma unroll
    for (int m = 0; m < 4; ++m) {
      const int a = lw * 4 + m;
      dma16<0>(rs_w1, ldsb + so + a * 1024, vo, (unsigned)(((4 * a) & 31) * 64 + 2 * kt + (a >> 3)) * ld + ft * 256u);
    }
  };
  const int tb = (g >> 2) * 128 + idx * 4 + (g & 3);
  auto gate = [&] {
    if (GATE == 2 && idx < 27) lds_wait(smem + P1_RED + 40);
    else if (GATE >= 1) { while (__hip_atomic_load((const LAS unsigned*)(smem + P1_RED + 16), __ATOMIC_RELAXED, __HIP_MEMORY_SCOPE_WORKGROUP) < 4u) __builtin_amdgcn_s_sleep(1); }
  };
  u64 tag;
  if (wave < 4) {
    const int tid = threadIdx.x, s2 = tb >> 2, t = (tb >> 1) & 1, h = tb & 1;
    const int rr = tid >> 5, l32 = tid & 31;
    const int s1 = 16 * t + 8 * (rr >> 2) + 4 * h + (rr & 3);
    const unsigned* xr = (const unsigned*)(x + (size_t)(64 * s1 + s2) * D_);
    unsigned raw[9];
#pragma unroll
    for (int i = 0; i < 9; ++i) raw[i] = xr[32 * i + l32];
    float v[18];
#pragma unroll
    for (int i = 0; i < 9; ++i) { v[2 * i] = lo16(raw[i]); v[2 * i + 1] = hi16(raw[i]); }
#pragma unroll
    for (int pass = 0; pass < 2; ++pass) {
      float sm = 0.f;
#pragma unroll
      for (int i = 0; i < 18; ++i) sm += v[i];
      const float mean = sum32(sm) * (1.0f / 576.0f);
      float sq = 0.f;
#pragma unroll
      for (int i = 0; i < 18; ++i) { float dlt = v[i] - mean; sq += dlt * dlt; }
      const float var = sum32(sq) * (1.0f / 576.0f);
      const float mb = rbf(mean);
      const float sd = rbf(__builtin_sqrtf(rbf(rbf(var) + 1e-5f)));
      const float rs = 1.0f / sd;
      char* dst = smem + P1_XS + pass * (8 * P1_RS) + rr * P1_RS + l32 * 4;
#pragma unroll
      for (int i = 0; i < 9; ++i) {
        unsigned p = pack_bf16(v[2 * i] - mb, v[2 * i + 1] - mb);
        p = pack_bf16(lo16(p) * rs, hi16(p) * rs);
        v[2 * i] = lo16(p); v[2 * i + 1] = hi16(p);
        *(unsigned*)(dst + i * 128) = pass == 0 ? pack_f16(v[2 * i], v[2 * i + 1]) : p;
      }
    }
    WAIT_LGKM0();
    if (wave == 0) DBG(10);
    BAR();
    if (wave == 0) DBG(13);
    { const u64 tv = *(const u64*)(smem + P1_RED + 24);
      tag = (u64)__builtin_amdgcn_readfirstlane((unsigned)(tv >> 32)) << 32 | (unsigned)__builtin_amdgcn_readfirstlane((unsigned)tv); }
    {
      const int hh = lane >> 5, blk = (lane >> 4) & 1, q = (lane & 15) >> 2, p = lane & 3;
      const char* xa = smem + P1_XS + (2 * wave) * P1_RS + blk * 32 + p * 8;
      const char* cf = smem + P1_C1 + lane * 16;
      f32x16 re = {}, im = {};
      f32x4 tw[8];
      tw_load(tw, smem + P1_TW + lane * 16);
#pragma unroll
      for (int ks = 0; ks < 3; ++ks) {
        const char* a0 = ks < 2 ? xa + ks * P1_RS + (4 * hh + q) * 64 : xa + (q >> 1) * P1_RS + (16 + (q & 1)) * 64;
        const u32x4 a = tr_frag(a0, ks < 2 ? a0 + 512 : a0);
        re = mfma32<true>(a, *(const u32x4*)(cf + (2 * ks) * 1024), re);
        im = mfma32<true>(a, *(const u32x4*)(cf + (2 * ks + 1) * 1024), im);
      }
      tw_apply(re, im, tw);
      f32x16 xr_ = {}, xi_ = {};
#pragma unroll
      for (int u = 0; u < 2; ++u) cm_dB(xr_, xi_, pack8_f16(re, u), pack8_f16(im, u), ef_load(smem + P1_E + u * 3072 + lane * 16));
      const int col = lane & 31, k1 = col & 15, row2 = 2 * (2 * wave + (col >> 4));
      if (k1 < 10) {
        char* o1 = smem + P1_O + 32 * k1 + 576 * 4 * hh + row2;
        char* o2 = smem + P1_O + 32 * (576 - k1) - 576 * (4 * hh + 27) + row2;
#pragma unroll
        for (int rho = 0; rho < 8; ++rho) {
          const unsigned pk = pack_f16(xr_[rho] * (1.0f / 32.0f), xi_[rho] * (1.0f / 32.0f));
          char* d = o1 + 576 * ((rho & 3) + 8 * (rho >> 2));
          *(u16*)d = (u16)pk;
          if (rho != 0 || k1 != 0 || hh != 0) *(u16*)(d + 16) = (u16)(pk >> 16);
        }
        if (k1 >= 1 && k1 <= 8) {
#pragma unroll
          for (int rho = 8; rho < 16; ++rho) {
            const unsigned pk = pack_f16(xr_[rho] * (1.0f / 32.0f), xi_[rho] * (-1.0f / 32.0f));
            char* d = o2 + 576 * (27 - ((rho & 3) + 8 * (rho >> 2)));
            *(u16*)d = (u16)pk;
            *(u16*)(d + 16) = (u16)(pk >> 16);
          }
        } else if (k1 == 0 && hh == 0) {
          *(u16*)(smem + P1_O + 16 + row2) = (u16)pack_f16(xr_[8] * (1.0f / 32.0f), 0.f);
        }
      }
    }
    if (wave == 0) DBG(14);
    WAIT_LGKM0();
    lds_arrive_wait(smem + P1_RED + 32, 4u, lane);
    gate();
    const unsigned P = ((s2 & 1) * 2 + t) * 64 + h * 32 + (s2 >> 1);
#pragma unroll
    for (int it = 0; it < 3; ++it) {
      const int p = tid + it * 256;
      if (p < 576) store_dev(ws, OFF_X1 + P * 9216u + p * 16, *(const u32x4*)(smem + P1_O + p * 16));
    }
    WAIT_VM(0);
    if (lane == 0 && lds_arrive(smem + P1_RED + 36) == 8u) set_flag(ws, SY_A + b * 8, tag);
  } else if (wave < 8) {
    const int tid = threadIdx.x - 256;
    const int rg = b >> 2, cg = b & 3, c0 = cg * 72 + (cg > 0), nc = cg == 0 ? 73 : 72, kq = rg >> 2, ko = rg & 3;
    unsigned rv[10], iv[10];
#pragma unroll
    for (int it = 0; it < 10; ++it) {
      const int i = tid + it * 256, row = i / 73, col = i - row * 73;
      const bool ok = i < 32 * 73 && col < nc;
      const int off = ok ? (8 * ko + (row & 7) + 32 * (4 * kq + (row >> 3))) * NC_ + c0 + col : 0;
      rv[it] = wr[off]; iv[it] = wi[off];
    }
    if (tid == 0) {
      const u64 ep = __hip_atomic_load((gu64*)(ws + SY_EPOCH), RLX_AGENT);
      *(u64*)(smem + P1_RED + 24) = ep * 6364136223846793005ull + 1442695040888963407ull;
    }
    unsigned mx = 0;
#pragma unroll
    for (int it = 0; it < 10; ++it) {
      const int i = tid + it * 256, row = i / 73, col = i - row * 73;
      if (i < 32 * 73 && col < nc) {
        mx = max(mx, max(rv[it] & 0x7fffu, iv[it] & 0x7fffu));
        *(unsigned*)(smem + P1_WT + col * 144 + (row & 7) * 16 + (row >> 3) * 4) = rv[it] | (iv[it] << 16);
      }
    }
#pragma unroll
    for (int o = 32; o; o >>= 1) mx = max(mx, (unsigned)__shfl_xor((int)mx, o));
    unsigned* red = (unsigned*)(smem + P1_RED);
    if (lane == 0) red[wave - 4] = mx;
    WAIT_LGKM0();
    if (wave == 4) DBG(11);
    BAR();
    { const u64 tv = *(const u64*)(smem + P1_RED + 24);
      tag = (u64)__builtin_amdgcn_readfirstlane((unsigned)(tv >> 32)) << 32 | (unsigned)__builtin_amdgcn_readfirstlane((unsigned)tv); }
    if (tid == 0) __hip_atomic_store((gu32*)(ws + OFF_WMAX + b * 4), max(max(red[0], red[1]), max(red[2], red[3])), RLX_AGENT);
    const int U = kq >> 3, h = kq & 1, rq = (kq >> 1) & 3;
    gate();
#pragma unroll
    for (int it = 0; it < 3; ++it) {
      const int p = tid + it * 256;
      if (p < nc * 8) {
        const int col = p >> 3, a = p & 7;
        store_dev(ws, OFF_WT + (unsigned)(c0 + col) * 8192u + ((U * 4 + rq) * 64 + h * 32 + 8 * ko + a) * 16,
                  *(const u32x4*)(smem + P1_WT + col * 144 + a * 16));
      }
    }
    WAIT_VM(0);
    if (lane == 0 && lds_arrive(smem + P1_RED + 36) == 8u) set_flag(ws, SY_A + b * 8, tag);
    if (idx < 27) { issue_w1x(wave - 4, 0, 0); issue_w1x(wave - 4, 1, CG1::STAGE); issue_w1x(wave - 4, 2, 2 * CG1::STAGE); }
  } else {
#pragma unroll
    for (int z = 0; z < 5; ++z) {
      const int fr = (wave - 8) + 4 * z;
      u32x4 val;
      const int col = lane & 31, hh = lane >> 5, r = col >> 4, k1 = col & 15;
      if (fr < 6) {
        const int ks = fr >> 1, which = fr & 1;
        float o[8];
#pragma unroll
        for (int j = 0; j < 8; ++j) {
          int row, d1;
          if (ks < 2) { row = ks; d1 = 8 * (j >> 2) + 4 * hh + (j & 3); }
          else { row = (hh == 0 && j < 4) ? (j >> 1) : 2; d1 = 16 + (j & 1); }
          const float v = cs(which, (float)((d1 * k1) % 18) * (1.0f / 18.0f));
          o[j] = (row == r && k1 < 10) ? v : 0.f;
        }
        val = u32x4{pack_f16(o[0], o[1]), pack_f16(o[2], o[3]), pack_f16(o[4], o[5]), pack_f16(o[6], o[7])};
      } else if (fr < 12) {
        val = e32_frag(fr - 6, lane);
      } else {
#pragma unroll
        for (int y = 0; y < 2; ++y) {
          const int rho = 2 * (fr - 12) + y, d2 = (rho & 3) + 8 * (rho >> 2) + 4 * hh;
          const float a = (float)(d2 * k1) * (1.0f / 576.0f);
          val[y] = __float_as_uint(__builtin_amdgcn_cosf(a)); val[2 + y] = __float_as_uint(-__builtin_amdgcn_sinf(a));
        }
      }
      *(u32x4*)(smem + P1_C1 + fr * 1024 + lane * 16) = val;
      if (z == W1_Z) {
#pragma unroll
        for (int i = 0; i < PF1_I; ++i)
          if (i + 1 < PF1_I || wave - 8 < PF1_W)
            dma4(rs_w1, ldsb + P1_DUMMY + wave * 256, w1_line((i * 128 + (i + 1 < PF1_I ? idx * 4 : idx * PF1_W) + (wave - 8)) * 64 + lane, PF1_ROWS * nl1, 0, nl1, ft0 * 256u));
      }
    }
    if (threadIdx.x == 512) { *(unsigned*)(smem + P1_RED + 16) = 0u; *(u64*)(smem + P1_RED + 32) = 0ull; *(unsigned*)(smem + P1_RED + 40) = 0u; }
    if (wave == 8 && b < 66) {
      const int r = lane & 31, h = lane >> 5;
      u32x4 val;
      if (b < 6) val = e32_frag(b, lane);
      else if (b < 18) {
        const bool inv = b >= 12;
        const int f2 = inv ? b - 12 : b - 6, u = f2 / 3, which = f2 - 3 * u;
        float o[8];
#pragma unroll
        for (int j = 0; j < 8; ++j) {
          const int kap = 16 * u + 8 * (j >> 2) + 4 * h + (j & 3);
          o[j] = cs(which, (float)((inv ? kap * (2 * r + 1) : r * (2 * kap + 1)) & 63) * (1.0f / 64.0f));
        }
        val = u32x4{pack_f16(o[0], o[1]), pack_f16(o[2], o[3]), pack_f16(o[4], o[5]), pack_f16(o[6], o[7])};
      } else if (b < 50) {
        const bool inv = b >= 34;
        const int q = inv ? b - 34 : b - 18, p = q & 7, T = q >> 3;
#pragma unroll
        for (int z = 0; z < 2; ++z) {
          const int rho = 2 * p + z, cr = (rho & 3) + 8 * (rho >> 2) + 4 * h;
          const int n = inv ? (2 * r + T) * cr : (2 * cr + T) * r;
          const float a = (float)(n & 2047) * (1.0f / 2048.0f);
          val[z] = __float_as_uint(__builtin_amdgcn_cosf(a) * (inv ? 0.125f : 1.0f));
          val[2 + z] = __float_as_uint(inv ? __builtin_amdgcn_sinf(a) * 0.125f : -__builtin_amdgcn_sinf(a));
        }
      } else if (b < 52) {
        float o[8];
#pragma unroll
        for (int j = 0; j < 8; ++j) {
          const int k1 = 8 * (j >> 2) + 4 * h + (j & 3);
          const float v = cs(b - 50, (float)((r * k1) % 18) * (1.0f / 18.0f)) * ((k1 == 0 || k1 == 9) ? 1.f : 2.f);
          o[j] = (k1 < 10 && r < 18) ? v : 0.f;
        }
        val = u32x4{pack_f16(o[0], o[1]), pack_f16(o[2], o[3]), pack_f16(o[4], o[5]), pack_f16(o[6], o[7])};
      } else if (b < 58) {
        val = e32_frag(b - 52, lane);
      } else {
#pragma unroll
        for (int y = 0; y < 2; ++y) {
          const int rho = 2 * (b - 58) + y, k1 = ((rho & 3) + 8 * (rho >> 2) + 4 * h) & 15;
          const float a = (float)(k1 * r) * (1.0f / 576.0f);
          val[y] = __float_as_uint(__builtin_amdgcn_cosf(a) * 0.125f); val[2 + y] = __float_as_uint(__builtin_amdgcn_sinf(a) * 0.125f);
        }
      }
      store_dev(ws, (b < 50 ? OFF_TAB + b * 1024u : OFF_RT + (b - 50) * 1024u) + lane * 16, val);
    }
    WAIT_LGKM0();
    if (wave == 8) DBG(12);
    BAR();
    { const u64 tv = *(const u64*)(smem + P1_RED + 24);
      tag = (u64)__builtin_amdgcn_readfirstlane((unsigned)(tv >> 32)) << 32 | (unsigned)__builtin_amdgcn_readfirstlane((unsigned)tv); }
    {
      const int s2 = tb >> 2, t = (tb >> 1) & 1, h = tb & 1;
      for (int cb = wave - 8; cb < 9; cb += 4) {
        const int gg = lane >> 4, q = (lane & 15) >> 2, pp = lane & 3;
        const char* src = smem + P1_HS + q * P1_RS + (cb * 64 + gg * 16 + pp * 4) * 2;
        const int df = cb * 2 + (gg >> 1), i = 8 * q + 4 * (gg & 1) + pp;
        store_dev(ws, OFF_H + (unsigned)(df * 128 + 2 * s2 + t) * 1024u + (h * 32 + i) * 16, tr_frag(src, src + 4 * P1_RS));
      }
      WAIT_VM(0);
      unsigned last = 0;
      if (lane == 0) last = lds_arrive(smem + P1_RED + 16);
      if (__builtin_amdgcn_readfirstlane(last) == 3u) { if (lane < 8) set_flag(ws, SY_H + lane * 2048 + tb * 8, tag); DBG(1); }
      if (wave == 8 && lane == 0 && lds_arrive(smem + P1_RED + 36) == 8u) set_flag(ws, SY_A + b * 8, tag);
    }
  }
  if (wave == 0) DBG(2);
  if (idx < 27) {
    char* ebuf = smem + (29 % 5) * CG1::STAGE;
    u32x2 bv[3][2];
#pragma unroll
    for (int it = 0; it < 3; ++it) {
      const u32x4* bp = (const u32x4*)(b1 + ft * 128 + (min(wave + 11 * it, 23) / 3) * 16);
      const u32x4 b0 = bp[0], b1v = bp[1];
      const bool hi = lane >= 32;
      bv[it][0] = u32x2{hi ? b0[2] : b0[0], hi ? b0[3] : b0[1]}; bv[it][1] = u32x2{hi ? b1v[2] : b1v[0], hi ? b1v[3] : b1v[1]};
      asm volatile("" : "+v"(bv[it][0]), "+v"(bv[it][1]));
    }
    if (wave == 11) {
      const unsigned fh = SY_H + g * 2048 + lane * 8;
      poll_flags<1>(ws, fh, true, tag);
      if (lane == 0) lds_arrive(smem + P1_RED + 40);
      bool ok[3];
#pragma unroll
      for (int q = 0; q < 3; ++q) ok[q] = __all(__hip_atomic_load((gu64*)(ws + fh + (q + 1) * 512), RLX_AGENT) == tag);
      for (int tau = 0; tau < 32; ++tau) {
#pragma unroll
        for (int q = 0; q < 3; ++q)
          if (tau == 8 * q + 5 && !ok[q]) poll_flags<1>(ws, fh + (q + 1) * 512, true, tag);
        if (tau == 21 && b == 0 && lane == 0) set_flag(ws, SY_EPOCH, tag);
        BAR();
        if (tau == PF2_TAU) {
#pragma unroll
          for (int i = 0; i < PF2T; ++i) {
            const int line = (i * 27 + idx) * 64 + lane;
            int row = (line >> 4) + (g & 1) * 576; if (row >= 1152) row -= 1152;
            dma4(rs_w2, ldsb + P1_DUMMY + wave * 256, (unsigned)((g >> 1) * 1152 + row) * 4096u + (g & 1) * 2048u + (line & 15) * 128u);
          }
        }
      }
    } else if (wave >= 8) {
      auto issue_h = [&](int kt, int so) {
#pragma unroll
        for (int m = 0; m < 4; ++m)
          dma16<2>(rs_ws, ldsb + so + CG1::A_BYTES + ((wave - 8) * 4 + m) * 1024, lane * 16,
                   OFF_H + (unsigned)((dt1 * 3 + (wave - 8)) * 128 + kt * 4 + m) * 1024u);
      };
      lds_wait(smem + P1_RED + 40);
      if (wave == 8) DBG(3);
      issue_h(0, 0); issue_h(1, CG1::STAGE); issue_h(2, 2 * CG1::STAGE);
      CG1::ring<4>(32, issue_h);
    } else if (wave >= 4) {
      CG1::ring<4>(32, [&](int kt, int so) { issue_w1x(wave - 4, kt, so); });
    } else {
      f32x16 acc[1][3] = {};
      int rslot = 0;
      CG1::consume<false>(smem, wave, lane, 32, rslot, acc);
      if (wave == 0) DBG(4);
#pragma unroll
      for (int j = 0; j < 3; ++j)
#pragma unroll
        for (int s = 0; s < 2; ++s) {
          u32x4 val;
#pragma unroll
          for (int e = 0; e < 4; ++e) val[e] = pack_bf16(acc[0][j][8 * s + 2 * e], acc[0][j][8 * s + 2 * e + 1]);
          *(u32x4*)(ebuf + ((wave * 2 + s) * 3 + j) * 1024 + lane * 16) = val;
        }
    }
    WAIT_LGKM0();
    BAR();
    if (wave == 0) DBG(15);
    if (wave < 11) {
#pragma unroll
      for (int it = 0; it < 3; ++it) {
        const int i = wave + 11 * it;
        if (i < 24) {
          const int z = i / 3, j = i - 3 * z;
          const u32x4 a = *(const u32x4*)(ebuf + i * 1024 + lane * 16);
          u32x4 val;
#pragma unroll
          for (int e = 0; e < 4; ++e) {
            const unsigned bb = bv[it][e >> 1][e & 1];
            const unsigned pk = pack_bf16(lo16(a[e]) + lo16(bb), hi16(a[e]) + hi16(bb));
            val[e] = pack_bf16(gelu_tanh(lo16(pk)), gelu_tanh(hi16(pk)));
          }
          store_dev(ws, OFF_U + (unsigned)((dt1 * 3 + j) * 288 + ft * 8 + z) * 1024u + lane * 16, val);
        }
      }
      WAIT_VM(0);
    } else if (lane == 0) *(unsigned*)(smem + 163832) = 0u;
    WAIT_LGKM0();
    BAR();
    if (threadIdx.x < 2) set_flag(ws, SY_U + threadIdx.x * 2048 + tile * 8, tag);
  } else {
    const int me = (idx - 27) * 8 + g;
    {
      const int total = (2048 - PF1_ROWS) * nl1;
      for (int i0 = ((idx - 27) * 12 + wave) * 64; i0 < total; i0 += 60 * 64) {
        dma4(rs_w1, ldsb + P1_DUMMY + wave * 256, w1_line(i0 + lane, total, PF1_ROWS, nl1, ft0 * 256u));
        WAIT_VM(7);
      }
    }
    for (int i0 = ((idx - 27) * 12 + wave) * 64; i0 < PF2_ROWS * 16; i0 += 60 * 64) {
      const int line = i0 + lane;
      int row = (line >> 4) + (g & 1) * 576; if (row >= 1152) row -= 1152;
      dma4(rs_w2, ldsb + P1_DUMMY + wave * 256, (unsigned)((g >> 1) * 1152 + row) * 4096u + (g & 1) * 2048u + (line & 15) * 128u);
      WAIT_VM(7);
    }
    if (wave == 0) DBG(3);
    if (me < 37) {
      if (wave == 8) poll_flags<4>(ws, SY_A + lane * 8, true, tag);
      BAR();
      if (wave == 0) DBG(4);
      const int cbk = me == 36 ? 0 : me;
      char* img = smem + 65536;
      for (int i = wave; i < 64; i += 12) {
        const int P = 4 * i + (lane >> 4);
        dma16<2>(rs_ws, ldsb + 65536 + i * 1024, (unsigned)P * 9216u + (unsigned)(((lane & 15) ^ (P & 15)) * 16), OFF_X1 + cbk * 256u);
      }
      for (int i = wave; i < TAB_BYTES / 1024; i += 12) dma16<2>(rs_ws, ldsb + i * 1024, lane * 16, OFF_TAB + i * 1024u);
      const bool act = me == 36 ? wave == 0 : wave < 8;
      const int c = me == 36 ? 288 : me * 8 + wave;
      const float wsc = __uint_as_float((253u - wmax_exp(ws, lane)) << 23) * (1.0f / 64.0f);
      WAIT_VM(0);
      BAR();
      if (wave == 0) DBG(7);
      const int slot_re = me == 36 ? 1 : 2 * wave, slot_im = c == 288 ? -2 : (c == 0 ? -1 : 2 * wave + 1);
      u32x4 zr[2][2], zi[2][2];
      if (act) {
#pragma unroll
        for (int z = 0; z < 4; ++z) {
          const char* p = img + (z * 64 + lane) * 256;
          zr[z >> 1][z & 1] = *(const u32x4*)(p + ((slot_re ^ (lane & 15)) * 16));
          zi[z >> 1][z & 1] = slot_im >= 0 ? *(const u32x4*)(p + ((slot_im ^ (lane & 15)) * 16)) : u32x4{0, 0, 0, 0};
        }
      }
      WAIT_LGKM0();
      BAR();
      if (act) colfft_wave(smem + lane * 16, ws, c, lane, wsc, zr, zi, img, slot_re, slot_im);
      else if (PF2C_ROWS > 0 && wave >= 8 && idx < 31) {
        for (int i = 0; i < PF2C_SLP; ++i) __builtin_amdgcn_s_sleep(64);
        for (int i0 = ((idx - 27) * 4 + wave - 8) * 64; i0 < PF2C_ROWS * 16; i0 += 16 * 64) {
          const int line = PF2T * 27 * 64 + i0 + lane;
          int row = (line >> 4) + (g & 1) * 576; if (row >= 1152) row -= 1152;
          dma4(rs_w2, ldsb + P1_DUMMY + wave * 256, (unsigned)((g >> 1) * 1152 + row) * 4096u + (g & 1) * 2048u + (line & 15) * 128u);
          WAIT_VM(7);
        }
      }
      if (wave == 0) DBG(8);
      WAIT_LGKM0();
      BAR();
      if (wave == 0) DBG(15);
      if (me == 36) {
        if (wave < 4) store_dev(ws, OFF_Y288 + wave * 1024 + lane * 16, *(const u32x4*)(img + wave * 1024 + lane * 16));
      } else {
        for (int i = wave; i < 64; i += 12) {
          const int Qy = 4 * i + (lane >> 4);
          store_dev(ws, OFF_Y1 + (unsigned)Qy * 9216u + cbk * 256 + (lane & 15) * 16,
                    *(const u32x4*)(img + Qy * 256 + (((lane & 15) ^ ((Qy >> 3) & 15)) * 16)));
        }
      }
      WAIT_VM(0);
      BAR();
      if (threadIdx.x == 0) set_flag(ws, SY_Y + me * 8, tag);
    }
  }
  if (wave == 0) DBG(5);
  if (idx < 24) {
    const int ks = g >> 1, dt = idx % 3, st = (g & 1) * 8 + idx / 3;
    const int rot = (g & 1) * 9;
    const unsigned myflags = SY_T + (unsigned)((st * 4 + ks) * 3 + dt) * 64u;
    if (wave >= 8) {
      auto issue_u = [&](int k0, int so) {
        const int kt = k0 + rot < 18 ? k0 + rot : k0 + rot - 18;
        if (k0 == 0 || k0 == 8) {
          const int kf = k0 == 0 ? rot : 9 - rot;
          if (wave == 8) {
            poll_flags<1>(ws, SY_U + (g & 1) * 2048 + (unsigned)((9 * ks + (kf >> 1) + (lane >> 1)) * 6 + 2 * dt + (lane & 1)) * 8u, lane < 10, tag);
            if (k0 == 0) { if (lane == 0) lds_arrive(smem + 163832); DBG(6); }
          } else if (k0 == 0) lds_wait(smem + 163832);
        }
#pragma unroll
        for (int m = 0; m < 6; ++m) {
          const int a = (wave - 8) * 6 + m;
          dma16<2>(rs_ws, ldsb + so + a * 1024, lane * 16, OFF_U + (unsigned)((dt * 6 + (a >> 2)) * 288 + (ks * 18 + kt) * 4 + (a & 3)) * 1024u);
        }
      };
      issue_u(0, 0); issue_u(1, CG2::STAGE);
      CG2::ring<6>(18, issue_u);
      BAR();
      if (wave == 8) poll_flags<1>(ws, myflags + (lane & 3) * 8, true, tag);
      BAR();
      return;
    }
    if (wave >= 4) {
      auto issue_w2 = [&](int k0, int so) {
        const int kt = k0 + rot < 18 ? k0 + rot : k0 + rot - 18;
        constexpr unsigned ld = S_ * 2;
        const unsigned vo = CG2::voff<TR128>(lane, ld);
#pragma unroll
        for (int m = 0; m < 4; ++m) {
          const int a = (wave - 4) * 4 + m;
          const unsigned sof = (unsigned)((ks * 18 + kt) * 64 + 4 * a) * ld + st * 256u;
          if (k0 >= NTF2) dma16<1>(rs_w2, ldsb + so + CG2::A_BYTES + a * 1024, vo, sof);
          else dma16<0>(rs_w2, ldsb + so + CG2::A_BYTES + a * 1024, vo, sof);
        }
      };
      issue_w2(0, 0); issue_w2(1, CG2::STAGE);
      CG2::ring<4>(18, issue_w2);
      BAR();
      BAR();
      return;
    }
    f32x16 acc[3][2] = {};
    int rslot = 0;
    CG2::consume<false>(smem, wave, lane, 18, rslot, acc);
    if (wave == 0) DBG(7);
    const int wa = wave >> 1, wb = wave & 1;
    const bool own0 = wb * 2 == ks, own1 = wb * 2 + 1 == ks;
#pragma unroll
    for (int j = 0; j < 2; ++j) {
      if (j ? own1 : own0) continue;
#pragma unroll
      for (int i = 0; i < 3; ++i) {
        const unsigned o = OFF_SLAB + (unsigned)((ks * 64 + st * 4 + wb * 2 + j) * 18 + dt * 6 + wa * 3 + i) * 2048u + lane * 16;
#pragma unroll
        for (int u = 0; u < 2; ++u) {
          u32x4 val;
#pragma unroll
          for (int e = 0; e < 4; ++e) val[e] = pack_bf16(acc[i][j][8 * u + 2 * e], acc[i][j][8 * u + 2 * e + 1]);
          store_dev(ws, o + u * 1024, val);
        }
      }
    }
    WAIT_VM(0);
    BAR();
    if (wave == 0 && lane < 4 && lane != ks) set_flag(ws, SY_T + (unsigned)((st * 4 + lane) * 3 + dt) * 64u + ks * 8, tag);
    BAR();
    if (wave == 0) DBG(8);
    if (!own0 && !own1) return;
    const int sf = st * 4 + ks, s = sf * 32 + (lane & 31);
    auto finish = [&](auto J) {
      constexpr int j = decltype(J)::value;
      u32x4 pv[3][2][4];
#pragma unroll
      for (int i = 0; i < 3; ++i)
#pragma unroll
        for (int u = 0; u < 2; ++u) {
          const unsigned o = (unsigned)(sf * 18 + dt * 6 + wa * 3 + i) * 2048u + u * 1024 + lane * 16;
#pragma unroll
          for (int q = 0; q < 3; ++q) pv[i][u][q] = load_dev(ws, OFF_SLAB + o + ((ks + 1 + q) & 3) * 2359296u);
          pv[i][u][3] = load_dev(ws, OFF_R + o);
        }
#pragma unroll
      for (int i = 0; i < 3; ++i)
#pragma unroll
        for (int u = 0; u < 2; ++u) {
          u32x4 o;
#pragma unroll
          for (int e = 0; e < 4; ++e)
            o[e] = pack_bf16(acc[i][j][8 * u + 2 * e] + (lo16(pv[i][u][0][e]) + lo16(pv[i][u][1][e])) + (lo16(pv[i][u][2][e]) + lo16(pv[i][u][3][e])),
                             acc[i][j][8 * u + 2 * e + 1] + (hi16(pv[i][u][0][e]) + hi16(pv[i][u][1][e])) + (hi16(pv[i][u][2][e]) + hi16(pv[i][u][3][e])));
          *(u32x4*)(out + (size_t)s * D_ + (dt * 6 + wa * 3 + i) * 32 + (lane >> 5) * 16 + u * 8) = o;
        }
    };
    if (own0) finish(IC<0>{}); else finish(IC<1>{});
  } else {
    const int me = (idx - 24) * 8 + g, sr = lane & 31;
    char* kt = smem + 37888; char* yb = kt + 17408;
    u32x4 xv[3];
#pragma unroll
    for (int it = 0; it < 3; ++it) xv[it] = *(const u32x4*)(x + (size_t)(me * 32 + sr) * D_ + ((it * 12 + wave) * 2 + (lane >> 5)) * 8);
    const float bias = lo16(b2[me * 32 + sr]);
    if (PF3_ROWS > 0 && idx < 27) {
      for (int i0 = ((idx - 24) * 12 + wave) * 64; i0 < PF3_ROWS * 16; i0 += 36 * 64) {
        const int line = PF2T * 27 * 64 + PF2C_ROWS * 16 + i0 + lane;
        int row = (line >> 4) + (g & 1) * 576; if (row >= 1152) row -= 1152;
        dma4(rs_w2, ldsb + P1_DUMMY + wave * 256, (unsigned)((g >> 1) * 1152 + row) * 4096u + (g & 1) * 2048u + (line & 15) * 128u);
        WAIT_VM(7);
      }
    }
    if (wave == 8) { poll_flags<1, 16>(ws, SY_Y + min(lane, 36) * 8, true, tag); DBG(6); }
    BAR();
#pragma unroll
    for (int it = 0; it < 3; ++it) dma16<2>(rs_ws, ldsb + (it * 12 + wave) * 1024, lane * 16, OFF_Y1 + me * 36864u + (it * 12 + wave) * 1024u);
    dma16<2>(rs_ws, ldsb + 37888 + wave * 1024, lane * 16, OFF_RT + wave * 1024u);
    if (wave < 4) dma16<2>(rs_ws, ldsb + 37888 + (12 + wave) * 1024, lane * 16, OFF_RT + (12 + wave) * 1024u);
    if (wave == 4) dma16<2>(rs_ws, ldsb + 37888 + 16384, (lane & 3) * 16, OFF_Y288 + me * 64u);
    const float fs = __uint_as_float((wmax_exp(ws, lane) + 1u) << 23) * (1.0f / 9.0f);
    WAIT_VM(0);
    BAR();
    for (int task = wave; task < 16; task += 12)
      irfft_wave(smem + (task >> 2) * 9216, kt + 16384 + (task >> 2) * 16, task & 3, lane, yb + task * 2 * FIN_OS, kt + lane * 16);
    WAIT_LGKM0();
    BAR();
#pragma unroll
    for (int it = 0; it < 3; ++it) {
      const int dp = (it * 12 + wave) * 2 + (lane >> 5);
      const char* yp = yb + sr * FIN_OS + dp * 32;
      const f32x4 y0 = *(const f32x4*)yp, y1 = *(const f32x4*)(yp + 16);
      u32x4 o;
#pragma unroll
      for (int e = 0; e < 4; ++e) {
        const float ya = e < 2 ? y0[2 * e] : y1[2 * e - 4], yc = e < 2 ? y0[2 * e + 1] : y1[2 * e - 3];
        o[e] = pack_bf16(lo16(xv[it][e]) + bias + ya * fs, hi16(xv[it][e]) + bias + yc * fs);
      }
      store_dev(ws, OFF_R + (unsigned)(me * 18 + (dp >> 2)) * 2048u + (dp & 1) * 1024 + (((dp >> 1) & 1) * 32 + sr) * 16, o);
    }
    WAIT_VM(0);
    BAR();
    if (wave == 0 && lane < 3) set_flag(ws, SY_T + (unsigned)(me * 3 + lane) * 64u + (me & 3) * 8, tag);
    if (wave == 0) DBG(9);
  }
}

extern "C" void kernel_launch(void* const* d_in, const int* in_sizes, int n_in, void* d_out, int out_size, void* d_ws, size_t ws_size,
                              hipStream_t stream) {
  (void)hipFuncSetAttribute((const void*)k_all, hipFuncAttributeMaxDynamicSharedMemorySize, 163840);
  k_all<<<256, 768, 163840, stream>>>((const u16*)d_in[0], (const u16*)d_in[1], (const u16*)d_in[2], (const char*)d_in[4], (const u16*)d_in[5],
                                      (const char*)d_in[6], (const u16*)d_in[7], (char*)d_ws, (u16*)d_out);
}
```

```cpp
#include <hip/hip_runtime.h>
#include <hip/hip_bf16.h>
#include <stdint.h>

#define DI __device__ __forceinline__
#define LAS __attribute__((address_space(3)))
typedef unsigned short u16;
typedef unsigned long long u64;
typedef __attribute__((ext_vector_type(2))) float f32x2;
typedef __attribute__((ext_vector_type(4))) float f32x4;
typedef __attribute__((ext_vector_type(16))) float f32x16;
typedef __attribute__((ext_vector_type(2))) _Float16 f16x2;
typedef __attribute__((ext_vector_type(8))) _Float16 f16x8;
typedef __attribute__((ext_vector_type(2))) __bf16 bf16x2;
typedef __attribute__((ext_vector_type(8))) __bf16 bf16x8;
typedef __attribute__((ext_vector_type(4))) short s16x4;
typedef __attribute__((ext_vector_type(2))) unsigned u32x2;
typedef __attribute__((ext_vector_type(4))) unsigned u32x4;
typedef __attribute__((address_space(1))) unsigned gu32;
typedef __attribute__((address_space(1))) u64 gu64;
#define RLX_AGENT __ATOMIC_RELAXED, __HIP_MEMORY_SCOPE_AGENT

constexpr int PF1_ROWS = (160 * 64) / 10;
constexpr int PF2T = 2;
constexpr int PF2C_ROWS = 488;
constexpr int NTF2 = 11;
constexpr int POLL_SLEEP = 4;

constexpr int S_ = 2048, D_ = 576, F_ = 4608, NC_ = 289;

constexpr unsigned OFF_H    = 0;
constexpr unsigned OFF_WT   = OFF_H + 2359296;
constexpr unsigned OFF_TAB  = OFF_WT + 2367488;
constexpr unsigned OFF_RT   = OFF_TAB + 65536;
constexpr unsigned OFF_Y288 = OFF_RT + 16384;
constexpr unsigned OFF_WMAX = OFF_Y288 + 4096;
constexpr unsigned OFF_X1   = OFF_WMAX + 4096;
constexpr unsigned OFF_Y1   = OFF_X1 + 2359296;
constexpr unsigned OFF_U    = OFF_Y1 + 2359296;
constexpr unsigned OFF_SLAB = OFF_U + 5308416;
constexpr unsigned OFF_R    = OFF_SLAB + 9437184;
constexpr unsigned OFF_SYNC = OFF_R + 2359296;
constexpr unsigned SY_EPOCH = OFF_SYNC, SY_TMO = OFF_SYNC + 256, SY_A = OFF_SYNC + 2048, SY_Y = OFF_SYNC + 4096, SY_U = OFF_SYNC + 6144,
                   SY_H = SY_U + 2 * 2048, SY_T = SY_H + 8 * 2048;
constexpr unsigned WS_END   = SY_T + 192 * 64;
constexpr int TAB_E1 = 0, TAB_EO = 6144, TAB_EOI = 12288, TAB_TW1 = 18432, TAB_TW2 = 34816, TAB_BYTES = 51200;

DI unsigned pack_bf16(float a, float b) { f32x2 v = {a, b}; return __builtin_bit_cast(unsigned, __builtin_convertvector(v, bf16x2)); }
DI unsigned pack_f16(float a, float b) { f32x2 v = {a, b}; return __builtin_bit_cast(unsigned, __builtin_convertvector(v, f16x2)); }
DI float lo16(unsigned p) { return __uint_as_float(p << 16); }
DI float hi16(unsigned p) { return __uint_as_float(p & 0xffff0000u); }
DI float rbf(float a) { return lo16(pack_bf16(a, a)); }

template <int CTRL> DI float dpp_add(float v) {
  return v + __int_as_float(__builtin_amdgcn_update_dpp(0, __float_as_int(v), CTRL, 0xf, 0xf, true));
}
DI float sum32(float v) {
  v = dpp_add<0xB1>(v); v = dpp_add<0x4E>(v); v = dpp_add<0x141>(v); v = dpp_add<0x140>(v);
  auto r = __builtin_amdgcn_permlane16_swap(__float_as_uint(v), __float_as_uint(v), false, false);
  return __uint_as_float(r[0]) + __uint_as_float(r[1]);
}

#define WAIT_VM(n) asm volatile("s_waitcnt vmcnt(%0)" ::"n"(n) : "memory")
#define WAIT_LGKM0() asm volatile("s_waitcnt lgkmcnt(0)" ::: "memory")
#define BAR() do { asm volatile("" ::: "memory"); __builtin_amdgcn_s_barrier(); asm volatile("" ::: "memory"); } while (0)
#define SCHED0() __builtin_amdgcn_sched_barrier(0)

DI u32x4 mk_rsrc(const void* p, unsigned bytes) {
  const u64 a = (u64)p;
  return u32x4{(unsigned)a, (unsigned)(a >> 32) & 0xffffu, bytes, 0x00020000u};
}
template <int POL> DI void dma16(u32x4 rsrc, unsigned lds_off, unsigned voff, unsigned soff) {
  if constexpr (POL == 0) asm volatile("s_mov_b32 m0, %0\n\ts_nop 0\n\tbuffer_load_dwordx4 %1, %2, %3 offen lds" ::"s"(lds_off), "v"(voff), "s"(rsrc), "s"(soff) : "memory");
  if constexpr (POL == 1) asm volatile("s_mov_b32 m0, %0\n\ts_nop 0\n\tbuffer_load_dwordx4 %1, %2, %3 offen nt lds" ::"s"(lds_off), "v"(voff), "s"(rsrc), "s"(soff) : "memory");
  if constexpr (POL == 2) asm volatile("s_mov_b32 m0, %0\n\ts_nop 0\n\tbuffer_load_dwordx4 %1, %2, %3 offen sc1 lds" ::"s"(lds_off), "v"(voff), "s"(rsrc), "s"(soff) : "memory");
}
DI void dma4(u32x4 rsrc, unsigned lds_off, unsigned voff) {
  asm volatile("s_mov_b32 m0, %0\n\ts_nop 0\n\tbuffer_load_dword %1, %2, 0 offen lds" ::"s"(lds_off), "v"(voff), "s"(rsrc) : "memory");
}
DI void store_dev(char* ws, unsigned off, u32x4 v) {
  __builtin_amdgcn_raw_buffer_store_b128(v, __builtin_amdgcn_make_buffer_rsrc((void*)ws, 0, WS_END, 0x00020000), off, 0, 16);
}
DI u32x4 load_dev(const char* ws, unsigned off) {
  return __builtin_amdgcn_raw_buffer_load_b128(__builtin_amdgcn_make_buffer_rsrc((void*)ws, 0, WS_END, 0x00020000), off, 0, 16);
}
DI void set_flag(char* ws, unsigned off, u64 tag) { __hip_atomic_store((gu64*)(ws + off), tag, RLX_AGENT); }
template <int N, int SLP = POLL_SLEEP> DI void poll_flags(char* ws, unsigned off, bool active, u64 tag) {
  for (unsigned spins = 0;; ++spins) {
    bool ok = true;
#pragma unroll
    for (int i = 0; i < N; ++i) { const u64 v = __hip_atomic_load((gu64*)(ws + off + i * 512), RLX_AGENT); ok &= (v == tag) || !active; }
    if (__all(ok)) break;
    __builtin_amdgcn_s_sleep(SLP);
    if (spins > (1u << 17)) { set_flag(ws, SY_TMO, tag); break; }
    if ((spins & 63u) == 63u && __all(__hip_atomic_load((gu64*)(ws + SY_TMO), RLX_AGENT) == tag)) break;
  }
  __builtin_amdgcn_fence(__ATOMIC_ACQUIRE, "wavefront");
}
DI unsigned w1_line(int i, int total, int r0, int nl, unsigned c0) {
  const int rr = i / nl, l = i - rr * nl, row = r0 + rr;
  return i < total ? (unsigned)(64 * (row & 31) + (row >> 5)) * (F_ * 2) + c0 + (unsigned)l * 128u : 0xfffffff0u;
}

DI unsigned w2_line(int line, int g) {
  int row = (line >> 4) + (g & 1) * 576; if (row >= 1152) row -= 1152;
  return (unsigned)((g >> 1) * 1152 + row) * 4096u + (g & 1) * 2048u + (line & 15) * 128u;
}

template <bool F16> DI f32x16 mfma32(u32x4 a, u32x4 b, f32x16 c) {
  if constexpr (F16) return __builtin_amdgcn_mfma_f32_32x32x16_f16(__builtin_bit_cast(f16x8, a), __builtin_bit_cast(f16x8, b), c, 0, 0, 0);
  else return __builtin_amdgcn_mfma_f32_32x32x16_bf16(__builtin_bit_cast(bf16x8, a), __builtin_bit_cast(bf16x8, b), c, 0, 0, 0);
}
DI u32x4 pack8_f16(const f32x16& v, int u) {
  return u32x4{pack_f16(v[8 * u], v[8 * u + 1]), pack_f16(v[8 * u + 2], v[8 * u + 3]), pack_f16(v[8 * u + 4], v[8 * u + 5]), pack_f16(v[8 * u + 6], v[8 * u + 7])};
}
DI u32x4 tr_frag(const char* p0, const char* p1) {
  s16x4 lo = __builtin_amdgcn_ds_read_tr16_b64_v4i16((LAS s16x4*)p0);
  s16x4 hi = __builtin_amdgcn_ds_read_tr16_b64_v4i16((LAS s16x4*)p1);
  u32x2 a = __builtin_bit_cast(u32x2, lo), c = __builtin_bit_cast(u32x2, hi);
  return u32x4{a[0], a[1], c[0], c[1]};
}
DI void tw_piece(f32x16& re, f32x16& im, int p, const f32x4 w) {
  const f32x2 r = {re[2 * p], re[2 * p + 1]}, i = {im[2 * p], im[2 * p + 1]}, wr = {w[0], w[1]}, wi = {w[2], w[3]};
  const f32x2 a = __builtin_elementwise_fma(r, wr, -(i * wi)), b = __builtin_elementwise_fma(r, wi, i * wr);
  re[2 * p] = a[0]; re[2 * p + 1] = a[1]; im[2 * p] = b[0]; im[2 * p + 1] = b[1];
}
DI void tw_load(f32x4 (&t)[8], const char* tw) {
#pragma unroll
  for (int p = 0; p < 8; ++p) t[p] = *(const f32x4*)(tw + p * 1024);
}
DI void tw_apply(f32x16& re, f32x16& im, const f32x4 (&t)[8]) {
#pragma unroll
  for (int p = 0; p < 8; ++p) tw_piece(re, im, p, t[p]);
}
DI float cs(int which, float rev) {
  return which == 0 ? __builtin_amdgcn_cosf(rev) : (which == 1 ? -__builtin_amdgcn_sinf(rev) : __builtin_amdgcn_sinf(rev));
}
DI u32x4 e32_frag(int frag, int lane) {
  const int t = frag / 3, which = frag - 3 * t, r = lane & 31, h = lane >> 5;
  float o[8];
#pragma unroll
  for (int j = 0; j < 8; ++j) o[j] = cs(which, (float)(((16 * t + 8 * (j >> 2) + 4 * h + (j & 3)) * r) & 31) * (1.0f / 32.0f));
  return u32x4{pack_f16(o[0], o[1]), pack_f16(o[2], o[3]), pack_f16(o[4], o[5]), pack_f16(o[6], o[7])};
}

enum { LIN = 0, TR128 = 1, TR64 = 2 };

template <int V> struct IC { static constexpr int value = V; };
template <int NA_W, int NB_W, int WA, int WB, int AK, int BK, int NS, int DPM, int DEPTH>
struct GC {
  static constexpr int NC = WA * WB, NFA = NA_W * WA, NFB = NB_W * WB;
  static constexpr int A_BYTES = NFA * 4096, B_BYTES = NFB * 4096, STAGE = A_BYTES + B_BYTES;
  static constexpr int LDS_BYTES = NS * STAGE;
  static_assert(AK == LIN || NFA == (AK == TR128 ? 4 : 2), "");
  static_assert(BK == LIN || NFB == (BK == TR128 ? 4 : 2), "");

  template <int K> static DI unsigned voff(int lane, unsigned rstride) {
    if constexpr (K == LIN) return lane * 16;
    else if constexpr (K == TR128) return (unsigned)(lane >> 4) * rstride + (unsigned)(((lane & 15) ^ ((lane >> 4) << 2)) * 16);
    else return (unsigned)(lane >> 3) * rstride + (unsigned)(((lane & 7) ^ (((lane >> 4) & 1) << 2)) * 16);
  }
  template <int K, int NF> static DI void frag_addr(unsigned (&addr)[K == LIN ? 1 : NF], int opoff, int wpos, int lane) {
    if constexpr (K == LIN) addr[0] = opoff + wpos * NF * 4096 + lane * 16;
    else {
      const int h = lane >> 5, blk = (lane >> 4) & 1, q = (lane & 15) >> 2, p = lane & 3;
#pragma unroll
      for (int i = 0; i < NF; ++i) {
        const int ch = 4 * (wpos * NF + i) + 2 * blk + (p >> 1);
        if constexpr (K == TR128) addr[i] = opoff + (4 * h + q) * 256 + ((ch ^ (q << 2)) * 16) + 8 * (p & 1);
        else addr[i] = opoff + (4 * h + q) * 128 + ((ch ^ (((q >> 1) & 1) << 2)) * 16) + 8 * (p & 1);
      }
    }
  }
  template <int K, int NF> static DI void rd(u32x4 (&f)[NF], const char* smem, const unsigned (&addr)[K == LIN ? 1 : NF], int sb, int hh) {
#pragma unroll
    for (int i = 0; i < NF; ++i) {
      if constexpr (K == LIN) f[i] = *(const u32x4*)(smem + addr[0] + sb + (i * 4 + hh) * 1024);
      else {
        constexpr int RB = K == TR128 ? 256 : 128;
        const char* p = smem + addr[i] + sb + hh * 16 * RB;
        s16x4 lo = __builtin_amdgcn_ds_read_tr16_b64_v4i16((LAS s16x4*)p);
        s16x4 hi = __builtin_amdgcn_ds_read_tr16_b64_v4i16((LAS s16x4*)(p + 8 * RB));
        u32x2 a = __builtin_bit_cast(u32x2, lo), c = __builtin_bit_cast(u32x2, hi);
        f[i] = u32x4{a[0], a[1], c[0], c[1]};
      }
    }
  }
  static DI void ilv() {
    constexpr int NR = NA_W * (AK == LIN ? 1 : 2) + NB_W * (BK == LIN ? 1 : 2), NM = NA_W * NB_W;
    constexpr int NG = (NR + DPM - 1) / DPM < NM ? (NR + DPM - 1) / DPM : NM;
#pragma unroll
    for (int i = 0; i < NG; ++i) {
      __builtin_amdgcn_sched_group_barrier(0x8, 1, 0);
      if (i + 1 < NG) __builtin_amdgcn_sched_group_barrier(0x100, DPM, 0);
      else __builtin_amdgcn_sched_group_barrier(0x100, NR - DPM * (NG - 1), 0);
    }
    if constexpr (NM > NG) __builtin_amdgcn_sched_group_barrier(0x8, NM - NG, 0);
    SCHED0();
  }
  template <int G, class ISSUE> static DI void ring(int nt, ISSUE issue) {
    int wslot = NS - 2;
    for (int tau = 0; tau < nt - (NS - 2); ++tau) {
      WAIT_VM((NS - 3) * G);
      BAR();
      issue(tau + NS - 2, wslot * STAGE);
      wslot = (wslot + 1 == NS) ? 0 : wslot + 1;
    }
    if constexpr (NS >= 5) { WAIT_VM(2 * G); BAR(); }
    if constexpr (NS >= 4) { WAIT_VM(G); BAR(); }
    WAIT_VM(0); BAR();
    static_assert(NS >= 3 && NS <= 5, "");
  }
  template <bool F16>
  static DI void consume(const char* smem, int wave, int lane, int n, int& rslot, f32x16 (&acc)[NA_W][NB_W]) {
    const int wa = wave / WB, wb = wave - wa * WB;
    unsigned addrA[AK == LIN ? 1 : NA_W], addrB[BK == LIN ? 1 : NB_W];
    frag_addr<AK, NA_W>(addrA, 0, wa, lane);
    frag_addr<BK, NB_W>(addrB, A_BYTES, wb, lane);
    u32x4 fa[DEPTH][NA_W], fb[DEPTH][NB_W];
#pragma unroll
    for (int d = 1; d < DEPTH; ++d) {
#pragma unroll
      for (int i = 0; i < NA_W; ++i) fa[d][i] = u32x4{0, 0, 0, 0};
#pragma unroll
      for (int i = 0; i < NB_W; ++i) fb[d][i] = u32x4{0, 0, 0, 0};
    }
    auto mm = [&](const u32x4 (&a)[NA_W], const u32x4 (&b)[NB_W]) {
#pragma unroll
      for (int i = 0; i < NA_W; ++i)
#pragma unroll
        for (int j = 0; j < NB_W; ++j) acc[i][j] = mfma32<F16>(a[i], b[j], acc[i][j]);
    };
    auto step = [&](auto Z0) {
      constexpr int z0 = decltype(Z0)::value;
      BAR();
      SCHED0();
      const int sb = rslot * STAGE;
#pragma unroll
      for (int hh = 0; hh < 4; ++hh) {
        rd<AK, NA_W>(fa[(z0 + hh) % DEPTH], smem, addrA, sb, hh);
        rd<BK, NB_W>(fb[(z0 + hh) % DEPTH], smem, addrB, sb, hh);
        mm(fa[(z0 + hh + 1) % DEPTH], fb[(z0 + hh + 1) % DEPTH]);
        ilv();
      }
      rslot = (rslot + 1 == NS) ? 0 : rslot + 1;
    };
    if constexpr (DEPTH == 2) {
      for (int tau = 0; tau < n; ++tau) step(IC<0>{});
      mm(fa[1], fb[1]);
    } else {
      int tau = 0;
      for (; tau + 3 <= n; tau += 3) { step(IC<0>{}); step(IC<1>{}); step(IC<2>{}); }
      if (n - tau == 2) { step(IC<0>{}); step(IC<1>{}); mm(fa[0], fb[0]); mm(fa[1], fb[1]); }
      else if (n - tau == 1) { step(IC<0>{}); mm(fa[2], fb[2]); mm(fa[0], fb[0]); }
      else { mm(fa[1], fb[1]); mm(fa[2], fb[2]); }
    }
  }
};

DI float gelu_tanh(float u) {
  const float z = u * (1.0f + 0.044715f * u * u) * (-2.0f * 0.7978845608028654f * 1.4426950408889634f);
  return u * __builtin_amdgcn_rcpf(1.0f + __builtin_amdgcn_exp2f(z));
}

template <int NV> DI void mv_ilv() {
#pragma unroll
  for (int i = 0; i < 8; ++i) { __builtin_amdgcn_sched_group_barrier(0x8, 1, 0); __builtin_amdgcn_sched_group_barrier(0x2, NV, 0); }
  SCHED0();
}
struct EF { u32x4 r, i, n; };
DI EF ef_load(const char* p) { EF e; e.r = *(const u32x4*)p; e.i = *(const u32x4*)(p + 1024); e.n = *(const u32x4*)(p + 2048); return e; }
#define MF(a, b, c) mfma32<true>(a, b, c)
DI void cm_dA(f32x16& re, f32x16& im, u32x4 dr, u32x4 di, const EF& e) { re = MF(dr, e.r, re); im = MF(dr, e.i, im); re = MF(di, e.n, re); im = MF(di, e.r, im); }
DI void cm_dB(f32x16& re, f32x16& im, u32x4 dr, u32x4 di, const EF& e) { re = MF(e.r, dr, re); im = MF(e.i, dr, im); re = MF(e.n, di, re); im = MF(e.r, di, im); }
DI void cm_dAc(f32x16& re, f32x16& im, u32x4 dr, u32x4 di, const EF& e) { re = MF(dr, e.r, re); im = MF(dr, e.n, im); re = MF(di, e.i, re); im = MF(di, e.r, im); }
DI void wmul(f32x16& re, f32x16& im, const u32x4 (&wv)[4], float wsc) {
#pragma unroll
  for (int p = 0; p < 8; ++p) {
    const unsigned w0 = wv[p >> 1][(2 * p) & 3], w1 = wv[p >> 1][(2 * p + 1) & 3];
    tw_piece(re, im, p, f32x4{lo16(w0), lo16(w1), hi16(w0), hi16(w1)} * wsc);
  }
}
DI void bfly(f32x16& a, f32x16& b) {
  const f32x16 x = a, y = b; a = x + y; b = x - y;
}
DI void colfft_wave(const char* tab  , const char* ws, int c, int lane, float wsc, const u32x4 (&zr)[2][2], const u32x4 (&zi)[2][2],
                    char* img, int slot_re, int slot_im) {
  f32x4 tw[8];
  f32x16 r0 = {}, i0 = {}, r1 = {}, i1 = {};
  u32x4 ar[2], ai[2];
  EF ea = ef_load(tab + TAB_E1), eb = ef_load(tab + TAB_E1 + 3072);
  tw_load(tw, tab + TAB_TW1);
  SCHED0();
  cm_dA(r0, i0, zr[0][0], zi[0][0], ea); cm_dA(r0, i0, zr[0][1], zi[0][1], eb);
  SCHED0();
  cm_dA(r1, i1, zr[1][0], zi[1][0], ea); cm_dA(r1, i1, zr[1][1], zi[1][1], eb);
#pragma unroll
  for (int p = 0; p < 8; ++p) { tw_piece(r0, i0, p, tw[p]); tw[p] = *(const f32x4*)(tab + TAB_TW1 + 8192 + p * 1024); }
  ar[0] = pack8_f16(r0, 0); ar[1] = pack8_f16(r0, 1); ai[0] = pack8_f16(i0, 0); ai[1] = pack8_f16(i0, 1);
  mv_ilv<6>();
  u32x4 wv0[4], wv1[4];
#pragma unroll
  for (int i = 0; i < 4; ++i) wv0[i] = load_dev(ws, OFF_WT + c * 8192u + i * 1024 + lane * 16);
  r0 = f32x16{}; i0 = f32x16{};
  cm_dB(r0, i0, ar[0], ai[0], ea); ea = ef_load(tab + TAB_EO);
  cm_dB(r0, i0, ar[1], ai[1], eb); eb = ef_load(tab + TAB_EO + 3072);
  tw_apply(r1, i1, tw);
  ar[0] = pack8_f16(r1, 0); ar[1] = pack8_f16(r1, 1); ai[0] = pack8_f16(i1, 0); ai[1] = pack8_f16(i1, 1);
  mv_ilv<6>();
#pragma unroll
  for (int i = 0; i < 4; ++i) wv1[i] = load_dev(ws, OFF_WT + c * 8192u + (4 + i) * 1024 + lane * 16);
  r1 = f32x16{}; i1 = f32x16{};
  cm_dB(r1, i1, ar[0], ai[0], ea); ea = ef_load(tab + TAB_E1);
  cm_dB(r1, i1, ar[1], ai[1], eb); eb = ef_load(tab + TAB_E1 + 3072);
  SCHED0();
  bfly(r0, r1); bfly(i0, i1);
  wmul(r0, i0, wv0, wsc);
  wmul(r1, i1, wv1, wsc);
  bfly(r0, r1); bfly(i0, i1);
  u32x4 dr[2], di[2];
  ar[0] = pack8_f16(r0, 0); ar[1] = pack8_f16(r0, 1); ai[0] = pack8_f16(i0, 0); ai[1] = pack8_f16(i0, 1);
  SCHED0();
  tw_load(tw, tab + TAB_TW2);
  r0 = f32x16{}; i0 = f32x16{};
  cm_dAc(r0, i0, ar[0], ai[0], ea); ea = ef_load(tab + TAB_EOI);
  cm_dAc(r0, i0, ar[1], ai[1], eb); eb = ef_load(tab + TAB_EOI + 3072);
  dr[0] = pack8_f16(r1, 0); dr[1] = pack8_f16(r1, 1); di[0] = pack8_f16(i1, 0); di[1] = pack8_f16(i1, 1);
  mv_ilv<2>();
  r1 = f32x16{}; i1 = f32x16{};
  cm_dAc(r1, i1, dr[0], di[0], ea); ea = ef_load(tab + TAB_E1);
  cm_dAc(r1, i1, dr[1], di[1], eb); eb = ef_load(tab + TAB_E1 + 3072);
#pragma unroll
  for (int p = 0; p < 8; ++p) { tw_piece(r0, i0, p, tw[p]); tw[p] = *(const f32x4*)(tab + TAB_TW2 + 8192 + p * 1024); }
  ar[0] = pack8_f16(r0, 0); ar[1] = pack8_f16(r0, 1); ai[0] = pack8_f16(i0, 0); ai[1] = pack8_f16(i0, 1);
  mv_ilv<6>();
  r0 = f32x16{}; i0 = f32x16{};
  cm_dAc(r0, i0, ar[0], ai[0], ea); cm_dAc(r0, i0, ar[1], ai[1], eb);
  tw_apply(r1, i1, tw);
  dr[0] = pack8_f16(r1, 0); dr[1] = pack8_f16(r1, 1); di[0] = pack8_f16(i1, 0); di[1] = pack8_f16(i1, 1);
  mv_ilv<6>();
  r1 = f32x16{}; i1 = f32x16{};
  cm_dAc(r1, i1, dr[0], di[0], ea); cm_dAc(r1, i1, dr[1], di[1], eb);
  SCHED0();
#pragma unroll
  for (int g = 0; g < 4; ++g) {
    const int Q = (lane & 31) * 8 + 2 * g + (lane >> 5), sw = lane & 15, r = 4 * g;
    if (slot_im == -2) {
      *(u32x4*)(img + Q * 16) = u32x4{pack_f16(r0[r], r1[r]), pack_f16(r0[r + 1], r1[r + 1]), pack_f16(r0[r + 2], r1[r + 2]), pack_f16(r0[r + 3], r1[r + 3])};
      continue;
    }
#pragma unroll
    for (int z = 0; z < 2; ++z) {
      const int q = r + 2 * z;
      *(u32x4*)(img + Q * 256 + (((slot_re + z) ^ sw) * 16)) =
          u32x4{pack_f16(r0[q], i0[q]), pack_f16(r1[q], i1[q]), pack_f16(r0[q + 1], i0[q + 1]), pack_f16(r1[q + 1], i1[q + 1])};
    }
  }
}
#undef MF
constexpr int FIN_OS = 2336;
DI void irfft_wave(const char* rec, const char* y288, int w, int lane, char* orow, const char* kt) {
  const int row = lane & 31, hh = lane >> 5, k1 = row & 15, rr = 2 * w + (row >> 4);
  const bool ok = k1 < 10;
  const int k1c = ok ? k1 : 0;
  const char* pd = rec + rr * 4 + 32 * (k1c + 72 * hh);
  const char* pm = rec + rr * 4 + 32 * (576 - k1c - 72 * hh - 18 * 27);
  unsigned v[2][8];
#pragma unroll
  for (int j = 0; j < 8; ++j) {
    v[0][j] = *(const unsigned*)(pd + 576 * (8 * (j >> 2) + (j & 3)));
    v[1][j] = *(const unsigned*)(pm + 576 * (27 - (16 + 8 * (j >> 2) + (j & 3))));
  }
  const unsigned v288 = *(const u16*)(y288 + rr * 2);
  f32x4 tw[8];
  tw_load(tw, kt + 8192);
  if (k1 == 0 && hh == 0) { v[0][0] &= 0xffffu; v[1][0] = v288; }
  f32x16 re = {}, im = {};
#pragma unroll
  for (int u = 0; u < 2; ++u) {
    u32x4 a_r, a_i;
#pragma unroll
    for (int e = 0; e < 4; ++e) {
      a_r[e] = ok ? __builtin_amdgcn_perm(v[u][2 * e + 1], v[u][2 * e], 0x05040100u) : 0u;
      a_i[e] = ok ? (__builtin_amdgcn_perm(v[u][2 * e + 1], v[u][2 * e], 0x07060302u) ^ (u ? 0x80008000u : 0u)) : 0u;
    }
    cm_dAc(re, im, a_r, a_i, ef_load(kt + 2048 + u * 3072));
  }
  const u32x4 cr = *(const u32x4*)kt, ci = *(const u32x4*)(kt + 1024);
  tw_apply(re, im, tw);
#pragma unroll
  for (int u = 0; u < 2; ++u) {
    f32x16 y = {};
    y = mfma32<true>(cr, pack8_f16(re, u), y);
    y = mfma32<true>(ci, pack8_f16(im, u), y);
    float* o = (float*)(orow + u * FIN_OS) + (lane & 31) + 128 * hh;
#pragma unroll
    for (int rho = 0; rho < 8; ++rho) o[32 * ((rho & 3) + 8 * (rho >> 2))] = y[rho];
    if (hh == 0) { o[32 * 16] = y[8]; o[32 * 17] = y[9]; }
  }
}

DI unsigned wmax_exp(const char* ws, int lane) {
  const u32x4 mv = load_dev(ws, OFF_WMAX + lane * 16);
  unsigned mx = max(max(mv[0], mv[1]), max(mv[2], mv[3]));
#pragma unroll
  for (int o = 32; o; o >>= 1) mx = max(mx, (unsigned)__shfl_xor((int)mx, o));
  return min(mx >> 7, 252u);
}

DI unsigned lds_arrive(char* p) { return __hip_atomic_fetch_add((LAS unsigned*)p, 1u, __ATOMIC_RELAXED, __HIP_MEMORY_SCOPE_WORKGROUP); }
DI void lds_arrive_wait(char* p, unsigned n, int lane) {
  if (lane == 0) lds_arrive(p);
  while (__hip_atomic_load((LAS unsigned*)p, __ATOMIC_RELAXED, __HIP_MEMORY_SCOPE_WORKGROUP) < n) __builtin_amdgcn_s_sleep(1);
  asm volatile("" ::: "memory");
}
DI void lds_wait(const char* p) {
  while (__hip_atomic_load((const LAS unsigned*)p, __ATOMIC_RELAXED, __HIP_MEMORY_SCOPE_WORKGROUP) == 0u) __builtin_amdgcn_s_sleep(1);
  asm volatile("" ::: "memory");
}
using CG1 = GC<1, 3, 4, 1, TR128, LIN, 5, 2, 3>;
using CG2 = GC<3, 2, 2, 2, LIN, TR128, 4, 2, 2>;
constexpr int P1_RS = 1216, P1_XS = 3 * CG1::STAGE, P1_HS = P1_XS + 8 * P1_RS, P1_C1 = P1_XS + 16 * P1_RS, P1_E = P1_C1 + 6144,
              P1_TW = P1_E + 6144, P1_O = P1_TW + 8192, P1_WT = P1_O + 9216, P1_RED = P1_WT + 10512, P1_DUMMY = 147456;
static_assert(P1_RED + 48 <= P1_DUMMY && P1_DUMMY >= CG1::LDS_BYTES, "");

__global__ __launch_bounds__(768, 3) void k_all(const u16* __restrict__ x, const u16* __restrict__ wr, const u16* __restrict__ wi,
                                                const char* __restrict__ w1, const u16* __restrict__ b1, const char* __restrict__ w2,
                                                const u16* __restrict__ b2, char* __restrict__ ws, u16* __restrict__ out) {
  extern __shared__ __attribute__((aligned(16))) char smem[];
  const int lane = threadIdx.x & 63, wave = __builtin_amdgcn_readfirstlane(threadIdx.x >> 6);
  const int b = blockIdx.x, g = b & 7, idx = b >> 3;
  const unsigned ldsb = (unsigned)(size_t)(LAS char*)smem;
  const u32x4 rs_ws = mk_rsrc(ws, WS_END), rs_w1 = mk_rsrc(w1, 18874368u), rs_w2 = mk_rsrc(w2, 18874368u);
  const int tile = g * 27 + idx, ft = tile / 6, dt1 = tile - ft * 6;
  const int ft0 = (g * 27) / 6, nl1 = ((g * 27 + 26) / 6 - ft0 + 1) * 2;
  auto issue_w1x = [&](int lw, int kt, int so) {
    constexpr unsigned ld = F_ * 2;
    const unsigned vo = CG1::voff<TR128>(lane, 64 * ld);
#pragma unroll
    for (int m = 0; m < 4; ++m) {
      const int a = lw * 4 + m;
      dma16<0>(rs_w1, ldsb + so + a * 1024, vo, (unsigned)(((4 * a) & 31) * 64 + 2 * kt + (a >> 3)) * ld + ft * 256u);
    }
  };
  const int tb = (g >> 2) * 128 + idx * 4 + (g & 3);
  auto gate = [&] {
    while (__hip_atomic_load((const LAS unsigned*)(smem + P1_RED + 16), __ATOMIC_RELAXED, __HIP_MEMORY_SCOPE_WORKGROUP) < 4u) __builtin_amdgcn_s_sleep(1);
  };
  u64 tag;
  if (wave < 4) {
    const int tid = threadIdx.x, s2 = tb >> 2, t = (tb >> 1) & 1, h = tb & 1;
    const int rr = tid >> 5, l32 = tid & 31;
    const int s1 = 16 * t + 8 * (rr >> 2) + 4 * h + (rr & 3);
    const unsigned* xr = (const unsigned*)(x + (size_t)(64 * s1 + s2) * D_);
    unsigned raw[9];
#pragma unroll
    for (int i = 0; i < 9; ++i) raw[i] = xr[32 * i + l32];
    float v[18];
#pragma unroll
    for (int i = 0; i < 9; ++i) { v[2 * i] = lo16(raw[i]); v[2 * i + 1] = hi16(raw[i]); }
#pragma unroll
    for (int pass = 0; pass < 2; ++pass) {
      float sm = 0.f;
#pragma unroll
      for (int i = 0; i < 18; ++i) sm += v[i];
      const float mean = sum32(sm) * (1.0f / 576.0f);
      float sq = 0.f;
#pragma unroll
      for (int i = 0; i < 18; ++i) { float dlt = v[i] - mean; sq += dlt * dlt; }
      const float var = sum32(sq) * (1.0f / 576.0f);
      const float mb = rbf(mean);
      const float sd = rbf(__builtin_sqrtf(rbf(rbf(var) + 1e-5f)));
      const float rs = 1.0f / sd;
      char* dst = smem + P1_XS + pass * (8 * P1_RS) + rr * P1_RS + l32 * 4;
#pragma unroll
      for (int i = 0; i < 9; ++i) {
        unsigned p = pack_bf16(v[2 * i] - mb, v[2 * i + 1] - mb);
        p = pack_bf16(lo16(p) * rs, hi16(p) * rs);
        v[2 * i] = lo16(p); v[2 * i + 1] = hi16(p);
        *(unsigned*)(dst + i * 128) = pass == 0 ? pack_f16(v[2 * i], v[2 * i + 1]) : p;
      }
    }
    WAIT_LGKM0();
    BAR();
    { const u64 tv = *(const u64*)(smem + P1_RED + 24);
      tag = (u64)__builtin_amdgcn_readfirstlane((unsigned)(tv >> 32)) << 32 | (unsigned)__builtin_amdgcn_readfirstlane((unsigned)tv); }
    {
      const int hh = lane >> 5, blk = (lane >> 4) & 1, q = (lane & 15) >> 2, p = lane & 3;
      const char* xa = smem + P1_XS + (2 * wave) * P1_RS + blk * 32 + p * 8;
      const char* cf = smem + P1_C1 + lane * 16;
      f32x16 re = {}, im = {};
      f32x4 tw[8];
      tw_load(tw, smem + P1_TW + lane * 16);
#pragma unroll
      for (int ks = 0; ks < 3; ++ks) {
        const char* a0 = ks < 2 ? xa + ks * P1_RS + (4 * hh + q) * 64 : xa + (q >> 1) * P1_RS + (16 + (q & 1)) * 64;
        const u32x4 a = tr_frag(a0, ks < 2 ? a0 + 512 : a0);
        re = mfma32<true>(a, *(const u32x4*)(cf + (2 * ks) * 1024), re);
        im = mfma32<true>(a, *(const u32x4*)(cf + (2 * ks + 1) * 1024), im);
      }
      tw_apply(re, im, tw);
      f32x16 xr_ = {}, xi_ = {};
#pragma unroll
      for (int u = 0; u < 2; ++u) cm_dB(xr_, xi_, pack8_f16(re, u), pack8_f16(im, u), ef_load(smem + P1_E + u * 3072 + lane * 16));
      const int col = lane & 31, k1 = col & 15, row2 = 2 * (2 * wave + (col >> 4));
      if (k1 < 10) {
        char* o1 = smem + P1_O + 32 * k1 + 576 * 4 * hh + row2;
        char* o2 = smem + P1_O + 32 * (576 - k1) - 576 * (4 * hh + 27) + row2;
#pragma unroll
        for (int rho = 0; rho < 8; ++rho) {
          const unsigned pk = pack_f16(xr_[rho] * (1.0f / 32.0f), xi_[rho] * (1.0f / 32.0f));
          char* d = o1 + 576 * ((rho & 3) + 8 * (rho >> 2));
          *(u16*)d = (u16)pk;
          if (rho != 0 || k1 != 0 || hh != 0) *(u16*)(d + 16) = (u16)(pk >> 16);
        }
        if (k1 >= 1 && k1 <= 8) {
#pragma unroll
          for (int rho = 8; rho < 16; ++rho) {
            const unsigned pk = pack_f16(xr_[rho] * (1.0f / 32.0f), xi_[rho] * (-1.0f / 32.0f));
            char* d = o2 + 576 * (27 - ((rho & 3) + 8 * (rho >> 2)));
            *(u16*)d = (u16)pk;
            *(u16*)(d + 16) = (u16)(pk >> 16);
          }
        } else if (k1 == 0 && hh == 0) {
          *(u16*)(smem + P1_O + 16 + row2) = (u16)pack_f16(xr_[8] * (1.0f / 32.0f), 0.f);
        }
      }
    }
    WAIT_LGKM0();
    lds_arrive_wait(smem + P1_RED + 32, 4u, lane);
    gate();
    const unsigned P = ((s2 & 1) * 2 + t) * 64 + h * 32 + (s2 >> 1);
#pragma unroll
    for (int it = 0; it < 3; ++it) {
      const int p = tid + it * 256;
      if (p < 576) store_dev(ws, OFF_X1 + P * 9216u + p * 16, *(const u32x4*)(smem + P1_O + p * 16));
    }
    WAIT_VM(0);
    if (lane == 0 && lds_arrive(smem + P1_RED + 36) == 8u) set_flag(ws, SY_A + b * 8, tag);
  } else if (wave < 8) {
    const int tid = threadIdx.x - 256;
    const int rg = b >> 2, cg = b & 3, c0 = cg * 72 + (cg > 0), nc = cg == 0 ? 73 : 72, kq = rg >> 2, ko = rg & 3;
    unsigned rv[10], iv[10];
#pragma unroll
    for (int it = 0; it < 10; ++it) {
      const int i = tid + it * 256, row = i / 73, col = i - row * 73;
      const bool ok = i < 32 * 73 && col < nc;
      const int off = ok ? (8 * ko + (row & 7) + 32 * (4 * kq + (row >> 3))) * NC_ + c0 + col : 0;
      rv[it] = wr[off]; iv[it] = wi[off];
    }
    if (tid == 0) {
      const u64 ep = __hip_atomic_load((gu64*)(ws + SY_EPOCH), RLX_AGENT);
      *(u64*)(smem + P1_RED + 24) = ep * 6364136223846793005ull + 1442695040888963407ull;
    }
    unsigned mx = 0;
#pragma unroll
    for (int it = 0; it < 10; ++it) {
      const int i = tid + it * 256, row = i / 73, col = i - row * 73;
      if (i < 32 * 73 && col < nc) {
        mx = max(mx, max(rv[it] & 0x7fffu, iv[it] & 0x7fffu));
        *(unsigned*)(smem + P1_WT + col * 144 + (row & 7) * 16 + (row >> 3) * 4) = rv[it] | (iv[it] << 16);
      }
    }
#pragma unroll
    for (int o = 32; o; o >>= 1) mx = max(mx, (unsigned)__shfl_xor((int)mx, o));
    unsigned* red = (unsigned*)(smem + P1_RED);
    if (lane == 0) red[wave - 4] = mx;
    WAIT_LGKM0();
    BAR();
    { const u64 tv = *(const u64*)(smem + P1_RED + 24);
      tag = (u64)__builtin_amdgcn_readfirstlane((unsigned)(tv >> 32)) << 32 | (unsigned)__builtin_amdgcn_readfirstlane((unsigned)tv); }
    if (tid == 0) __hip_atomic_store((gu32*)(ws + OFF_WMAX + b * 4), max(max(red[0], red[1]), max(red[2], red[3])), RLX_AGENT);
    const int U = kq >> 3, h = kq & 1, rq = (kq >> 1) & 3;
    gate();
#pragma unroll
    for (int it = 0; it < 3; ++it) {
      const int p = tid + it * 256;
      if (p < nc * 8) {
        const int col = p >> 3, a = p & 7;
        store_dev(ws, OFF_WT + (unsigned)(c0 + col) * 8192u + ((U * 4 + rq) * 64 + h * 32 + 8 * ko + a) * 16,
                  *(const u32x4*)(smem + P1_WT + col * 144 + a * 16));
      }
    }
    WAIT_VM(0);
    if (lane == 0 && lds_arrive(smem + P1_RED + 36) == 8u) set_flag(ws, SY_A + b * 8, tag);
    if (idx < 27) { issue_w1x(wave - 4, 0, 0); issue_w1x(wave - 4, 1, CG1::STAGE); issue_w1x(wave - 4, 2, 2 * CG1::STAGE); }
  } else {
#pragma unroll
    for (int z = 0; z < 5; ++z) {
      const int fr = (wave - 8) + 4 * z;
      u32x4 val;
      const int col = lane & 31, hh = lane >> 5, r = col >> 4, k1 = col & 15;
      if (fr < 6) {
        const int ks = fr >> 1, which = fr & 1;
        float o[8];
#pragma unroll
        for (int j = 0; j < 8; ++j) {
          int row, d1;
          if (ks < 2) { row = ks; d1 = 8 * (j >> 2) + 4 * hh + (j & 3); }
          else { row = (hh == 0 && j < 4) ? (j >> 1) : 2; d1 = 16 + (j & 1); }
          const float v = cs(which, (float)((d1 * k1) % 18) * (1.0f / 18.0f));
          o[j] = (row == r && k1 < 10) ? v : 0.f;
        }
        val = u32x4{pack_f16(o[0], o[1]), pack_f16(o[2], o[3]), pack_f16(o[4], o[5]), pack_f16(o[6], o[7])};
      } else if (fr < 12) {
        val = e32_frag(fr - 6, lane);
      } else {
#pragma unroll
        for (int y = 0; y < 2; ++y) {
          const int rho = 2 * (fr - 12) + y, d2 = (rho & 3) + 8 * (rho >> 2) + 4 * hh;
          const float a = (float)(d2 * k1) * (1.0f / 576.0f);
          val[y] = __float_as_uint(__builtin_amdgcn_cosf(a)); val[2 + y] = __float_as_uint(-__builtin_amdgcn_sinf(a));
        }
      }
      *(u32x4*)(smem + P1_C1 + fr * 1024 + lane * 16) = val;
      if (z == 2) {
        dma4(rs_w1, ldsb + P1_DUMMY + wave * 256, w1_line((idx * 4 + (wave - 8)) * 64 + lane, PF1_ROWS * nl1, 0, nl1, ft0 * 256u));
        if (wave == 8) dma4(rs_w1, ldsb + P1_DUMMY + wave * 256, w1_line((128 + idx) * 64 + lane, PF1_ROWS * nl1, 0, nl1, ft0 * 256u));
      }
    }
    if (threadIdx.x == 512) { *(unsigned*)(smem + P1_RED + 16) = 0u; *(u64*)(smem + P1_RED + 32) = 0ull; *(unsigned*)(smem + P1_RED + 40) = 0u; }
    if (wave == 8 && b < 66) {
      const int r = lane & 31, h = lane >> 5;
      u32x4 val;
      if (b < 6) val = e32_frag(b, lane);
      else if (b < 18) {
        const bool inv = b >= 12;
        const int f2 = inv ? b - 12 : b - 6, u = f2 / 3, which = f2 - 3 * u;
        float o[8];
#pragma unroll
        for (int j = 0; j < 8; ++j) {
          const int kap = 16 * u + 8 * (j >> 2) + 4 * h + (j & 3);
          o[j] = cs(which, (float)((inv ? kap * (2 * r + 1) : r * (2 * kap + 1)) & 63) * (1.0f / 64.0f));
        }
        val = u32x4{pack_f16(o[0], o[1]), pack_f16(o[2], o[3]), pack_f16(o[4], o[5]), pack_f16(o[6], o[7])};
      } else if (b < 50) {
        const bool inv = b >= 34;
        const int q = inv ? b - 34 : b - 18, p = q & 7, T = q >> 3;
#pragma unroll
        for (int z = 0; z < 2; ++z) {
          const int rho = 2 * p + z, cr = (rho & 3) + 8 * (rho >> 2) + 4 * h;
          const int n = inv ? (2 * r + T) * cr : (2 * cr + T) * r;
          const float a = (float)(n & 2047) * (1.0f / 2048.0f);
          val[z] = __float_as_uint(__builtin_amdgcn_cosf(a) * (inv ? 0.125f : 1.0f));
          val[2 + z] = __float_as_uint(inv ? __builtin_amdgcn_sinf(a) * 0.125f : -__builtin_amdgcn_sinf(a));
        }
      } else if (b < 52) {
        float o[8];
#pragma unroll
        for (int j = 0; j < 8; ++j) {
          const int k1 = 8 * (j >> 2) + 4 * h + (j & 3);
          const float v = cs(b - 50, (float)((r * k1) % 18) * (1.0f / 18.0f)) * ((k1 == 0 || k1 == 9) ? 1.f : 2.f);
          o[j] = (k1 < 10 && r < 18) ? v : 0.f;
        }
        val = u32x4{pack_f16(o[0], o[1]), pack_f16(o[2], o[3]), pack_f16(o[4], o[5]), pack_f16(o[6], o[7])};
      } else if (b < 58) {
        val = e32_frag(b - 52, lane);
      } else {
#pragma unroll
        for (int y = 0; y < 2; ++y) {
          const int rho = 2 * (b - 58) + y, k1 = ((rho & 3) + 8 * (rho >> 2) + 4 * h) & 15;
          const float a = (float)(k1 * r) * (1.0f / 576.0f);
          val[y] = __float_as_uint(__builtin_amdgcn_cosf(a) * 0.125f); val[2 + y] = __float_as_uint(__builtin_amdgcn_sinf(a) * 0.125f);
        }
      }
      store_dev(ws, (b < 50 ? OFF_TAB + b * 1024u : OFF_RT + (b - 50) * 1024u) + lane * 16, val);
    }
    WAIT_LGKM0();
    BAR();
    { const u64 tv = *(const u64*)(smem + P1_RED + 24);
      tag = (u64)__builtin_amdgcn_readfirstlane((unsigned)(tv >> 32)) << 32 | (unsigned)__builtin_amdgcn_readfirstlane((unsigned)tv); }
    {
      const int s2 = tb >> 2, t = (tb >> 1) & 1, h = tb & 1;
      for (int cb = wave - 8; cb < 9; cb += 4) {
        const int gg = lane >> 4, q = (lane & 15) >> 2, pp = lane & 3;
        const char* src = smem + P1_HS + q * P1_RS + (cb * 64 + gg * 16 + pp * 4) * 2;
        const int df = cb * 2 + (gg >> 1), i = 8 * q + 4 * (gg & 1) + pp;
        store_dev(ws, OFF_H + (unsigned)(df * 128 + 2 * s2 + t) * 1024u + (h * 32 + i) * 16, tr_frag(src, src + 4 * P1_RS));
      }
      WAIT_VM(0);
      unsigned last = 0;
      if (lane == 0) last = lds_arrive(smem + P1_RED + 16);
      if (__builtin_amdgcn_readfirstlane(last) == 3u) { if (lane < 8) set_flag(ws, SY_H + lane * 2048 + tb * 8, tag); }
      if (wave == 8 && lane == 0 && lds_arrive(smem + P1_RED + 36) == 8u) set_flag(ws, SY_A + b * 8, tag);
    }
  }
  if (idx < 27) {
    char* ebuf = smem + (29 % 5) * CG1::STAGE;
    u32x2 bv[3][2];
#pragma unroll
    for (int it = 0; it < 3; ++it) {
      const u32x4* bp = (const u32x4*)(b1 + ft * 128 + (min(wave + 11 * it, 23) / 3) * 16);
      const u32x4 b0 = bp[0], b1v = bp[1];
      const bool hi = lane >= 32;
      bv[it][0] = u32x2{hi ? b0[2] : b0[0], hi ? b0[3] : b0[1]}; bv[it][1] = u32x2{hi ? b1v[2] : b1v[0], hi ? b1v[3] : b1v[1]};
      asm volatile("" : "+v"(bv[it][0]), "+v"(bv[it][1]));
    }
    if (wave == 11) {
      const unsigned fh = SY_H + g * 2048 + lane * 8;
      poll_flags<1>(ws, fh, true, tag);
      if (lane == 0) lds_arrive(smem + P1_RED + 40);
      bool ok[3];
#pragma unroll
      for (int q = 0; q < 3; ++q) ok[q] = __all(__hip_atomic_load((gu64*)(ws + fh + (q + 1) * 512), RLX_AGENT) == tag);
      for (int tau = 0; tau < 32; ++tau) {
#pragma unroll
        for (int q = 0; q < 3; ++q)
          if (tau == 8 * q + 5 && !ok[q]) poll_flags<1>(ws, fh + (q + 1) * 512, true, tag);
        if (tau == 21 && b == 0 && lane == 0) set_flag(ws, SY_EPOCH, tag);
        BAR();
        if (tau == 29) {
#pragma unroll
          for (int i = 0; i < PF2T; ++i) dma4(rs_w2, ldsb + P1_DUMMY + wave * 256, w2_line((i * 27 + idx) * 64 + lane, g));
        }
      }
    } else if (wave >= 8) {
      auto issue_h = [&](int kt, int so) {
#pragma unroll
        for (int m = 0; m < 4; ++m)
          dma16<2>(rs_ws, ldsb + so + CG1::A_BYTES + ((wave - 8) * 4 + m) * 1024, lane * 16,
                   OFF_H + (unsigned)((dt1 * 3 + (wave - 8)) * 128 + kt * 4 + m) * 1024u);
      };
      lds_wait(smem + P1_RED + 40);
      issue_h(0, 0); issue_h(1, CG1::STAGE); issue_h(2, 2 * CG1::STAGE);
      CG1::ring<4>(32, issue_h);
    } else if (wave >= 4) {
      CG1::ring<4>(32, [&](int kt, int so) { issue_w1x(wave - 4, kt, so); });
    } else {
      f32x16 acc[1][3] = {};
      int rslot = 0;
      CG1::consume<false>(smem, wave, lane, 32, rslot, acc);
#pragma unroll
      for (int j = 0; j < 3; ++j)
#pragma unroll
        for (int s = 0; s < 2; ++s) {
          u32x4 val;
#pragma unroll
          for (int e = 0; e < 4; ++e) val[e] = pack_bf16(acc[0][j][8 * s + 2 * e], acc[0][j][8 * s + 2 * e + 1]);
          *(u32x4*)(ebuf + ((wave * 2 + s) * 3 + j) * 1024 + lane * 16) = val;
        }
    }
    WAIT_LGKM0();
    BAR();
    if (wave < 11) {
#pragma unroll
      for (int it = 0; it < 3; ++it) {
        const int i = wave + 11 * it;
        if (i < 24) {
          const int z = i / 3, j = i - 3 * z;
          const u32x4 a = *(const u32x4*)(ebuf + i * 1024 + lane * 16);
          u32x4 val;
#pragma unroll
          for (int e = 0; e < 4; ++e) {
            const unsigned bb = bv[it][e >> 1][e & 1];
            const unsigned pk = pack_bf16(lo16(a[e]) + lo16(bb), hi16(a[e]) + hi16(bb));
            val[e] = pack_bf16(gelu_tanh(lo16(pk)), gelu_tanh(hi16(pk)));
          }
          store_dev(ws, OFF_U + (unsigned)((dt1 * 3 + j) * 288 + ft * 8 + z) * 1024u + lane * 16, val);
        }
      }
      WAIT_VM(0);
    } else if (lane == 0) *(unsigned*)(smem + 163832) = 0u;
    WAIT_LGKM0();
    BAR();
    if (threadIdx.x < 2) set_flag(ws, SY_U + threadIdx.x * 2048 + tile * 8, tag);
  } else {
    const int me = (idx - 27) * 8 + g;
    {
      const int total = (2048 - PF1_ROWS) * nl1;
      for (int i0 = ((idx - 27) * 12 + wave) * 64; i0 < total; i0 += 60 * 64) {
        dma4(rs_w1, ldsb + P1_DUMMY + wave * 256, w1_line(i0 + lane, total, PF1_ROWS, nl1, ft0 * 256u));
        WAIT_VM(7);
      }
    }
    if (me < 37) {
      if (wave == 8) poll_flags<4>(ws, SY_A + lane * 8, true, tag);
      BAR();
      const int cbk = me == 36 ? 0 : me;
      char* img = smem + 65536;
      for (int i = wave; i < 64; i += 12) {
        const int P = 4 * i + (lane >> 4);
        dma16<2>(rs_ws, ldsb + 65536 + i * 1024, (unsigned)P * 9216u + (unsigned)(((lane & 15) ^ (P & 15)) * 16), OFF_X1 + cbk * 256u);
      }
      for (int i = wave; i < TAB_BYTES / 1024; i += 12) dma16<2>(rs_ws, ldsb + i * 1024, lane * 16, OFF_TAB + i * 1024u);
      const bool act = me == 36 ? wave == 0 : wave < 8;
      const int c = me == 36 ? 288 : me * 8 + wave;
      const float wsc = __uint_as_float((253u - wmax_exp(ws, lane)) << 23) * (1.0f / 64.0f);
      WAIT_VM(0);
      BAR();
      const int slot_re = me == 36 ? 1 : 2 * wave, slot_im = c == 288 ? -2 : (c == 0 ? -1 : 2 * wave + 1);
      u32x4 zr[2][2], zi[2][2];
      if (act) {
#pragma unroll
        for (int z = 0; z < 4; ++z) {
          const char* p = img + (z * 64 + lane) * 256;
          zr[z >> 1][z & 1] = *(const u32x4*)(p + ((slot_re ^ (lane & 15)) * 16));
          zi[z >> 1][z & 1] = slot_im >= 0 ? *(const u32x4*)(p + ((slot_im ^ (lane & 15)) * 16)) : u32x4{0, 0, 0, 0};
        }
      }
      WAIT_LGKM0();
      BAR();
      if (act) colfft_wave(smem + lane * 16, ws, c, lane, wsc, zr, zi, img, slot_re, slot_im);
      else if (wave >= 8 && idx < 31) {
        __builtin_amdgcn_s_sleep(64);
        for (int i0 = ((idx - 27) * 4 + wave - 8) * 64; i0 < PF2C_ROWS * 16; i0 += 16 * 64) {
          dma4(rs_w2, ldsb + P1_DUMMY + wave * 256, w2_line(PF2T * 27 * 64 + i0 + lane, g));
          WAIT_VM(7);
        }
      }
      WAIT_LGKM0();
      BAR();
      if (me == 36) {
        if (wave < 4) store_dev(ws, OFF_Y288 + wave * 1024 + lane * 16, *(const u32x4*)(img + wave * 1024 + lane * 16));
      } else {
        for (int i = wave; i < 64; i += 12) {
          const int Qy = 4 * i + (lane >> 4);
          store_dev(ws, OFF_Y1 + (unsigned)Qy * 9216u + cbk * 256 + (lane & 15) * 16,
                    *(const u32x4*)(img + Qy * 256 + (((lane & 15) ^ ((Qy >> 3) & 15)) * 16)));
        }
      }
      WAIT_VM(0);
      BAR();
      if (threadIdx.x == 0) set_flag(ws, SY_Y + me * 8, tag);
    }
  }
  if (idx < 24) {
    const int ks = g >> 1, dt = idx % 3, st = (g & 1) * 8 + idx / 3;
    const int rot = (g & 1) * 9;
    const unsigned myflags = SY_T + (unsigned)((st * 4 + ks) * 3 + dt) * 64u;
    if (wave >= 8) {
      auto issue_u = [&](int k0, int so) {
        const int kt = k0 + rot < 18 ? k0 + rot : k0 + rot - 18;
        if (k0 == 0 || k0 == 8) {
          const int kf = k0 == 0 ? rot : 9 - rot;
          if (wave == 8) {
            poll_flags<1>(ws, SY_U + (g & 1) * 2048 + (unsigned)((9 * ks + (kf >> 1) + (lane >> 1)) * 6 + 2 * dt + (lane & 1)) * 8u, lane < 10, tag);
            if (k0 == 0) { if (lane == 0) lds_arrive(smem + 163832); }
          } else if (k0 == 0) lds_wait(smem + 163832);
        }
#pragma unroll
        for (int m = 0; m < 6; ++m) {
          const int a = (wave - 8) * 6 + m;
          dma16<2>(rs_ws, ldsb + so + a * 1024, lane * 16, OFF_U + (unsigned)((dt * 6 + (a >> 2)) * 288 + (ks * 18 + kt) * 4 + (a & 3)) * 1024u);
        }
      };
      issue_u(0, 0); issue_u(1, CG2::STAGE);
      CG2::ring<6>(18, issue_u);
      BAR();
      if (wave == 8) poll_flags<1>(ws, myflags + (lane & 3) * 8, true, tag);
      BAR();
      return;
    }
    if (wave >= 4) {
      auto issue_w2 = [&](int k0, int so) {
        const int kt = k0 + rot < 18 ? k0 + rot : k0 + rot - 18;
        constexpr unsigned ld = S_ * 2;
        const unsigned vo = CG2::voff<TR128>(lane, ld);
#pragma unroll
        for (int m = 0; m < 4; ++m) {
          const int a = (wave - 4) * 4 + m;
          const unsigned sof = (unsigned)((ks * 18 + kt) * 64 + 4 * a) * ld + st * 256u;
          if (k0 >= NTF2) dma16<1>(rs_w2, ldsb + so + CG2::A_BYTES + a * 1024, vo, sof);
          else dma16<0>(rs_w2, ldsb + so + CG2::A_BYTES + a * 1024, vo, sof);
        }
      };
      issue_w2(0, 0); issue_w2(1, CG2::STAGE);
      CG2::ring<4>(18, issue_w2);
      BAR();
      BAR();
      return;
    }
    f32x16 acc[3][2] = {};
    int rslot = 0;
    CG2::consume<false>(smem, wave, lane, 18, rslot, acc);
    const int wa = wave >> 1, wb = wave & 1;
    const bool own0 = wb * 2 == ks, own1 = wb * 2 + 1 == ks;
#pragma unroll
    for (int j = 0; j < 2; ++j) {
      if (j ? own1 : own0) continue;
#pragma unroll
      for (int i = 0; i < 3; ++i) {
        const unsigned o = OFF_SLAB + (unsigned)((ks * 64 + st * 4 + wb * 2 + j) * 18 + dt * 6 + wa * 3 + i) * 2048u + lane * 16;
#pragma unroll
        for (int u = 0; u < 2; ++u) {
          u32x4 val;
#pragma unroll
          for (int e = 0; e < 4; ++e) val[e] = pack_bf16(acc[i][j][8 * u + 2 * e], acc[i][j][8 * u + 2 * e + 1]);
          store_dev(ws, o + u * 1024, val);
        }
      }
    }
    WAIT_VM(0);
    BAR();
    if (wave == 0 && lane < 4 && lane != ks) set_flag(ws, SY_T + (unsigned)((st * 4 + lane) * 3 + dt) * 64u + ks * 8, tag);
    BAR();
    if (!own0 && !own1) return;
    const int sf = st * 4 + ks, s = sf * 32 + (lane & 31);
    auto finish = [&](auto J) {
      constexpr int j = decltype(J)::value;
      u32x4 pv[3][2][4];
#pragma unroll
      for (int i = 0; i < 3; ++i)
#pragma unroll
        for (int u = 0; u < 2; ++u) {
          const unsigned o = (unsigned)(sf * 18 + dt * 6 + wa * 3 + i) * 2048u + u * 1024 + lane * 16;
#pragma unroll
          for (int q = 0; q < 3; ++q) pv[i][u][q] = load_dev(ws, OFF_SLAB + o + ((ks + 1 + q) & 3) * 2359296u);
          pv[i][u][3] = load_dev(ws, OFF_R + o);
        }
#pragma unroll
      for (int i = 0; i < 3; ++i)
#pragma unroll
        for (int u = 0; u < 2; ++u) {
          u32x4 o;
#pragma unroll
          for (int e = 0; e < 4; ++e)
            o[e] = pack_bf16(acc[i][j][8 * u + 2 * e] + (lo16(pv[i][u][0][e]) + lo16(pv[i][u][1][e])) + (lo16(pv[i][u][2][e]) + lo16(pv[i][u][3][e])),
                             acc[i][j][8 * u + 2 * e + 1] + (hi16(pv[i][u][0][e]) + hi16(pv[i][u][1][e])) + (hi16(pv[i][u][2][e]) + hi16(pv[i][u][3][e])));
          *(u32x4*)(out + (size_t)s * D_ + (dt * 6 + wa * 3 + i) * 32 + (lane >> 5) * 16 + u * 8) = o;
        }
    };
    if (own0) finish(IC<0>{}); else finish(IC<1>{});
  } else {
    const int me = (idx - 24) * 8 + g, sr = lane & 31;
    char* kt = smem + 37888; char* yb = kt + 17408;
    u32x4 xv[3];
#pragma unroll
    for (int it = 0; it < 3; ++it) xv[it] = *(const u32x4*)(x + (size_t)(me * 32 + sr) * D_ + ((it * 12 + wave) * 2 + (lane >> 5)) * 8);
    const float bias = lo16(b2[me * 32 + sr]);
    if (wave == 8) { poll_flags<1, 16>(ws, SY_Y + min(lane, 36) * 8, true, tag); }
    BAR();
#pragma unroll
    for (int it = 0; it < 3; ++it) dma16<2>(rs_ws, ldsb + (it * 12 + wave) * 1024, lane * 16, OFF_Y1 + me * 36864u + (it * 12 + wave) * 1024u);
    dma16<2>(rs_ws, ldsb + 37888 + wave * 1024, lane * 16, OFF_RT + wave * 1024u);
    if (wave < 4) dma16<2>(rs_ws, ldsb + 37888 + (12 + wave) * 1024, lane * 16, OFF_RT + (12 + wave) * 1024u);
    if (wave == 4) dma16<2>(rs_ws, ldsb + 37888 + 16384, (lane & 3) * 16, OFF_Y288 + me * 64u);
    const float fs = __uint_as_float((wmax_exp(ws, lane) + 1u) << 23) * (1.0f / 9.0f);
    WAIT_VM(0);
    BAR();
    for (int task = wave; task < 16; task += 12)
      irfft_wave(smem + (task >> 2) * 9216, kt + 16384 + (task >> 2) * 16, task & 3, lane, yb + task * 2 * FIN_OS, kt + lane * 16);
    WAIT_LGKM0();
    BAR();
#pragma unroll
    for (int it = 0; it < 3; ++it) {
      const int dp = (it * 12 + wave) * 2 + (lane >> 5);
      const char* yp = yb + sr * FIN_OS + dp * 32;
      const f32x4 y0 = *(const f32x4*)yp, y1 = *(const f32x4*)(yp + 16);
      u32x4 o;
#pragma unroll
      for (int e = 0; e < 4; ++e) {
        const float ya = e < 2 ? y0[2 * e] : y1[2 * e - 4], yc = e < 2 ? y0[2 * e + 1] : y1[2 * e - 3];
        o[e] = pack_bf16(lo16(xv[it][e]) + bias + ya * fs, hi16(xv[it][e]) + bias + yc * fs);
      }
      store_dev(ws, OFF_R + (unsigned)(me * 18 + (dp >> 2)) * 2048u + (dp & 1) * 1024 + (((dp >> 1) & 1) * 32 + sr) * 16, o);
    }
    WAIT_VM(0);
    BAR();
    if (wave == 0 && lane < 3) set_flag(ws, SY_T + (unsigned)(me * 3 + lane) * 64u + (me & 3) * 8, tag);
  }
}

extern "C" void kernel_launch(void* const* d_in, const int* in_sizes, int n_in, void* d_out, int out_size, void* d_ws, size_t ws_size,
                              hipStream_t stream) {
  (void)hipFuncSetAttribute((const void*)k_all, hipFuncAttributeMaxDynamicSharedMemorySize, 163840);
  k_all<<<256, 768, 163840, stream>>>((const u16*)d_in[0], (const u16*)d_in[1], (const u16*)d_in[2], (const char*)d_in[4], (const u16*)d_in[5],
                                      (const char*)d_in[6], (const u16*)d_in[7], (char*)d_ws, (u16*)d_out);
}
```

```cpp
#include <hip/hip_runtime.h>
#include <hip/hip_bf16.h>
#include <stdint.h>

#define DI __device__ __forceinline__
#define LAS __attribute__((address_space(3)))
typedef unsigned short u16;
typedef unsigned long long u64;
typedef __attribute__((ext_vector_type(2))) float f32x2;
typedef __attribute__((ext_vector_type(4))) float f32x4;
typedef __attribute__((ext_vector_type(16))) float f32x16;
typedef __attribute__((ext_vector_type(2))) _Float16 f16x2;
typedef __attribute__((ext_vector_type(8))) _Float16 f16x8;
typedef __attribute__((ext_vector_type(2))) __bf16 bf16x2;
typedef __attribute__((ext_vector_type(8))) __bf16 bf16x8;
typedef __attribute__((ext_vector_type(4))) short s16x4;
typedef __attribute__((ext_vector_type(2))) unsigned u32x2;
typedef __attribute__((ext_vector_type(4))) unsigned u32x4;
typedef __attribute__((address_space(1))) unsigned gu32;
typedef __attribute__((address_space(1))) u64 gu64;
#define RLX_AGENT __ATOMIC_RELAXED, __HIP_MEMORY_SCOPE_AGENT

constexpr int PF1_ROWS = (160 * 64) / 10;
constexpr int PF2T = 2;
constexpr int PF2C_ROWS = 488;
constexpr int NTF2 = 11;
constexpr unsigned CODE_WARM = 45056;
constexpr int POLL_SLEEP = 4;

constexpr int S_ = 2048, D_ = 576, F_ = 4608, NC_ = 289;

constexpr unsigned OFF_H    = 0;
constexpr unsigned OFF_WT   = OFF_H + 2359296;
constexpr unsigned OFF_TAB  = OFF_WT + 2367488;
constexpr unsigned OFF_RT   = OFF_TAB + 65536;
constexpr unsigned OFF_Y288 = OFF_RT + 16384;
constexpr unsigned OFF_WMAX = OFF_Y288 + 4096;
constexpr unsigned OFF_X1   = OFF_WMAX + 4096;
constexpr unsigned OFF_Y1   = OFF_X1 + 2359296;
constexpr unsigned OFF_U    = OFF_Y1 + 2359296;
constexpr unsigned OFF_SLAB = OFF_U + 5308416;
constexpr unsigned OFF_R    = OFF_SLAB + 9437184;
constexpr unsigned OFF_SYNC = OFF_R + 2359296;
constexpr unsigned SY_EPOCH = OFF_SYNC, SY_TMO = OFF_SYNC + 256, SY_A = OFF_SYNC + 2048, SY_Y = OFF_SYNC + 4096, SY_U = OFF_SYNC + 6144,
                   SY_H = SY_U + 2 * 2048, SY_T = SY_H + 8 * 2048;
constexpr unsigned WS_END   = SY_T + 192 * 64;
constexpr int TAB_E1 = 0, TAB_EO = 6144, TAB_EOI = 12288, TAB_TW1 = 18432, TAB_TW2 = 34816, TAB_BYTES = 51200;

DI unsigned pack_bf16(float a, float b) { f32x2 v = {a, b}; return __builtin_bit_cast(unsigned, __builtin_convertvector(v, bf16x2)); }
DI unsigned pack_f16(float a, float b) { f32x2 v = {a, b}; return __builtin_bit_cast(unsigned, __builtin_convertvector(v, f16x2)); }
DI float lo16(unsigned p) { return __uint_as_float(p << 16); }
DI float hi16(unsigned p) { return __uint_as_float(p & 0xffff0000u); }
DI float rbf(float a) { return lo16(pack_bf16(a, a)); }

template <int CTRL> DI float dpp_add(float v) {
  return v + __int_as_float(__builtin_amdgcn_update_dpp(0, __float_as_int(v), CTRL, 0xf, 0xf, true));
}
DI float sum32(float v) {
  v = dpp_add<0xB1>(v); v = dpp_add<0x4E>(v); v = dpp_add<0x141>(v); v = dpp_add<0x140>(v);
  auto r = __builtin_amdgcn_permlane16_swap(__float_as_uint(v), __float_as_uint(v), false, false);
  return __uint_as_float(r[0]) + __uint_as_float(r[1]);
}

#define WAIT_VM(n) asm volatile("s_waitcnt vmcnt(%0)" ::"n"(n) : "memory")
#define WAIT_LGKM0() asm volatile("s_waitcnt lgkmcnt(0)" ::: "memory")
#define BAR() do { asm volatile("" ::: "memory"); __builtin_amdgcn_s_barrier(); asm volatile("" ::: "memory"); } while (0)
#define SCHED0() __builtin_amdgcn_sched_barrier(0)

DI u32x4 mk_rsrc(const void* p, unsigned bytes) {
  const u64 a = (u64)p;
  return u32x4{(unsigned)a, (unsigned)(a >> 32) & 0xffffu, bytes, 0x00020000u};
}
template <int POL> DI void dma16(u32x4 rsrc, unsigned lds_off, unsigned voff, unsigned soff) {
  if constexpr (POL == 0) asm volatile("s_mov_b32 m0, %0\n\ts_nop 0\n\tbuffer_load_dwordx4 %1, %2, %3 offen lds" ::"s"(lds_off), "v"(voff), "s"(rsrc), "s"(soff) : "memory");
  if constexpr (POL == 1) asm volatile("s_mov_b32 m0, %0\n\ts_nop 0\n\tbuffer_load_dwordx4 %1, %2, %3 offen nt lds" ::"s"(lds_off), "v"(voff), "s"(rsrc), "s"(soff) : "memory");
  if constexpr (POL == 2) asm volatile("s_mov_b32 m0, %0\n\ts_nop 0\n\tbuffer_load_dwordx4 %1, %2, %3 offen sc1 lds" ::"s"(lds_off), "v"(voff), "s"(rsrc), "s"(soff) : "memory");
}
DI void dma4(u32x4 rsrc, unsigned lds_off, unsigned voff) {
  asm volatile("s_mov_b32 m0, %0\n\ts_nop 0\n\tbuffer_load_dword %1, %2, 0 offen lds" ::"s"(lds_off), "v"(voff), "s"(rsrc) : "memory");
}
DI void store_dev(char* ws, unsigned off, u32x4 v) {
  __builtin_amdgcn_raw_buffer_store_b128(v, __builtin_amdgcn_make_buffer_rsrc((void*)ws, 0, WS_END, 0x00020000), off, 0, 16);
}
DI u32x4 load_dev(const char* ws, unsigned off) {
  return __builtin_amdgcn_raw_buffer_load_b128(__builtin_amdgcn_make_buffer_rsrc((void*)ws, 0, WS_END, 0x00020000), off, 0, 16);
}
DI void set_flag(char* ws, unsigned off, u64 tag) { __hip_atomic_store((gu64*)(ws + off), tag, RLX_AGENT); }
template <int N, int SLP = POLL_SLEEP> DI void poll_flags(char* ws, unsigned off, bool active, u64 tag) {
  for (unsigned spins = 0;; ++spins) {
    bool ok = true;
#pragma unroll
    for (int i = 0; i < N; ++i) { const u64 v = __hip_atomic_load((gu64*)(ws + off + i * 512), RLX_AGENT); ok &= (v == tag) || !active; }
    if (__all(ok)) break;
    __builtin_amdgcn_s_sleep(SLP);
    if (spins > (1u << 17)) { set_flag(ws, SY_TMO, tag); break; }
    if ((spins & 63u) == 63u && __all(__hip_atomic_load((gu64*)(ws + SY_TMO), RLX_AGENT) == tag)) break;
  }
  __builtin_amdgcn_fence(__ATOMIC_ACQUIRE, "wavefront");
}
DI unsigned w1_line(int i, int total, int r0, int nl, unsigned c0) {
  const int rr = i / nl, l = i - rr * nl, row = r0 + rr;
  return i < total ? (unsigned)(64 * (row & 31) + (row >> 5)) * (F_ * 2) + c0 + (unsigned)l * 128u : 0xfffffff0u;
}

DI unsigned w2_line(int line, int g) {
  int row = (line >> 4) + (g & 1) * 576; if (row >= 1152) row -= 1152;
  return (unsigned)((g >> 1) * 1152 + row) * 4096u + (g & 1) * 2048u + (line & 15) * 128u;
}

template <bool F16> DI f32x16 mfma32(u32x4 a, u32x4 b, f32x16 c) {
  if constexpr (F16) return __builtin_amdgcn_mfma_f32_32x32x16_f16(__builtin_bit_cast(f16x8, a), __builtin_bit_cast(f16x8, b), c, 0, 0, 0);
  else return __builtin_amdgcn_mfma_f32_32x32x16_bf16(__builtin_bit_cast(bf16x8, a), __builtin_bit_cast(bf16x8, b), c, 0, 0, 0);
}
DI u32x4 pack8_f16(const f32x16& v, int u) {
  return u32x4{pack_f16(v[8 * u], v[8 * u + 1]), pack_f16(v[8 * u + 2], v[8 * u + 3]), pack_f16(v[8 * u + 4], v[8 * u + 5]), pack_f16(v[8 * u + 6], v[8 * u + 7])};
}
DI u32x4 tr_frag(const char* p0, const char* p1) {
  s16x4 lo = __builtin_amdgcn_ds_read_tr16_b64_v4i16((LAS s16x4*)p0);
  s16x4 hi = __builtin_amdgcn_ds_read_tr16_b64_v4i16((LAS s16x4*)p1);
  u32x2 a = __builtin_bit_cast(u32x2, lo), c = __builtin_bit_cast(u32x2, hi);
  return u32x4{a[0], a[1], c[0], c[1]};
}
DI void tw_piece(f32x16& re, f32x16& im, int p, const f32x4 w) {
  const f32x2 r = {re[2 * p], re[2 * p + 1]}, i = {im[2 * p], im[2 * p + 1]}, wr = {w[0], w[1]}, wi = {w[2], w[3]};
  const f32x2 a = __builtin_elementwise_fma(r, wr, -(i * wi)), b = __builtin_elementwise_fma(r, wi, i * wr);
  re[2 * p] = a[0]; re[2 * p + 1] = a[1]; im[2 * p] = b[0]; im[2 * p + 1] = b[1];
}
DI void tw_load(f32x4 (&t)[8], const char* tw) {
#pragma unroll
  for (int p = 0; p < 8; ++p) t[p] = *(const f32x4*)(tw + p * 1024);
}
DI void tw_apply(f32x16& re, f32x16& im, const f32x4 (&t)[8]) {
#pragma unroll
  for (int p = 0; p < 8; ++p) tw_piece(re, im, p, t[p]);
}
DI float cs(int which, float rev) {
  return which == 0 ? __builtin_amdgcn_cosf(rev) : (which == 1 ? -__builtin_amdgcn_sinf(rev) : __builtin_amdgcn_sinf(rev));
}
DI u32x4 e32_frag(int frag, int lane) {
  const int t = frag / 3, which = frag - 3 * t, r = lane & 31, h = lane >> 5;
  float o[8];
#pragma unroll
  for (int j = 0; j < 8; ++j) o[j] = cs(which, (float)(((16 * t + 8 * (j >> 2) + 4 * h + (j & 3)) * r) & 31) * (1.0f / 32.0f));
  return u32x4{pack_f16(o[0], o[1]), pack_f16(o[2], o[3]), pack_f16(o[4], o[5]), pack_f16(o[6], o[7])};
}

enum { LIN = 0, TR128 = 1, TR64 = 2 };

template <int V> struct IC { static constexpr int value = V; };
template <int NA_W, int NB_W, int WA, int WB, int AK, int BK, int NS, int DPM, int DEPTH>
struct GC {
  static constexpr int NC = WA * WB, NFA = NA_W * WA, NFB = NB_W * WB;
  static constexpr int A_BYTES = NFA * 4096, B_BYTES = NFB * 4096, STAGE = A_BYTES + B_BYTES;
  static constexpr int LDS_BYTES = NS * STAGE;
  static_assert(AK == LIN || NFA == (AK == TR128 ? 4 : 2), "");
  static_assert(BK == LIN || NFB == (BK == TR128 ? 4 : 2), "");

  template <int K> static DI unsigned voff(int lane, unsigned rstride) {
    if constexpr (K == LIN) return lane * 16;
    else if constexpr (K == TR128) return (unsigned)(lane >> 4) * rstride + (unsigned)(((lane & 15) ^ ((lane >> 4) << 2)) * 16);
    else return (unsigned)(lane >> 3) * rstride + (unsigned)(((lane & 7) ^ (((lane >> 4) & 1) << 2)) * 16);
  }
  template <int K, int NF> static DI void frag_addr(unsigned (&addr)[K == LIN ? 1 : NF], int opoff, int wpos, int lane) {
    if constexpr (K == LIN) addr[0] = opoff + wpos * NF * 4096 + lane * 16;
    else {
      const int h = lane >> 5, blk = (lane >> 4) & 1, q = (lane & 15) >> 2, p = lane & 3;
#pragma unroll
      for (int i = 0; i < NF; ++i) {
        const int ch = 4 * (wpos * NF + i) + 2 * blk + (p >> 1);
        if constexpr (K == TR128) addr[i] = opoff + (4 * h + q) * 256 + ((ch ^ (q << 2)) * 16) + 8 * (p & 1);
        else addr[i] = opoff + (4 * h + q) * 128 + ((ch ^ (((q >> 1) & 1) << 2)) * 16) + 8 * (p & 1);
      }
    }
  }
  template <int K, int NF> static DI void rd(u32x4 (&f)[NF], const char* smem, const unsigned (&addr)[K == LIN ? 1 : NF], int sb, int hh) {
#pragma unroll
    for (int i = 0; i < NF; ++i) {
      if constexpr (K == LIN) f[i] = *(const u32x4*)(smem + addr[0] + sb + (i * 4 + hh) * 1024);
      else {
        constexpr int RB = K == TR128 ? 256 : 128;
        const char* p = smem + addr[i] + sb + hh * 16 * RB;
        s16x4 lo = __builtin_amdgcn_ds_read_tr16_b64_v4i16((LAS s16x4*)p);
        s16x4 hi = __builtin_amdgcn_ds_read_tr16_b64_v4i16((LAS s16x4*)(p + 8 * RB));
        u32x2 a = __builtin_bit_cast(u32x2, lo), c = __builtin_bit_cast(u32x2, hi);
        f[i] = u32x4{a[0], a[1], c[0], c[1]};
      }
    }
  }
  static DI void ilv() {
    constexpr int NR = NA_W * (AK == LIN ? 1 : 2) + NB_W * (BK == LIN ? 1 : 2), NM = NA_W * NB_W;
    constexpr int NG = (NR + DPM - 1) / DPM < NM ? (NR + DPM - 1) / DPM : NM;
#pragma unroll
    for (int i = 0; i < NG; ++i) {
      __builtin_amdgcn_sched_group_barrier(0x8, 1, 0);
      if (i + 1 < NG) __builtin_amdgcn_sched_group_barrier(0x100, DPM, 0);
      else __builtin_amdgcn_sched_group_barrier(0x100, NR - DPM * (NG - 1), 0);
    }
    if constexpr (NM > NG) __builtin_amdgcn_sched_group_barrier(0x8, NM - NG, 0);
    SCHED0();
  }
  template <int G, class ISSUE> static DI void ring(int nt, ISSUE issue) {
    int wslot = NS - 2;
    for (int tau = 0; tau < nt - (NS - 2); ++tau) {
      WAIT_VM((NS - 3) * G);
      BAR();
      issue(tau + NS - 2, wslot * STAGE);
      wslot = (wslot + 1 == NS) ? 0 : wslot + 1;
    }
    if constexpr (NS >= 5) { WAIT_VM(2 * G); BAR(); }
    if constexpr (NS >= 4) { WAIT_VM(G); BAR(); }
    WAIT_VM(0); BAR();
    static_assert(NS >= 3 && NS <= 5, "");
  }
  template <bool F16>
  static DI void consume(const char* smem, int wave, int lane, int n, int& rslot, f32x16 (&acc)[NA_W][NB_W]) {
    const int wa = wave / WB, wb = wave - wa * WB;
    unsigned addrA[AK == LIN ? 1 : NA_W], addrB[BK == LIN ? 1 : NB_W];
    frag_addr<AK, NA_W>(addrA, 0, wa, lane);
    frag_addr<BK, NB_W>(addrB, A_BYTES, wb, lane);
    u32x4 fa[DEPTH][NA_W], fb[DEPTH][NB_W];
#pragma unroll
    for (int d = 1; d < DEPTH; ++d) {
#pragma unroll
      for (int i = 0; i < NA_W; ++i) fa[d][i] = u32x4{0, 0, 0, 0};
#pragma unroll
      for (int i = 0; i < NB_W; ++i) fb[d][i] = u32x4{0, 0, 0, 0};
    }
    auto mm = [&](const u32x4 (&a)[NA_W], const u32x4 (&b)[NB_W]) {
#pragma unroll
      for (int i = 0; i < NA_W; ++i)
#pragma unroll
        for (int j = 0; j < NB_W; ++j) acc[i][j] = mfma32<F16>(a[i], b[j], acc[i][j]);
    };
    auto step = [&](auto Z0) {
      constexpr int z0 = decltype(Z0)::value;
      BAR();
      SCHED0();
      const int sb = rslot * STAGE;
#pragma unroll
      for (int hh = 0; hh < 4; ++hh) {
        rd<AK, NA_W>(fa[(z0 + hh) % DEPTH], smem, addrA, sb, hh);
        rd<BK, NB_W>(fb[(z0 + hh) % DEPTH], smem, addrB, sb, hh);
        mm(fa[(z0 + hh + 1) % DEPTH], fb[(z0 + hh + 1) % DEPTH]);
        ilv();
      }
      rslot = (rslot + 1 == NS) ? 0 : rslot + 1;
    };
    if constexpr (DEPTH == 2) {
      for (int tau = 0; tau < n; ++tau) step(IC<0>{});
      mm(fa[1], fb[1]);
    } else {
      int tau = 0;
      for (; tau + 3 <= n; tau += 3) { step(IC<0>{}); step(IC<1>{}); step(IC<2>{}); }
      if (n - tau == 2) { step(IC<0>{}); step(IC<1>{}); mm(fa[0], fb[0]); mm(fa[1], fb[1]); }
      else if (n - tau == 1) { step(IC<0>{}); mm(fa[2], fb[2]); mm(fa[0], fb[0]); }
      else { mm(fa[1], fb[1]); mm(fa[2], fb[2]); }
    }
  }
};

DI float gelu_tanh(float u) {
  const float z = u * (1.0f + 0.044715f * u * u) * (-2.0f * 0.7978845608028654f * 1.4426950408889634f);
  return u * __builtin_amdgcn_rcpf(1.0f + __builtin_amdgcn_exp2f(z));
}

template <int NV> DI void mv_ilv() {
#pragma unroll
  for (int i = 0; i < 8; ++i) { __builtin_amdgcn_sched_group_barrier(0x8, 1, 0); __builtin_amdgcn_sched_group_barrier(0x2, NV, 0); }
  SCHED0();
}
struct EF { u32x4 r, i, n; };
DI EF ef_load(const char* p) { EF e; e.r = *(const u32x4*)p; e.i = *(const u32x4*)(p + 1024); e.n = *(const u32x4*)(p + 2048); return e; }
#define MF(a, b, c) mfma32<true>(a, b, c)
DI void cm_dA(f32x16& re, f32x16& im, u32x4 dr, u32x4 di, const EF& e) { re = MF(dr, e.r, re); im = MF(dr, e.i, im); re = MF(di, e.n, re); im = MF(di, e.r, im); }
DI void cm_dB(f32x16& re, f32x16& im, u32x4 dr, u32x4 di, const EF& e) { re = MF(e.r, dr, re); im = MF(e.i, dr, im); re = MF(e.n, di, re); im = MF(e.r, di, im); }
DI void cm_dAc(f32x16& re, f32x16& im, u32x4 dr, u32x4 di, const EF& e) { re = MF(dr, e.r, re); im = MF(dr, e.n, im); re = MF(di, e.i, re); im = MF(di, e.r, im); }
DI void wmul(f32x16& re, f32x16& im, const u32x4 (&wv)[4], float wsc) {
#pragma unroll
  for (int p = 0; p < 8; ++p) {
    const unsigned w0 = wv[p >> 1][(2 * p) & 3], w1 = wv[p >> 1][(2 * p + 1) & 3];
    tw_piece(re, im, p, f32x4{lo16(w0), lo16(w1), hi16(w0), hi16(w1)} * wsc);
  }
}
DI void bfly(f32x16& a, f32x16& b) {
  const f32x16 x = a, y = b; a = x + y; b = x - y;
}
DI void colfft_wave(const char* tab  , const char* ws, int c, int lane, float wsc, const u32x4 (&zr)[2][2], const u32x4 (&zi)[2][2],
                    char* img, int slot_re, int slot_im) {
  f32x4 tw[8];
  f32x16 r0 = {}, i0 = {}, r1 = {}, i1 = {};
  u32x4 ar[2], ai[2];
  EF ea = ef_load(tab + TAB_E1), eb = ef_load(tab + TAB_E1 + 3072);
  tw_load(tw, tab + TAB_TW1);
  SCHED0();
  cm_dA(r0, i0, zr[0][0], zi[0][0], ea); cm_dA(r0, i0, zr[0][1], zi[0][1], eb);
  SCHED0();
  cm_dA(r1, i1, zr[1][0], zi[1][0], ea); cm_dA(r1, i1, zr[1][1], zi[1][1], eb);
#pragma unroll
  for (int p = 0; p < 8; ++p) { tw_piece(r0, i0, p, tw[p]); tw[p] = *(const f32x4*)(tab + TAB_TW1 + 8192 + p * 1024); }
  ar[0] = pack8_f16(r0, 0); ar[1] = pack8_f16(r0, 1); ai[0] = pack8_f16(i0, 0); ai[1] = pack8_f16(i0, 1);
  mv_ilv<6>();
  u32x4 wv0[4], wv1[4];
#pragma unroll
  for (int i = 0; i < 4; ++i) wv0[i] = load_dev(ws, OFF_WT + c * 8192u + i * 1024 + lane * 16);
  r0 = f32x16{}; i0 = f32x16{};
  cm_dB(r0, i0, ar[0], ai[0], ea); ea = ef_load(tab + TAB_EO);
  cm_dB(r0, i0, ar[1], ai[1], eb); eb = ef_load(tab + TAB_EO + 3072);
  tw_apply(r1, i1, tw);
  ar[0] = pack8_f16(r1, 0); ar[1] = pack8_f16(r1, 1); ai[0] = pack8_f16(i1, 0); ai[1] = pack8_f16(i1, 1);
  mv_ilv<6>();
#pragma unroll
  for (int i = 0; i < 4; ++i) wv1[i] = load_dev(ws, OFF_WT + c * 8192u + (4 + i) * 1024 + lane * 16);
  r1 = f32x16{}; i1 = f32x16{};
  cm_dB(r1, i1, ar[0], ai[0], ea); ea = ef_load(tab + TAB_E1);
  cm_dB(r1, i1, ar[1], ai[1], eb); eb = ef_load(tab + TAB_E1 + 3072);
  SCHED0();
  bfly(r0, r1); bfly(i0, i1);
  wmul(r0, i0, wv0, wsc);
  wmul(r1, i1, wv1, wsc);
  bfly(r0, r1); bfly(i0, i1);
  u32x4 dr[2], di[2];
  ar[0] = pack8_f16(r0, 0); ar[1] = pack8_f16(r0, 1); ai[0] = pack8_f16(i0, 0); ai[1] = pack8_f16(i0, 1);
  SCHED0();
  tw_load(tw, tab + TAB_TW2);
  r0 = f32x16{}; i0 = f32x16{};
  cm_dAc(r0, i0, ar[0], ai[0], ea); ea = ef_load(tab + TAB_EOI);
  cm_dAc(r0, i0, ar[1], ai[1], eb); eb = ef_load(tab + TAB_EOI + 3072);
  dr[0] = pack8_f16(r1, 0); dr[1] = pack8_f16(r1, 1); di[0] = pack8_f16(i1, 0); di[1] = pack8_f16(i1, 1);
  mv_ilv<2>();
  r1 = f32x16{}; i1 = f32x16{};
  cm_dAc(r1, i1, dr[0], di[0], ea); ea = ef_load(tab + TAB_E1);
  cm_dAc(r1, i1, dr[1], di[1], eb); eb = ef_load(tab + TAB_E1 + 3072);
#pragma unroll
  for (int p = 0; p < 8; ++p) { tw_piece(r0, i0, p, tw[p]); tw[p] = *(const f32x4*)(tab + TAB_TW2 + 8192 + p * 1024); }
  ar[0] = pack8_f16(r0, 0); ar[1] = pack8_f16(r0, 1); ai[0] = pack8_f16(i0, 0); ai[1] = pack8_f16(i0, 1);
  mv_ilv<6>();
  r0 = f32x16{}; i0 = f32x16{};
  cm_dAc(r0, i0, ar[0], ai[0], ea); cm_dAc(r0, i0, ar[1], ai[1], eb);
  tw_apply(r1, i1, tw);
  dr[0] = pack8_f16(r1, 0); dr[1] = pack8_f16(r1, 1); di[0] = pack8_f16(i1, 0); di[1] = pack8_f16(i1, 1);
  mv_ilv<6>();
  r1 = f32x16{}; i1 = f32x16{};
  cm_dAc(r1, i1, dr[0], di[0], ea); cm_dAc(r1, i1, dr[1], di[1], eb);
  SCHED0();
#pragma unroll
  for (int g = 0; g < 4; ++g) {
    const int Q = (lane & 31) * 8 + 2 * g + (lane >> 5), sw = lane & 15, r = 4 * g;
    if (slot_im == -2) {
      *(u32x4*)(img + Q * 16) = u32x4{pack_f16(r0[r], r1[r]), pack_f16(r0[r + 1], r1[r + 1]), pack_f16(r0[r + 2], r1[r + 2]), pack_f16(r0[r + 3], r1[r + 3])};
      continue;
    }
#pragma unroll
    for (int z = 0; z < 2; ++z) {
      const int q = r + 2 * z;
      *(u32x4*)(img + Q * 256 + (((slot_re + z) ^ sw) * 16)) =
          u32x4{pack_f16(r0[q], i0[q]), pack_f16(r1[q], i1[q]), pack_f16(r0[q + 1], i0[q + 1]), pack_f16(r1[q + 1], i1[q + 1])};
    }
  }
}
#undef MF
constexpr int FIN_OS = 2336;
DI void irfft_wave(const char* rec, const char* y288, int w, int lane, char* orow, const char* kt) {
  const int row = lane & 31, hh = lane >> 5, k1 = row & 15, rr = 2 * w + (row >> 4);
  const bool ok = k1 < 10;
  const int k1c = ok ? k1 : 0;
  const char* pd = rec + rr * 4 + 32 * (k1c + 72 * hh);
  const char* pm = rec + rr * 4 + 32 * (576 - k1c - 72 * hh - 18 * 27);
  unsigned v[2][8];
#pragma unroll
  for (int j = 0; j < 8; ++j) {
    v[0][j] = *(const unsigned*)(pd + 576 * (8 * (j >> 2) + (j & 3)));
    v[1][j] = *(const unsigned*)(pm + 576 * (27 - (16 + 8 * (j >> 2) + (j & 3))));
  }
  const unsigned v288 = *(const u16*)(y288 + rr * 2);
  f32x4 tw[8];
  tw_load(tw, kt + 8192);
  if (k1 == 0 && hh == 0) { v[0][0] &= 0xffffu; v[1][0] = v288; }
  f32x16 re = {}, im = {};
#pragma unroll
  for (int u = 0; u < 2; ++u) {
    u32x4 a_r, a_i;
#pragma unroll
    for (int e = 0; e < 4; ++e) {
      a_r[e] = ok ? __builtin_amdgcn_perm(v[u][2 * e + 1], v[u][2 * e], 0x05040100u) : 0u;
      a_i[e] = ok ? (__builtin_amdgcn_perm(v[u][2 * e + 1], v[u][2 * e], 0x07060302u) ^ (u ? 0x80008000u : 0u)) : 0u;
    }
    cm_dAc(re, im, a_r, a_i, ef_load(kt + 2048 + u * 3072));
  }
  const u32x4 cr = *(const u32x4*)kt, ci = *(const u32x4*)(kt + 1024);
  tw_apply(re, im, tw);
#pragma unroll
  for (int u = 0; u < 2; ++u) {
    f32x16 y = {};
    y = mfma32<true>(cr, pack8_f16(re, u), y);
    y = mfma32<true>(ci, pack8_f16(im, u), y);
    float* o = (float*)(orow + u * FIN_OS) + (lane & 31) + 128 * hh;
#pragma unroll
    for (int rho = 0; rho < 8; ++rho) o[32 * ((rho & 3) + 8 * (rho >> 2))] = y[rho];
    if (hh == 0) { o[32 * 16] = y[8]; o[32 * 17] = y[9]; }
  }
}

DI unsigned wmax_exp(const char* ws, int lane) {
  const u32x4 mv = load_dev(ws, OFF_WMAX + lane * 16);
  unsigned mx = max(max(mv[0], mv[1]), max(mv[2], mv[3]));
#pragma unroll
  for (int o = 32; o; o >>= 1) mx = max(mx, (unsigned)__shfl_xor((int)mx, o));
  return min(mx >> 7, 252u);
}

DI unsigned lds_arrive(char* p) { return __hip_atomic_fetch_add((LAS unsigned*)p, 1u, __ATOMIC_RELAXED, __HIP_MEMORY_SCOPE_WORKGROUP); }
DI void lds_arrive_wait(char* p, unsigned n, int lane) {
  if (lane == 0) lds_arrive(p);
  while (__hip_atomic_load((LAS unsigned*)p, __ATOMIC_RELAXED, __HIP_MEMORY_SCOPE_WORKGROUP) < n) __builtin_amdgcn_s_sleep(1);
  asm volatile("" ::: "memory");
}
DI void lds_wait(const char* p) {
  while (__hip_atomic_load((const LAS unsigned*)p, __ATOMIC_RELAXED, __HIP_MEMORY_SCOPE_WORKGROUP) == 0u) __builtin_amdgcn_s_sleep(1);
  asm volatile("" ::: "memory");
}
using CG1 = GC<1, 3, 4, 1, TR128, LIN, 5, 2, 3>;
using CG2 = GC<3, 2, 2, 2, LIN, TR128, 4, 2, 2>;
constexpr int P1_RS = 1216, P1_XS = 3 * CG1::STAGE, P1_HS = P1_XS + 8 * P1_RS, P1_C1 = P1_XS + 16 * P1_RS, P1_E = P1_C1 + 6144,
              P1_TW = P1_E + 6144, P1_O = P1_TW + 8192, P1_WT = P1_O + 9216, P1_RED = P1_WT + 10512, P1_DUMMY = 147456;
static_assert(P1_RED + 48 <= P1_DUMMY && P1_DUMMY >= CG1::LDS_BYTES, "");

__global__ __launch_bounds__(768, 3) void k_all(const u16* __restrict__ x, const u16* __restrict__ wr, const u16* __restrict__ wi,
                                                const char* __restrict__ w1, const u16* __restrict__ b1, const char* __restrict__ w2,
                                                const u16* __restrict__ b2, char* __restrict__ ws, u16* __restrict__ out) {
  extern __shared__ __attribute__((aligned(16))) char smem[];
  const int lane = threadIdx.x & 63, wave = __builtin_amdgcn_readfirstlane(threadIdx.x >> 6);
  const int b = blockIdx.x, g = b & 7, idx = b >> 3;
  const unsigned ldsb = (unsigned)(size_t)(LAS char*)smem;
  const u32x4 rs_ws = mk_rsrc(ws, WS_END), rs_w1 = mk_rsrc(w1, 18874368u), rs_w2 = mk_rsrc(w2, 18874368u);
  if (wave == 11 && idx < 6) {
    const u64 pc = __builtin_amdgcn_s_getpc() & ~(u64)127;
    dma4(mk_rsrc((const void*)pc, CODE_WARM), ldsb + P1_DUMMY + wave * 256, (unsigned)(idx * 64 + lane) * 128u);
  }
  const int tile = g * 27 + idx, ft = tile / 6, dt1 = tile - ft * 6;
  const int ft0 = (g * 27) / 6, nl1 = ((g * 27 + 26) / 6 - ft0 + 1) * 2;
  auto issue_w1x = [&](int lw, int kt, int so) {
    constexpr unsigned ld = F_ * 2;
    const unsigned vo = CG1::voff<TR128>(lane, 64 * ld);
#pragma unroll
    for (int m = 0; m < 4; ++m) {
      const int a = lw * 4 + m;
      dma16<0>(rs_w1, ldsb + so + a * 1024, vo, (unsigned)(((4 * a) & 31) * 64 + 2 * kt + (a >> 3)) * ld + ft * 256u);
    }
  };
  const int tb = (g >> 2) * 128 + idx * 4 + (g & 3);
  auto gate = [&] {
    while (__hip_atomic_load((const LAS unsigned*)(smem + P1_RED + 16), __ATOMIC_RELAXED, __HIP_MEMORY_SCOPE_WORKGROUP) < 3u) __builtin_amdgcn_s_sleep(1);
  };
  u64 tag;
  if (wave < 4) {
    const int tid = threadIdx.x, s2 = tb >> 2, t = (tb >> 1) & 1, h = tb & 1;
    const int rr = tid >> 5, l32 = tid & 31;
    const int s1 = 16 * t + 8 * (rr >> 2) + 4 * h + (rr & 3);
    const unsigned* xr = (const unsigned*)(x + (size_t)(64 * s1 + s2) * D_);
    unsigned raw[9];
#pragma unroll
    for (int i = 0; i < 9; ++i) raw[i] = xr[32 * i + l32];
    float v[18];
#pragma unroll
    for (int i = 0; i < 9; ++i) { v[2 * i] = lo16(raw[i]); v[2 * i + 1] = hi16(raw[i]); }
#pragma unroll
    for (int pass = 0; pass < 2; ++pass) {
      float sm = 0.f;
#pragma unroll
      for (int i = 0; i < 18; ++i) sm += v[i];
      const float mean = sum32(sm) * (1.0f / 576.0f);
      float sq = 0.f;
#pragma unroll
      for (int i = 0; i < 18; ++i) { float dlt = v[i] - mean; sq += dlt * dlt; }
      const float var = sum32(sq) * (1.0f / 576.0f);
      const float mb = rbf(mean);
      const float sd = rbf(__builtin_sqrtf(rbf(rbf(var) + 1e-5f)));
      const float rs = 1.0f / sd;
      char* dst = smem + P1_XS + pass * (8 * P1_RS) + rr * P1_RS + l32 * 4;
#pragma unroll
      for (int i = 0; i < 9; ++i) {
        unsigned p = pack_bf16(v[2 * i] - mb, v[2 * i + 1] - mb);
        p = pack_bf16(lo16(p) * rs, hi16(p) * rs);
        v[2 * i] = lo16(p); v[2 * i + 1] = hi16(p);
        *(unsigned*)(dst + i * 128) = pass == 0 ? pack_f16(v[2 * i], v[2 * i + 1]) : p;
      }
    }
    WAIT_LGKM0();
    BAR();
    { const u64 tv = *(const u64*)(smem + P1_RED + 24);
      tag = (u64)__builtin_amdgcn_readfirstlane((unsigned)(tv >> 32)) << 32 | (unsigned)__builtin_amdgcn_readfirstlane((unsigned)tv); }
    {
      const int hh = lane >> 5, blk = (lane >> 4) & 1, q = (lane & 15) >> 2, p = lane & 3;
      const char* xa = smem + P1_XS + (2 * wave) * P1_RS + blk * 32 + p * 8;
      const char* cf = smem + P1_C1 + lane * 16;
      f32x16 re = {}, im = {};
      f32x4 tw[8];
      tw_load(tw, smem + P1_TW + lane * 16);
#pragma unroll
      for (int ks = 0; ks < 3; ++ks) {
        const char* a0 = ks < 2 ? xa + ks * P1_RS + (4 * hh + q) * 64 : xa + (q >> 1) * P1_RS + (16 + (q & 1)) * 64;
        const u32x4 a = tr_frag(a0, ks < 2 ? a0 + 512 : a0);
        re = mfma32<true>(a, *(const u32x4*)(cf + (2 * ks) * 1024), re);
        im = mfma32<true>(a, *(const u32x4*)(cf + (2 * ks + 1) * 1024), im);
      }
      tw_apply(re, im, tw);
      f32x16 xr_ = {}, xi_ = {};
#pragma unroll
      for (int u = 0; u < 2; ++u) cm_dB(xr_, xi_, pack8_f16(re, u), pack8_f16(im, u), ef_load(smem + P1_E + u * 3072 + lane * 16));
      const int col = lane & 31, k1 = col & 15, row2 = 2 * (2 * wave + (col >> 4));
      if (k1 < 10) {
        char* o1 = smem + P1_O + 32 * k1 + 576 * 4 * hh + row2;
        char* o2 = smem + P1_O + 32 * (576 - k1) - 576 * (4 * hh + 27) + row2;
#pragma unroll
        for (int rho = 0; rho < 8; ++rho) {
          const unsigned pk = pack_f16(xr_[rho] * (1.0f / 32.0f), xi_[rho] * (1.0f / 32.0f));
          char* d = o1 + 576 * ((rho & 3) + 8 * (rho >> 2));
          *(u16*)d = (u16)pk;
          if (rho != 0 || k1 != 0 || hh != 0) *(u16*)(d + 16) = (u16)(pk >> 16);
        }
        if (k1 >= 1 && k1 <= 8) {
#pragma unroll
          for (int rho = 8; rho < 16; ++rho) {
            const unsigned pk = pack_f16(xr_[rho] * (1.0f / 32.0f), xi_[rho] * (-1.0f / 32.0f));
            char* d = o2 + 576 * (27 - ((rho & 3) + 8 * (rho >> 2)));
            *(u16*)d = (u16)pk;
            *(u16*)(d + 16) = (u16)(pk >> 16);
          }
        } else if (k1 == 0 && hh == 0) {
          *(u16*)(smem + P1_O + 16 + row2) = (u16)pack_f16(xr_[8] * (1.0f / 32.0f), 0.f);
        }
      }
    }
    WAIT_LGKM0();
    lds_arrive_wait(smem + P1_RED + 32, 4u, lane);
    gate();
    const unsigned P = ((s2 & 1) * 2 + t) * 64 + h * 32 + (s2 >> 1);
#pragma unroll
    for (int it = 0; it < 3; ++it) {
      const int p = tid + it * 256;
      if (p < 576) store_dev(ws, OFF_X1 + P * 9216u + p * 16, *(const u32x4*)(smem + P1_O + p * 16));
    }
    WAIT_VM(0);
    if (lane == 0 && lds_arrive(smem + P1_RED + 36) == 8u) set_flag(ws, SY_A + b * 8, tag);
  } else if (wave < 8) {
    const int tid = threadIdx.x - 256;
    const int rg = b >> 2, cg = b & 3, c0 = cg * 72 + (cg > 0), nc = cg == 0 ? 73 : 72, kq = rg >> 2, ko = rg & 3;
    unsigned rv[10], iv[10];
#pragma unroll
    for (int it = 0; it < 10; ++it) {
      const int i = tid + it * 256, row = i / 73, col = i - row * 73;
      const bool ok = i < 32 * 73 && col < nc;
      const int off = ok ? (8 * ko + (row & 7) + 32 * (4 * kq + (row >> 3))) * NC_ + c0 + col : 0;
      rv[it] = wr[off]; iv[it] = wi[off];
    }
    BAR();
    { const u64 tv = *(const u64*)(smem + P1_RED + 24);
      tag = (u64)__builtin_amdgcn_readfirstlane((unsigned)(tv >> 32)) << 32 | (unsigned)__builtin_amdgcn_readfirstlane((unsigned)tv); }
    unsigned mx = 0;
#pragma unroll
    for (int it = 0; it < 10; ++it) {
      const int i = tid + it * 256, row = i / 73, col = i - row * 73;
      if (i < 32 * 73 && col < nc) {
        mx = max(mx, max(rv[it] & 0x7fffu, iv[it] & 0x7fffu));
        *(unsigned*)(smem + P1_WT + col * 144 + (row & 7) * 16 + (row >> 3) * 4) = rv[it] | (iv[it] << 16);
      }
    }
#pragma unroll
    for (int o = 32; o; o >>= 1) mx = max(mx, (unsigned)__shfl_xor((int)mx, o));
    unsigned* red = (unsigned*)(smem + P1_RED);
    if (lane == 0) red[wave - 4] = mx;
    WAIT_LGKM0();
    lds_arrive_wait(smem + P1_RED + 44, 4u, lane);
    if (tid == 0) __hip_atomic_store((gu32*)(ws + OFF_WMAX + b * 4), max(max(red[0], red[1]), max(red[2], red[3])), RLX_AGENT);
    const int U = kq >> 3, h = kq & 1, rq = (kq >> 1) & 3;
    gate();
#pragma unroll
    for (int it = 0; it < 3; ++it) {
      const int p = tid + it * 256;
      if (p < nc * 8) {
        const int col = p >> 3, a = p & 7;
        store_dev(ws, OFF_WT + (unsigned)(c0 + col) * 8192u + ((U * 4 + rq) * 64 + h * 32 + 8 * ko + a) * 16,
                  *(const u32x4*)(smem + P1_WT + col * 144 + a * 16));
      }
    }
    WAIT_VM(0);
    if (lane == 0 && lds_arrive(smem + P1_RED + 36) == 8u) set_flag(ws, SY_A + b * 8, tag);
    if (idx < 27) { issue_w1x(wave - 4, 0, 0); issue_w1x(wave - 4, 1, CG1::STAGE); issue_w1x(wave - 4, 2, 2 * CG1::STAGE); }
  } else {
    u64 ep = 0;
    if (threadIdx.x == 512) ep = __hip_atomic_load((gu64*)(ws + SY_EPOCH), RLX_AGENT);
#pragma unroll
    for (int z = 0; z < 5; ++z) {
      const int fr = (wave - 8) + 4 * z;
      u32x4 val;
      const int col = lane & 31, hh = lane >> 5, r = col >> 4, k1 = col & 15;
      if (fr < 6) {
        const int ks = fr >> 1, which = fr & 1;
        float o[8];
#pragma unroll
        for (int j = 0; j < 8; ++j) {
          int row, d1;
          if (ks < 2) { row = ks; d1 = 8 * (j >> 2) + 4 * hh + (j & 3); }
          else { row = (hh == 0 && j < 4) ? (j >> 1) : 2; d1 = 16 + (j & 1); }
          const float v = cs(which, (float)((d1 * k1) % 18) * (1.0f / 18.0f));
          o[j] = (row == r && k1 < 10) ? v : 0.f;
        }
        val = u32x4{pack_f16(o[0], o[1]), pack_f16(o[2], o[3]), pack_f16(o[4], o[5]), pack_f16(o[6], o[7])};
      } else if (fr < 12) {
        val = e32_frag(fr - 6, lane);
      } else {
#pragma unroll
        for (int y = 0; y < 2; ++y) {
          const int rho = 2 * (fr - 12) + y, d2 = (rho & 3) + 8 * (rho >> 2) + 4 * hh;
          const float a = (float)(d2 * k1) * (1.0f / 576.0f);
          val[y] = __float_as_uint(__builtin_amdgcn_cosf(a)); val[2 + y] = __float_as_uint(-__builtin_amdgcn_sinf(a));
        }
      }
      *(u32x4*)(smem + P1_C1 + fr * 1024 + lane * 16) = val;
      if (z == 2) {
        if (threadIdx.x == 512) *(u64*)(smem + P1_RED + 24) = ep * 6364136223846793005ull + 1442695040888963407ull;
        if (wave == 10) {
#pragma unroll
          for (int i = 0; i < 5; ++i)
            dma4(rs_w1, ldsb + P1_DUMMY + wave * 256, w1_line((i * 32 + idx) * 64 + lane, PF1_ROWS * nl1, 0, nl1, ft0 * 256u));
        }
      }
    }
    if (threadIdx.x == 512) { *(unsigned*)(smem + P1_RED + 16) = 0u; *(u64*)(smem + P1_RED + 32) = 0ull; *(u64*)(smem + P1_RED + 40) = 0ull; }
    if (wave == 8 && b < 66) {
      const int r = lane & 31, h = lane >> 5;
      u32x4 val;
      if (b < 6) val = e32_frag(b, lane);
      else if (b < 18) {
        const bool inv = b >= 12;
        const int f2 = inv ? b - 12 : b - 6, u = f2 / 3, which = f2 - 3 * u;
        float o[8];
#pragma unroll
        for (int j = 0; j < 8; ++j) {
          const int kap = 16 * u + 8 * (j >> 2) + 4 * h + (j & 3);
          o[j] = cs(which, (float)((inv ? kap * (2 * r + 1) : r * (2 * kap + 1)) & 63) * (1.0f / 64.0f));
        }
        val = u32x4{pack_f16(o[0], o[1]), pack_f16(o[2], o[3]), pack_f16(o[4], o[5]), pack_f16(o[6], o[7])};
      } else if (b < 50) {
        const bool inv = b >= 34;
        const int q = inv ? b - 34 : b - 18, p = q & 7, T = q >> 3;
#pragma unroll
        for (int z = 0; z < 2; ++z) {
          const int rho = 2 * p + z, cr = (rho & 3) + 8 * (rho >> 2) + 4 * h;
          const int n = inv ? (2 * r + T) * cr : (2 * cr + T) * r;
          const float a = (float)(n & 2047) * (1.0f / 2048.0f);
          val[z] = __float_as_uint(__builtin_amdgcn_cosf(a) * (inv ? 0.125f : 1.0f));
          val[2 + z] = __float_as_uint(inv ? __builtin_amdgcn_sinf(a) * 0.125f : -__builtin_amdgcn_sinf(a));
        }
      } else if (b < 52) {
        float o[8];
#pragma unroll
        for (int j = 0; j < 8; ++j) {
          const int k1 = 8 * (j >> 2) + 4 * h + (j & 3);
          const float v = cs(b - 50, (float)((r * k1) % 18) * (1.0f / 18.0f)) * ((k1 == 0 || k1 == 9) ? 1.f : 2.f);
          o[j] = (k1 < 10 && r < 18) ? v : 0.f;
        }
        val = u32x4{pack_f16(o[0], o[1]), pack_f16(o[2], o[3]), pack_f16(o[4], o[5]), pack_f16(o[6], o[7])};
      } else if (b < 58) {
        val = e32_frag(b - 52, lane);
      } else {
#pragma unroll
        for (int y = 0; y < 2; ++y) {
          const int rho = 2 * (b - 58) + y, k1 = ((rho & 3) + 8 * (rho >> 2) + 4 * h) & 15;
          const float a = (float)(k1 * r) * (1.0f / 576.0f);
          val[y] = __float_as_uint(__builtin_amdgcn_cosf(a) * 0.125f); val[2 + y] = __float_as_uint(__builtin_amdgcn_sinf(a) * 0.125f);
        }
      }
      store_dev(ws, (b < 50 ? OFF_TAB + b * 1024u : OFF_RT + (b - 50) * 1024u) + lane * 16, val);
    }
    WAIT_LGKM0();
    BAR();
    { const u64 tv = *(const u64*)(smem + P1_RED + 24);
      tag = (u64)__builtin_amdgcn_readfirstlane((unsigned)(tv >> 32)) << 32 | (unsigned)__builtin_amdgcn_readfirstlane((unsigned)tv); }
    if (wave != 10) {
      const int s2 = tb >> 2, t = (tb >> 1) & 1, h = tb & 1;
      for (int cb = wave == 11 ? 2 : wave - 8; cb < 9; cb += 3) {
        const int gg = lane >> 4, q = (lane & 15) >> 2, pp = lane & 3;
        const char* src = smem + P1_HS + q * P1_RS + (cb * 64 + gg * 16 + pp * 4) * 2;
        const int df = cb * 2 + (gg >> 1), i = 8 * q + 4 * (gg & 1) + pp;
        store_dev(ws, OFF_H + (unsigned)(df * 128 + 2 * s2 + t) * 1024u + (h * 32 + i) * 16, tr_frag(src, src + 4 * P1_RS));
      }
      WAIT_VM(0);
      unsigned last = 0;
      if (lane == 0) last = lds_arrive(smem + P1_RED + 16);
      if (__builtin_amdgcn_readfirstlane(last) == 2u) { if (lane < 8) set_flag(ws, SY_H + lane * 2048 + tb * 8, tag); }
      if (wave == 8 && lane == 0 && lds_arrive(smem + P1_RED + 36) == 8u) set_flag(ws, SY_A + b * 8, tag);
    }
  }
  if (idx < 27) {
    char* ebuf = smem + (29 % 5) * CG1::STAGE;
    u32x2 bv[3][2];
#pragma unroll
    for (int it = 0; it < 3; ++it) {
      const u32x4* bp = (const u32x4*)(b1 + ft * 128 + (min(wave + 11 * it, 23) / 3) * 16);
      const u32x4 b0 = bp[0], b1v = bp[1];
      const bool hi = lane >= 32;
      bv[it][0] = u32x2{hi ? b0[2] : b0[0], hi ? b0[3] : b0[1]}; bv[it][1] = u32x2{hi ? b1v[2] : b1v[0], hi ? b1v[3] : b1v[1]};
      asm volatile("" : "+v"(bv[it][0]), "+v"(bv[it][1]));
    }
    if (wave == 11) {
      const unsigned fh = SY_H + g * 2048 + lane * 8;
      poll_flags<1>(ws, fh, true, tag);
      if (lane == 0) lds_arrive(smem + P1_RED + 40);
      bool ok[3];
#pragma unroll
      for (int q = 0; q < 3; ++q) ok[q] = __all(__hip_atomic_load((gu64*)(ws + fh + (q + 1) * 512), RLX_AGENT) == tag);
      for (int tau = 0; tau < 32; ++tau) {
#pragma unroll
        for (int q = 0; q < 3; ++q)
          if (tau == 8 * q + 5 && !ok[q]) poll_flags<1>(ws, fh + (q + 1) * 512, true, tag);
        if (tau == 21 && b == 0 && lane == 0) set_flag(ws, SY_EPOCH, tag);
        BAR();
        if (tau == 29) {
#pragma unroll
          for (int i = 0; i < PF2T; ++i) dma4(rs_w2, ldsb + P1_DUMMY + wave * 256, w2_line((i * 27 + idx) * 64 + lane, g));
        }
      }
    } else if (wave >= 8) {
      auto issue_h = [&](int kt, int so) {
#pragma unroll
        for (int m = 0; m < 4; ++m)
          dma16<2>(rs_ws, ldsb + so + CG1::A_BYTES + ((wave - 8) * 4 + m) * 1024, lane * 16,
                   OFF_H + (unsigned)((dt1 * 3 + (wave - 8)) * 128 + kt * 4 + m) * 1024u);
      };
      lds_wait(smem + P1_RED + 40);
      issue_h(0, 0); issue_h(1, CG1::STAGE); issue_h(2, 2 * CG1::STAGE);
      CG1::ring<4>(32, issue_h);
    } else if (wave >= 4) {
      CG1::ring<4>(32, [&](int kt, int so) { issue_w1x(wave - 4, kt, so); });
    } else {
      f32x16 acc[1][3] = {};
      int rslot = 0;
      CG1::consume<false>(smem, wave, lane, 32, rslot, acc);
#pragma unroll
      for (int j = 0; j < 3; ++j)
#pragma unroll
        for (int s = 0; s < 2; ++s) {
          u32x4 val;
#pragma unroll
          for (int e = 0; e < 4; ++e) val[e] = pack_bf16(acc[0][j][8 * s + 2 * e], acc[0][j][8 * s + 2 * e + 1]);
          *(u32x4*)(ebuf + ((wave * 2 + s) * 3 + j) * 1024 + lane * 16) = val;
        }
    }
    WAIT_LGKM0();
    BAR();
    if (wave < 11) {
#pragma unroll
      for (int it = 0; it < 3; ++it) {
        const int i = wave + 11 * it;
        if (i < 24) {
          const int z = i / 3, j = i - 3 * z;
          const u32x4 a = *(const u32x4*)(ebuf + i * 1024 + lane * 16);
          u32x4 val;
#pragma unroll
          for (int e = 0; e < 4; ++e) {
            const unsigned bb = bv[it][e >> 1][e & 1];
            const unsigned pk = pack_bf16(lo16(a[e]) + lo16(bb), hi16(a[e]) + hi16(bb));
            val[e] = pack_bf16(gelu_tanh(lo16(pk)), gelu_tanh(hi16(pk)));
          }
          store_dev(ws, OFF_U + (unsigned)((dt1 * 3 + j) * 288 + ft * 8 + z) * 1024u + lane * 16, val);
        }
      }
      WAIT_VM(0);
    } else if (lane == 0) *(unsigned*)(smem + 163832) = 0u;
    WAIT_LGKM0();
    BAR();
    if (threadIdx.x < 2) set_flag(ws, SY_U + threadIdx.x * 2048 + tile * 8, tag);
  } else {
    const int me = (idx - 27) * 8 + g;
    {
      const int total = (2048 - PF1_ROWS) * nl1;
      for (int i0 = ((idx - 27) * 12 + wave) * 64; i0 < total; i0 += 60 * 64) {
        dma4(rs_w1, ldsb + P1_DUMMY + wave * 256, w1_line(i0 + lane, total, PF1_ROWS, nl1, ft0 * 256u));
        WAIT_VM(7);
      }
    }
    if (me < 37) {
      if (wave == 8) poll_flags<4>(ws, SY_A + lane * 8, true, tag);
      BAR();
      const int cbk = me == 36 ? 0 : me;
      char* img = smem + 65536;
      for (int i = wave; i < 64; i += 12) {
        const int P = 4 * i + (lane >> 4);
        dma16<2>(rs_ws, ldsb + 65536 + i * 1024, (unsigned)P * 9216u + (unsigned)(((lane & 15) ^ (P & 15)) * 16), OFF_X1 + cbk * 256u);
      }
      for (int i = wave; i < TAB_BYTES / 1024; i += 12) dma16<2>(rs_ws, ldsb + i * 1024, lane * 16, OFF_TAB + i * 1024u);
      const bool act = me == 36 ? wave == 0 : wave < 8;
      const int c = me == 36 ? 288 : me * 8 + wave;
      const float wsc = __uint_as_float((253u - wmax_exp(ws, lane)) << 23) * (1.0f / 64.0f);
      WAIT_VM(0);
      BAR();
      const int slot_re = me == 36 ? 1 : 2 * wave, slot_im = c == 288 ? -2 : (c == 0 ? -1 : 2 * wave + 1);
      u32x4 zr[2][2], zi[2][2];
      if (act) {
#pragma unroll
        for (int z = 0; z < 4; ++z) {
          const char* p = img + (z * 64 + lane) * 256;
          zr[z >> 1][z & 1] = *(const u32x4*)(p + ((slot_re ^ (lane & 15)) * 16));
          zi[z >> 1][z & 1] = slot_im >= 0 ? *(const u32x4*)(p + ((slot_im ^ (lane & 15)) * 16)) : u32x4{0, 0, 0, 0};
        }
      }
      WAIT_LGKM0();
      BAR();
      if (act) colfft_wave(smem + lane * 16, ws, c, lane, wsc, zr, zi, img, slot_re, slot_im);
      else if (wave >= 8 && idx < 31) {
        __builtin_amdgcn_s_sleep(64);
        for (int i0 = ((idx - 27) * 4 + wave - 8) * 64; i0 < PF2C_ROWS * 16; i0 += 16 * 64) {
          dma4(rs_w2, ldsb + P1_DUMMY + wave * 256, w2_line(PF2T * 27 * 64 + i0 + lane, g));
          WAIT_VM(7);
        }
      }
      WAIT_LGKM0();
      BAR();
      if (me == 36) {
        if (wave < 4) store_dev(ws, OFF_Y288 + wave * 1024 + lane * 16, *(const u32x4*)(img + wave * 1024 + lane * 16));
      } else {
        for (int i = wave; i < 64; i += 12) {
          const int Qy = 4 * i + (lane >> 4);
          store_dev(ws, OFF_Y1 + (unsigned)Qy * 9216u + cbk * 256 + (lane & 15) * 16,
                    *(const u32x4*)(img + Qy * 256 + (((lane & 15) ^ ((Qy >> 3) & 15)) * 16)));
        }
      }
      WAIT_VM(0);
      BAR();
      if (threadIdx.x == 0) set_flag(ws, SY_Y + me * 8, tag);
    }
  }
  if (idx < 24) {
    const int ks = g >> 1, dt = idx % 3, st = (g & 1) * 8 + idx / 3;
    const int rot = (g & 1) * 9;
    const unsigned myflags = SY_T + (unsigned)((st * 4 + ks) * 3 + dt) * 64u;
    if (wave >= 8) {
      auto issue_u = [&](int k0, int so) {
        const int kt = k0 + rot < 18 ? k0 + rot : k0 + rot - 18;
        if (k0 == 0 || k0 == 8) {
          const int kf = k0 == 0 ? rot : 9 - rot;
          if (wave == 8) {
            poll_flags<1>(ws, SY_U + (g & 1) * 2048 + (unsigned)((9 * ks + (kf >> 1) + (lane >> 1)) * 6 + 2 * dt + (lane & 1)) * 8u, lane < 10, tag);
            if (k0 == 0) { if (lane == 0) lds_arrive(smem + 163832); }
          } else if (k0 == 0) lds_wait(smem + 163832);
        }
#pragma unroll
        for (int m = 0; m < 6; ++m) {
          const int a = (wave - 8) * 6 + m;
          dma16<2>(rs_ws, ldsb + so + a * 1024, lane * 16, OFF_U + (unsigned)((dt * 6 + (a >> 2)) * 288 + (ks * 18 + kt) * 4 + (a & 3)) * 1024u);
        }
      };
      issue_u(0, 0); issue_u(1, CG2::STAGE);
      CG2::ring<6>(18, issue_u);
      BAR();
      if (wave == 8) poll_flags<1>(ws, myflags + (lane & 3) * 8, true, tag);
      BAR();
      return;
    }
    if (wave >= 4) {
      auto issue_w2 = [&](int k0, int so) {
        const int kt = k0 + rot < 18 ? k0 + rot : k0 + rot - 18;
        constexpr unsigned ld = S_ * 2;
        const unsigned vo = CG2::voff<TR128>(lane, ld);
#pragma unroll
        for (int m = 0; m < 4; ++m) {
          const int a = (wave - 4) * 4 + m;
          const unsigned sof = (unsigned)((ks * 18 + kt) * 64 + 4 * a) * ld + st * 256u;
          if (k0 >= NTF2) dma16<1>(rs_w2, ldsb + so + CG2::A_BYTES + a * 1024, vo, sof);
          else dma16<0>(rs_w2, ldsb + so + CG2::A_BYTES + a * 1024, vo, sof);
        }
      };
      issue_w2(0, 0); issue_w2(1, CG2::STAGE);
      CG2::ring<4>(18, issue_w2);
      BAR();
      BAR();
      return;
    }
    f32x16 acc[3][2] = {};
    int rslot = 0;
    CG2::consume<false>(smem, wave, lane, 18, rslot, acc);
    const int wa = wave >> 1, wb = wave & 1;
    const bool own0 = wb * 2 == ks, own1 = wb * 2 + 1 == ks;
#pragma unroll
    for (int j = 0; j < 2; ++j) {
      if (j ? own1 : own0) continue;
#pragma unroll
      for (int i = 0; i < 3; ++i) {
        const unsigned o = OFF_SLAB + (unsigned)((ks * 64 + st * 4 + wb * 2 + j) * 18 + dt * 6 + wa * 3 + i) * 2048u + lane * 16;
#pragma unroll
        for (int u = 0; u < 2; ++u) {
          u32x4 val;
#pragma unroll
          for (int e = 0; e < 4; ++e) val[e] = pack_bf16(acc[i][j][8 * u + 2 * e], acc[i][j][8 * u + 2 * e + 1]);
          store_dev(ws, o + u * 1024, val);
        }
      }
    }
    WAIT_VM(0);
    BAR();
    if (wave == 0 && lane < 4 && lane != ks) set_flag(ws, SY_T + (unsigned)((st * 4 + lane) * 3 + dt) * 64u + ks * 8, tag);
    BAR();
    if (!own0 && !own1) return;
    const int sf = st * 4 + ks, s = sf * 32 + (lane & 31);
    auto finish = [&](auto J) {
      constexpr int j = decltype(J)::value;
      u32x4 pv[3][2][4];
#pragma unroll
      for (int i = 0; i < 3; ++i)
#pragma unroll
        for (int u = 0; u < 2; ++u) {
          const unsigned o = (unsigned)(sf * 18 + dt * 6 + wa * 3 + i) * 2048u + u * 1024 + lane * 16;
#pragma unroll
          for (int q = 0; q < 3; ++q) pv[i][u][q] = load_dev(ws, OFF_SLAB + o + ((ks + 1 + q) & 3) * 2359296u);
          pv[i][u][3] = load_dev(ws, OFF_R + o);
        }
#pragma unroll
      for (int i = 0; i < 3; ++i)
#pragma unroll
        for (int u = 0; u < 2; ++u) {
          u32x4 o;
#pragma unroll
          for (int e = 0; e < 4; ++e)
            o[e] = pack_bf16(acc[i][j][8 * u + 2 * e] + (lo16(pv[i][u][0][e]) + lo16(pv[i][u][1][e])) + (lo16(pv[i][u][2][e]) + lo16(pv[i][u][3][e])),
                             acc[i][j][8 * u + 2 * e + 1] + (hi16(pv[i][u][0][e]) + hi16(pv[i][u][1][e])) + (hi16(pv[i][u][2][e]) + hi16(pv[i][u][3][e])));
          *(u32x4*)(out + (size_t)s * D_ + (dt * 6 + wa * 3 + i) * 32 + (lane >> 5) * 16 + u * 8) = o;
        }
    };
    if (own0) finish(IC<0>{}); else finish(IC<1>{});
  } else {
    const int me = (idx - 24) * 8 + g, sr = lane & 31;
    char* kt = smem + 37888; char* yb = kt + 17408;
    u32x4 xv[3];
#pragma unroll
    for (int it = 0; it < 3; ++it) xv[it] = *(const u32x4*)(x + (size_t)(me * 32 + sr) * D_ + ((it * 12 + wave) * 2 + (lane >> 5)) * 8);
    const float bias = lo16(b2[me * 32 + sr]);
    if (wave == 8) { poll_flags<1, 16>(ws, SY_Y + min(lane, 36) * 8, true, tag); }
    BAR();
#pragma unroll
    for (int it = 0; it < 3; ++it) dma16<2>(rs_ws, ldsb + (it * 12 + wave) * 1024, lane * 16, OFF_Y1 + me * 36864u + (it * 12 + wave) * 1024u);
    dma16<2>(rs_ws, ldsb + 37888 + wave * 1024, lane * 16, OFF_RT + wave * 1024u);
    if (wave < 4) dma16<2>(rs_ws, ldsb + 37888 + (12 + wave) * 1024, lane * 16, OFF_RT + (12 + wave) * 1024u);
    if (wave == 4) dma16<2>(rs_ws, ldsb + 37888 + 16384, (lane & 3) * 16, OFF_Y288 + me * 64u);
    const float fs = __uint_as_float((wmax_exp(ws, lane) + 1u) << 23) * (1.0f / 9.0f);
    WAIT_VM(0);
    BAR();
    for (int task = wave; task < 16; task += 12)
      irfft_wave(smem + (task >> 2) * 9216, kt + 16384 + (task >> 2) * 16, task & 3, lane, yb + task * 2 * FIN_OS, kt + lane * 16);
    WAIT_LGKM0();
    BAR();
#pragma unroll
    for (int it = 0; it < 3; ++it) {
      const int dp = (it * 12 + wave) * 2 + (lane >> 5);
      const char* yp = yb + sr * FIN_OS + dp * 32;
      const f32x4 y0 = *(const f32x4*)yp, y1 = *(const f32x4*)(yp + 16);
      u32x4 o;
#pragma unroll
      for (int e = 0; e < 4; ++e) {
        const float ya = e < 2 ? y0[2 * e] : y1[2 * e - 4], yc = e < 2 ? y0[2 * e + 1] : y1[2 * e - 3];
        o[e] = pack_bf16(lo16(xv[it][e]) + bias + ya * fs, hi16(xv[it][e]) + bias + yc * fs);
      }
      store_dev(ws, OFF_R + (unsigned)(me * 18 + (dp >> 2)) * 2048u + (dp & 1) * 1024 + (((dp >> 1) & 1) * 32 + sr) * 16, o);
    }
    WAIT_VM(0);
    BAR();
    if (wave == 0 && lane < 3) set_flag(ws, SY_T + (unsigned)(me * 3 + lane) * 64u + (me & 3) * 8, tag);
  }
}

extern "C" void kernel_launch(void* const* d_in, const int* in_sizes, int n_in, void* d_out, int out_size, void* d_ws, size_t ws_size,
                              hipStream_t stream) {
  (void)hipFuncSetAttribute((const void*)k_all, hipFuncAttributeMaxDynamicSharedMemorySize, 163840);
  k_all<<<256, 768, 163840, stream>>>((const u16*)d_in[0], (const u16*)d_in[1], (const u16*)d_in[2], (const char*)d_in[4], (const u16*)d_in[5],
                                      (const char*)d_in[6], (const u16*)d_in[7], (char*)d_ws, (u16*)d_out);
}
```

```cpp
#include <hip/hip_runtime.h>
#include <hip/hip_bf16.h>
#include <stdint.h>

#define DI __device__ __forceinline__
#define LAS __attribute__((address_space(3)))
typedef unsigned short u16;
typedef unsigned long long u64;
typedef __attribute__((ext_vector_type(2))) float f32x2;
typedef __attribute__((ext_vector_type(4))) float f32x4;
typedef __attribute__((ext_vector_type(16))) float f32x16;
typedef __attribute__((ext_vector_type(2))) _Float16 f16x2;
typedef __attribute__((ext_vector_type(8))) _Float16 f16x8;
typedef __attribute__((ext_vector_type(2))) __bf16 bf16x2;
typedef __attribute__((ext_vector_type(8))) __bf16 bf16x8;
typedef __attribute__((ext_vector_type(4))) short s16x4;
typedef __attribute__((ext_vector_type(2))) unsigned u32x2;
typedef __attribute__((ext_vector_type(4))) unsigned u32x4;
typedef __attribute__((address_space(1))) unsigned gu32;
typedef __attribute__((address_space(1))) u64 gu64;
#define RLX_AGENT __ATOMIC_RELAXED, __HIP_MEMORY_SCOPE_AGENT

constexpr int PF1_ROWS = (160 * 64) / 10;
constexpr int PF2T = 2;
constexpr int PF2C_ROWS = 488;
constexpr int NTF2 = 11;
constexpr unsigned CODE_WARM = 40960;
constexpr int POLL_SLEEP = 4;

constexpr int S_ = 2048, D_ = 576, F_ = 4608, NC_ = 289;

constexpr unsigned OFF_H    = 0;
constexpr unsigned OFF_WT   = OFF_H + 2359296;
constexpr unsigned OFF_Y288 = OFF_WT + 2367488;
constexpr unsigned OFF_WMAX = OFF_Y288 + 4096;
constexpr unsigned OFF_X1   = OFF_WMAX + 4096;
constexpr unsigned OFF_Y1   = OFF_X1 + 2359296;
constexpr unsigned OFF_U    = OFF_Y1 + 2359296;
constexpr unsigned OFF_SLAB = OFF_U + 5308416;
constexpr unsigned OFF_R    = OFF_SLAB + 9437184;
constexpr unsigned OFF_SYNC = OFF_R + 2359296;
constexpr unsigned SY_EPOCH = OFF_SYNC, SY_TMO = OFF_SYNC + 256, SY_A = OFF_SYNC + 2048, SY_Y = OFF_SYNC + 4096, SY_U = OFF_SYNC + 6144,
                   SY_H = SY_U + 2 * 2048, SY_T = SY_H + 8 * 2048;
constexpr unsigned WS_END   = SY_T + 192 * 64;
constexpr int TAB_E1 = 0, TAB_EO = 6144, TAB_EOI = 12288, TAB_TW1 = 18432, TAB_TW2 = 34816, TAB_BYTES = 51200;

constexpr double cx_sincos(int n, int N, bool want_sin) {
  n %= N; if (n < 0) n += N;
  int m = 8 * n, oct = m / N; m -= oct * N;
  bool s = want_sin; double sign = 1.0;
  if (oct & 1) m = N - m;
  const int q = ((oct + 1) >> 1) & 3;
  double sg = (oct & 1) ? -1.0 : 1.0;
  bool use_sin = false;
  if (s) { use_sin = (q & 1) == 0; sign = (q == 2 || q == 3) ? -1.0 : 1.0; }
  else { use_sin = (q & 1) == 1; sign = (q == 1 || q == 2) ? -1.0 : 1.0; }
  const double r = 0.78539816339744830962 * (double)m / (double)N, r2 = r * r;
  double t = use_sin ? r : 1.0, sum = t;
  for (int k = use_sin ? 2 : 1; k < 22; k += 2) { t *= -r2 / (double)(k * (k + 1)); sum += t; }
  return sign * (use_sin ? sg * sum : sum);
}
constexpr u16 cx_half(double v) {
  const u16 sg = v < 0 ? 0x8000 : 0; if (v < 0) v = -v;
  if (v < 2.9802322387695312e-08) return sg;
  int e = 1; double p = 2.0;
  while (v < p && e > -14) { p *= 0.5; --e; }
  const double x = v / p * 1024.0;
  int i = (int)x; const double fr = x - i;
  if (fr > 0.5 || (fr == 0.5 && (i & 1))) ++i;
  return (u16)(sg | (v < p ? i : ((e + 15) << 10) + (i - 1024)));
}
struct CxBase { double c576[576], s576[576]; float c2k[2048], s2k[2048]; };
constexpr CxBase cx_base() {
  CxBase b{};
  for (int i = 0; i < 576; ++i) { b.c576[i] = cx_sincos(i, 576, false); b.s576[i] = cx_sincos(i, 576, true); }
  for (int i = 0; i < 2048; ++i) { b.c2k[i] = (float)cx_sincos(i, 2048, false); b.s2k[i] = (float)cx_sincos(i, 2048, true); }
  return b;
}
constexpr CxBase CXB = cx_base();
constexpr double cx_cs(int which, int n576) { return which == 0 ? CXB.c576[n576 % 576] : (which == 1 ? -CXB.s576[n576 % 576] : CXB.s576[n576 % 576]); }
constexpr int cx_kap(int t, int h, int j) { return 16 * t + 8 * (j >> 2) + 4 * h + (j & 3); }
constexpr int cx_row(int rho, int h) { return (rho & 3) + 8 * (rho >> 2) + 4 * h; }
template <int NH, int NF> struct alignas(1024) CxTab { u16 h[NH * 512]; float f[NF * 256]; };
template <class T> constexpr void cx_e32(T& o, int base) {
  for (int fr = 0; fr < 6; ++fr) for (int l = 0; l < 64; ++l) for (int j = 0; j < 8; ++j)
    o.h[((base + fr) * 64 + l) * 8 + j] = cx_half(cx_cs(fr % 3, 18 * ((cx_kap(fr / 3, l >> 5, j) * (l & 31)) & 31)));
}
constexpr CxTab<12, 8> cx_fwd() {
  CxTab<12, 8> o{};
  for (int fr = 0; fr < 6; ++fr) for (int l = 0; l < 64; ++l) for (int j = 0; j < 8; ++j) {
    const int ks = fr >> 1, hh = l >> 5, r = (l & 31) >> 4, k1 = l & 15;
    const int row = ks < 2 ? ks : ((hh == 0 && j < 4) ? (j >> 1) : 2), d1 = ks < 2 ? cx_kap(0, hh, j) : 16 + (j & 1);
    o.h[(fr * 64 + l) * 8 + j] = (row == r && k1 < 10) ? cx_half(cx_cs(fr & 1, 32 * ((d1 * k1) % 18))) : (u16)0;
  }
  cx_e32(o, 6);
  for (int p = 0; p < 8; ++p) for (int l = 0; l < 64; ++l) for (int y = 0; y < 2; ++y) {
    const int n = cx_row(2 * p + y, l >> 5) * (l & 15);
    o.f[(p * 64 + l) * 4 + y] = (float)CXB.c576[n]; o.f[(p * 64 + l) * 4 + 2 + y] = (float)-CXB.s576[n];
  }
  return o;
}
constexpr CxTab<18, 32> cx_col() {
  CxTab<18, 32> o{};
  cx_e32(o, 0);
  for (int fr = 0; fr < 12; ++fr) for (int l = 0; l < 64; ++l) for (int j = 0; j < 8; ++j) {
    const int f2 = fr % 6, r = l & 31, kap = cx_kap(f2 / 3, l >> 5, j);
    o.h[((6 + fr) * 64 + l) * 8 + j] = cx_half(cx_cs(f2 % 3, 9 * ((fr >= 6 ? kap * (2 * r + 1) : r * (2 * kap + 1)) & 63)));
  }
  for (int q = 0; q < 32; ++q) for (int l = 0; l < 64; ++l) for (int z = 0; z < 2; ++z) {
    const bool inv = q >= 16;
    const int T = (q >> 3) & 1, r = l & 31, cr = cx_row(2 * (q & 7) + z, l >> 5), n = (inv ? (2 * r + T) * cr : (2 * cr + T) * r) & 2047;
    o.f[(q * 64 + l) * 4 + z] = CXB.c2k[n] * (inv ? 0.125f : 1.0f); o.f[(q * 64 + l) * 4 + 2 + z] = CXB.s2k[n] * (inv ? 0.125f : -1.0f);
  }
  return o;
}
constexpr CxTab<8, 8> cx_inv() {
  CxTab<8, 8> o{};
  for (int fr = 0; fr < 2; ++fr) for (int l = 0; l < 64; ++l) for (int j = 0; j < 8; ++j) {
    const int r = l & 31, k1 = cx_kap(0, l >> 5, j);
    o.h[(fr * 64 + l) * 8 + j] = (k1 < 10 && r < 18) ? cx_half(cx_cs(fr, 32 * ((r * k1) % 18)) * ((k1 == 0 || k1 == 9) ? 1.0 : 2.0)) : (u16)0;
  }
  cx_e32(o, 2);
  for (int p = 0; p < 8; ++p) for (int l = 0; l < 64; ++l) for (int y = 0; y < 2; ++y) {
    const int n = (cx_row(2 * p + y, l >> 5) & 15) * (l & 31);
    o.f[(p * 64 + l) * 4 + y] = (float)(CXB.c576[n] * 0.125); o.f[(p * 64 + l) * 4 + 2 + y] = (float)(CXB.s576[n] * 0.125);
  }
  return o;
}
__device__ const CxTab<12, 8> T_FWD = cx_fwd();
__device__ const CxTab<18, 32> T_COL = cx_col();
__device__ const CxTab<8, 8> T_INV = cx_inv();

DI unsigned pack_bf16(float a, float b) { f32x2 v = {a, b}; return __builtin_bit_cast(unsigned, __builtin_convertvector(v, bf16x2)); }
DI unsigned pack_f16(float a, float b) { f32x2 v = {a, b}; return __builtin_bit_cast(unsigned, __builtin_convertvector(v, f16x2)); }
DI float lo16(unsigned p) { return __uint_as_float(p << 16); }
DI float hi16(unsigned p) { return __uint_as_float(p & 0xffff0000u); }
DI float rbf(float a) { return lo16(pack_bf16(a, a)); }

template <int CTRL> DI float dpp_add(float v) {
  return v + __int_as_float(__builtin_amdgcn_update_dpp(0, __float_as_int(v), CTRL, 0xf, 0xf, true));
}
DI float sum32(float v) {
  v = dpp_add<0xB1>(v); v = dpp_add<0x4E>(v); v = dpp_add<0x141>(v); v = dpp_add<0x140>(v);
  auto r = __builtin_amdgcn_permlane16_swap(__float_as_uint(v), __float_as_uint(v), false, false);
  return __uint_as_float(r[0]) + __uint_as_float(r[1]);
}

#define WAIT_VM(n) asm volatile("s_waitcnt vmcnt(%0)" ::"n"(n) : "memory")
#define WAIT_LGKM0() asm volatile("s_waitcnt lgkmcnt(0)" ::: "memory")
#define BAR() do { asm volatile("" ::: "memory"); __builtin_amdgcn_s_barrier(); asm volatile("" ::: "memory"); } while (0)
#define SCHED0() __builtin_amdgcn_sched_barrier(0)

DI u32x4 mk_rsrc(const void* p, unsigned bytes) {
  const u64 a = (u64)p;
  return u32x4{(unsigned)a, (unsigned)(a >> 32) & 0xffffu, bytes, 0x00020000u};
}
template <int POL> DI void dma16(u32x4 rsrc, unsigned lds_off, unsigned voff, unsigned soff) {
  if constexpr (POL == 0) asm volatile("s_mov_b32 m0, %0\n\ts_nop 0\n\tbuffer_load_dwordx4 %1, %2, %3 offen lds" ::"s"(lds_off), "v"(voff), "s"(rsrc), "s"(soff) : "memory");
  if constexpr (POL == 1) asm volatile("s_mov_b32 m0, %0\n\ts_nop 0\n\tbuffer_load_dwordx4 %1, %2, %3 offen nt lds" ::"s"(lds_off), "v"(voff), "s"(rsrc), "s"(soff) : "memory");
  if constexpr (POL == 2) asm volatile("s_mov_b32 m0, %0\n\ts_nop 0\n\tbuffer_load_dwordx4 %1, %2, %3 offen sc1 lds" ::"s"(lds_off), "v"(voff), "s"(rsrc), "s"(soff) : "memory");
}
DI void dma4(u32x4 rsrc, unsigned lds_off, unsigned voff) {
  asm volatile("s_mov_b32 m0, %0\n\ts_nop 0\n\tbuffer_load_dword %1, %2, 0 offen lds" ::"s"(lds_off), "v"(voff), "s"(rsrc) : "memory");
}
DI void store_dev(char* ws, unsigned off, u32x4 v) {
  __builtin_amdgcn_raw_buffer_store_b128(v, __builtin_amdgcn_make_buffer_rsrc((void*)ws, 0, WS_END, 0x00020000), off, 0, 16);
}
DI u32x4 load_dev(const char* ws, unsigned off) {
  return __builtin_amdgcn_raw_buffer_load_b128(__builtin_amdgcn_make_buffer_rsrc((void*)ws, 0, WS_END, 0x00020000), off, 0, 16);
}
DI void set_flag(char* ws, unsigned off, u64 tag) { __hip_atomic_store((gu64*)(ws + off), tag, RLX_AGENT); }
template <int N, int SLP = POLL_SLEEP> DI void poll_flags(char* ws, unsigned off, bool active, u64 tag) {
  for (unsigned spins = 0;; ++spins) {
    bool ok = true;
#pragma unroll
    for (int i = 0; i < N; ++i) { const u64 v = __hip_atomic_load((gu64*)(ws + off + i * 512), RLX_AGENT); ok &= (v == tag) || !active; }
    if (__all(ok)) break;
    __builtin_amdgcn_s_sleep(SLP);
    if (spins > (1u << 17)) { set_flag(ws, SY_TMO, tag); break; }
    if ((spins & 63u) == 63u && __all(__hip_atomic_load((gu64*)(ws + SY_TMO), RLX_AGENT) == tag)) break;
  }
  __builtin_amdgcn_fence(__ATOMIC_ACQUIRE, "wavefront");
}
DI unsigned w1_line(int i, int total, int r0, int nl, unsigned c0) {
  const int rr = i / nl, l = i - rr * nl, row = r0 + rr;
  return i < total ? (unsigned)(64 * (row & 31) + (row >> 5)) * (F_ * 2) + c0 + (unsigned)l * 128u : 0xfffffff0u;
}

DI unsigned w2_line(int line, int g) {
  int row = (line >> 4) + (g & 1) * 576; if (row >= 1152) row -= 1152;
  return (unsigned)((g >> 1) * 1152 + row) * 4096u + (g & 1) * 2048u + (line & 15) * 128u;
}

template <bool F16> DI f32x16 mfma32(u32x4 a, u32x4 b, f32x16 c) {
  if constexpr (F16) return __builtin_amdgcn_mfma_f32_32x32x16_f16(__builtin_bit_cast(f16x8, a), __builtin_bit_cast(f16x8, b), c, 0, 0, 0);
  else return __builtin_amdgcn_mfma_f32_32x32x16_bf16(__builtin_bit_cast(bf16x8, a), __builtin_bit_cast(bf16x8, b), c, 0, 0, 0);
}
DI u32x4 pack8_f16(const f32x16& v, int u) {
  return u32x4{pack_f16(v[8 * u], v[8 * u + 1]), pack_f16(v[8 * u + 2], v[8 * u + 3]), pack_f16(v[8 * u + 4], v[8 * u + 5]), pack_f16(v[8 * u + 6], v[8 * u + 7])};
}
DI u32x4 tr_frag(const char* p0, const char* p1) {
  s16x4 lo = __builtin_amdgcn_ds_read_tr16_b64_v4i16((LAS s16x4*)p0);
  s16x4 hi = __builtin_amdgcn_ds_read_tr16_b64_v4i16((LAS s16x4*)p1);
  u32x2 a = __builtin_bit_cast(u32x2, lo), c = __builtin_bit_cast(u32x2, hi);
  return u32x4{a[0], a[1], c[0], c[1]};
}
DI void tw_piece(f32x16& re, f32x16& im, int p, const f32x4 w) {
  const f32x2 r = {re[2 * p], re[2 * p + 1]}, i = {im[2 * p], im[2 * p + 1]}, wr = {w[0], w[1]}, wi = {w[2], w[3]};
  const f32x2 a = __builtin_elementwise_fma(r, wr, -(i * wi)), b = __builtin_elementwise_fma(r, wi, i * wr);
  re[2 * p] = a[0]; re[2 * p + 1] = a[1]; im[2 * p] = b[0]; im[2 * p + 1] = b[1];
}
DI void tw_load(f32x4 (&t)[8], const char* tw) {
#pragma unroll
  for (int p = 0; p < 8; ++p) t[p] = *(const f32x4*)(tw + p * 1024);
}
DI void tw_apply(f32x16& re, f32x16& im, const f32x4 (&t)[8]) {
#pragma unroll
  for (int p = 0; p < 8; ++p) tw_piece(re, im, p, t[p]);
}
enum { LIN = 0, TR128 = 1, TR64 = 2 };

template <int V> struct IC { static constexpr int value = V; };
template <int NA_W, int NB_W, int WA, int WB, int AK, int BK, int NS, int DPM, int DEPTH>
struct GC {
  static constexpr int NC = WA * WB, NFA = NA_W * WA, NFB = NB_W * WB;
  static constexpr int A_BYTES = NFA * 4096, B_BYTES = NFB * 4096, STAGE = A_BYTES + B_BYTES;
  static constexpr int LDS_BYTES = NS * STAGE;
  static_assert(AK == LIN || NFA == (AK == TR128 ? 4 : 2), "");
  static_assert(BK == LIN || NFB == (BK == TR128 ? 4 : 2), "");

  template <int K> static DI unsigned voff(int lane, unsigned rstride) {
    if constexpr (K == LIN) return lane * 16;
    else if constexpr (K == TR128) return (unsigned)(lane >> 4) * rstride + (unsigned)(((lane & 15) ^ ((lane >> 4) << 2)) * 16);
    else return (unsigned)(lane >> 3) * rstride + (unsigned)(((lane & 7) ^ (((lane >> 4) & 1) << 2)) * 16);
  }
  template <int K, int NF> static DI void frag_addr(unsigned (&addr)[K == LIN ? 1 : NF], int opoff, int wpos, int lane) {
    if constexpr (K == LIN) addr[0] = opoff + wpos * NF * 4096 + lane * 16;
    else {
      const int h = lane >> 5, blk = (lane >> 4) & 1, q = (lane & 15) >> 2, p = lane & 3;
#pragma unroll
      for (int i = 0; i < NF; ++i) {
        const int ch = 4 * (wpos * NF + i) + 2 * blk + (p >> 1);
        if constexpr (K == TR128) addr[i] = opoff + (4 * h + q) * 256 + ((ch ^ (q << 2)) * 16) + 8 * (p & 1);
        else addr[i] = opoff + (4 * h + q) * 128 + ((ch ^ (((q >> 1) & 1) << 2)) * 16) + 8 * (p & 1);
      }
    }
  }
  template <int K, int NF> static DI void rd(u32x4 (&f)[NF], const char* smem, const unsigned (&addr)[K == LIN ? 1 : NF], int sb, int hh) {
#pragma unroll
    for (int i = 0; i < NF; ++i) {
      if constexpr (K == LIN) f[i] = *(const u32x4*)(smem + addr[0] + sb + (i * 4 + hh) * 1024);
      else {
        constexpr int RB = K == TR128 ? 256 : 128;
        const char* p = smem + addr[i] + sb + hh * 16 * RB;
        s16x4 lo = __builtin_amdgcn_ds_read_tr16_b64_v4i16((LAS s16x4*)p);
        s16x4 hi = __builtin_amdgcn_ds_read_tr16_b64_v4i16((LAS s16x4*)(p + 8 * RB));
        u32x2 a = __builtin_bit_cast(u32x2, lo), c = __builtin_bit_cast(u32x2, hi);
        f[i] = u32x4{a[0], a[1], c[0], c[1]};
      }
    }
  }
  static DI void ilv() {
    constexpr int NR = NA_W * (AK == LIN ? 1 : 2) + NB_W * (BK == LIN ? 1 : 2), NM = NA_W * NB_W;
    constexpr int NG = (NR + DPM - 1) / DPM < NM ? (NR + DPM - 1) / DPM : NM;
#pragma unroll
    for (int i = 0; i < NG; ++i) {
      __builtin_amdgcn_sched_group_barrier(0x8, 1, 0);
      if (i + 1 < NG) __builtin_amdgcn_sched_group_barrier(0x100, DPM, 0);
      else __builtin_amdgcn_sched_group_barrier(0x100, NR - DPM * (NG - 1), 0);
    }
    if constexpr (NM > NG) __builtin_amdgcn_sched_group_barrier(0x8, NM - NG, 0);
    SCHED0();
  }
  template <int G, class ISSUE> static DI void ring(int nt, ISSUE issue) {
    int wslot = NS - 2;
    for (int tau = 0; tau < nt - (NS - 2); ++tau) {
      WAIT_VM((NS - 3) * G);
      BAR();
      issue(tau + NS - 2, wslot * STAGE);
      wslot = (wslot + 1 == NS) ? 0 : wslot + 1;
    }
    if constexpr (NS >= 5) { WAIT_VM(2 * G); BAR(); }
    if constexpr (NS >= 4) { WAIT_VM(G); BAR(); }
    WAIT_VM(0); BAR();
    static_assert(NS >= 3 && NS <= 5, "");
  }
  template <bool F16>
  static DI void consume(const char* smem, int wave, int lane, int n, int& rslot, f32x16 (&acc)[NA_W][NB_W]) {
    const int wa = wave / WB, wb = wave - wa * WB;
    unsigned addrA[AK == LIN ? 1 : NA_W], addrB[BK == LIN ? 1 : NB_W];
    frag_addr<AK, NA_W>(addrA, 0, wa, lane);
    frag_addr<BK, NB_W>(addrB, A_BYTES, wb, lane);
    u32x4 fa[DEPTH][NA_W], fb[DEPTH][NB_W];
#pragma unroll
    for (int d = 1; d < DEPTH; ++d) {
#pragma unroll
      for (int i = 0; i < NA_W; ++i) fa[d][i] = u32x4{0, 0, 0, 0};
#pragma unroll
      for (int i = 0; i < NB_W; ++i) fb[d][i] = u32x4{0, 0, 0, 0};
    }
    auto mm = [&](const u32x4 (&a)[NA_W], const u32x4 (&b)[NB_W]) {
#pragma unroll
      for (int i = 0; i < NA_W; ++i)
#pragma unroll
        for (int j = 0; j < NB_W; ++j) acc[i][j] = mfma32<F16>(a[i], b[j], acc[i][j]);
    };
    auto step = [&](auto Z0) {
      constexpr int z0 = decltype(Z0)::value;
      BAR();
      SCHED0();
      const int sb = rslot * STAGE;
#pragma unroll
      for (int hh = 0; hh < 4; ++hh) {
        rd<AK, NA_W>(fa[(z0 + hh) % DEPTH], smem, addrA, sb, hh);
        rd<BK, NB_W>(fb[(z0 + hh) % DEPTH], smem, addrB, sb, hh);
        mm(fa[(z0 + hh + 1) % DEPTH], fb[(z0 + hh + 1) % DEPTH]);
        ilv();
      }
      rslot = (rslot + 1 == NS) ? 0 : rslot + 1;
    };
    if constexpr (DEPTH == 2) {
      for (int tau = 0; tau < n; ++tau) step(IC<0>{});
      mm(fa[1], fb[1]);
    } else {
      int tau = 0;
      for (; tau + 3 <= n; tau += 3) { step(IC<0>{}); step(IC<1>{}); step(IC<2>{}); }
      if (n - tau == 2) { step(IC<0>{}); step(IC<1>{}); mm(fa[0], fb[0]); mm(fa[1], fb[1]); }
      else if (n - tau == 1) { step(IC<0>{}); mm(fa[2], fb[2]); mm(fa[0], fb[0]); }
      else { mm(fa[1], fb[1]); mm(fa[2], fb[2]); }
    }
  }
};

DI float gelu_tanh(float u) {
  const float z = u * (1.0f + 0.044715f * u * u) * (-2.0f * 0.7978845608028654f * 1.4426950408889634f);
  return u * __builtin_amdgcn_rcpf(1.0f + __builtin_amdgcn_exp2f(z));
}

template <int NV> DI void mv_ilv() {
#pragma unroll
  for (int i = 0; i < 8; ++i) { __builtin_amdgcn_sched_group_barrier(0x8, 1, 0); __builtin_amdgcn_sched_group_barrier(0x2, NV, 0); }
  SCHED0();
}
struct EF { u32x4 r, i, n; };
DI EF ef_load(const char* p) { EF e; e.r = *(const u32x4*)p; e.i = *(const u32x4*)(p + 1024); e.n = *(const u32x4*)(p + 2048); return e; }
#define MF(a, b, c) mfma32<true>(a, b, c)
DI void cm_dA(f32x16& re, f32x16& im, u32x4 dr, u32x4 di, const EF& e) { re = MF(dr, e.r, re); im = MF(dr, e.i, im); re = MF(di, e.n, re); im = MF(di, e.r, im); }
DI void cm_dB(f32x16& re, f32x16& im, u32x4 dr, u32x4 di, const EF& e) { re = MF(e.r, dr, re); im = MF(e.i, dr, im); re = MF(e.n, di, re); im = MF(e.r, di, im); }
DI void cm_dAc(f32x16& re, f32x16& im, u32x4 dr, u32x4 di, const EF& e) { re = MF(dr, e.r, re); im = MF(dr, e.n, im); re = MF(di, e.i, re); im = MF(di, e.r, im); }
DI void wmul(f32x16& re, f32x16& im, const u32x4 (&wv)[4], float wsc) {
#pragma unroll
  for (int p = 0; p < 8; ++p) {
    const unsigned w0 = wv[p >> 1][(2 * p) & 3], w1 = wv[p >> 1][(2 * p + 1) & 3];
    tw_piece(re, im, p, f32x4{lo16(w0), lo16(w1), hi16(w0), hi16(w1)} * wsc);
  }
}
DI void bfly(f32x16& a, f32x16& b) {
  const f32x16 x = a, y = b; a = x + y; b = x - y;
}
DI void colfft_wave(const char* tab  , const char* ws, int c, int lane, float wsc, const u32x4 (&zr)[2][2], const u32x4 (&zi)[2][2],
                    char* img, int slot_re, int slot_im) {
  f32x4 tw[8];
  f32x16 r0 = {}, i0 = {}, r1 = {}, i1 = {};
  u32x4 ar[2], ai[2];
  EF ea = ef_load(tab + TAB_E1), eb = ef_load(tab + TAB_E1 + 3072);
  tw_load(tw, tab + TAB_TW1);
  SCHED0();
  cm_dA(r0, i0, zr[0][0], zi[0][0], ea); cm_dA(r0, i0, zr[0][1], zi[0][1], eb);
  SCHED0();
  cm_dA(r1, i1, zr[1][0], zi[1][0], ea); cm_dA(r1, i1, zr[1][1], zi[1][1], eb);
#pragma unroll
  for (int p = 0; p < 8; ++p) { tw_piece(r0, i0, p, tw[p]); tw[p] = *(const f32x4*)(tab + TAB_TW1 + 8192 + p * 1024); }
  ar[0] = pack8_f16(r0, 0); ar[1] = pack8_f16(r0, 1); ai[0] = pack8_f16(i0, 0); ai[1] = pack8_f16(i0, 1);
  mv_ilv<6>();
  u32x4 wv0[4], wv1[4];
#pragma unroll
  for (int i = 0; i < 4; ++i) wv0[i] = load_dev(ws, OFF_WT + c * 8192u + i * 1024 + lane * 16);
  r0 = f32x16{}; i0 = f32x16{};
  cm_dB(r0, i0, ar[0], ai[0], ea); ea = ef_load(tab + TAB_EO);
  cm_dB(r0, i0, ar[1], ai[1], eb); eb = ef_load(tab + TAB_EO + 3072);
  tw_apply(r1, i1, tw);
  ar[0] = pack8_f16(r1, 0); ar[1] = pack8_f16(r1, 1); ai[0] = pack8_f16(i1, 0); ai[1] = pack8_f16(i1, 1);
  mv_ilv<6>();
#pragma unroll
  for (int i = 0; i < 4; ++i) wv1[i] = load_dev(ws, OFF_WT + c * 8192u + (4 + i) * 1024 + lane * 16);
  r1 = f32x16{}; i1 = f32x16{};
  cm_dB(r1, i1, ar[0], ai[0], ea); ea = ef_load(tab + TAB_E1);
  cm_dB(r1, i1, ar[1], ai[1], eb); eb = ef_load(tab + TAB_E1 + 3072);
  SCHED0();
  bfly(r0, r1); bfly(i0, i1);
  wmul(r0, i0, wv0, wsc);
  wmul(r1, i1, wv1, wsc);
  bfly(r0, r1); bfly(i0, i1);
  u32x4 dr[2], di[2];
  ar[0] = pack8_f16(r0, 0); ar[1] = pack8_f16(r0, 1); ai[0] = pack8_f16(i0, 0); ai[1] = pack8_f16(i0, 1);
  SCHED0();
  tw_load(tw, tab + TAB_TW2);
  r0 = f32x16{}; i0 = f32x16{};
  cm_dAc(r0, i0, ar[0], ai[0], ea); ea = ef_load(tab + TAB_EOI);
  cm_dAc(r0, i0, ar[1], ai[1], eb); eb = ef_load(tab + TAB_EOI + 3072);
  dr[0] = pack8_f16(r1, 0); dr[1] = pack8_f16(r1, 1); di[0] = pack8_f16(i1, 0); di[1] = pack8_f16(i1, 1);
  mv_ilv<2>();
  r1 = f32x16{}; i1 = f32x16{};
  cm_dAc(r1, i1, dr[0], di[0], ea); ea = ef_load(tab + TAB_E1);
  cm_dAc(r1, i1, dr[1], di[1], eb); eb = ef_load(tab + TAB_E1 + 3072);
#pragma unroll
  for (int p = 0; p < 8; ++p) { tw_piece(r0, i0, p, tw[p]); tw[p] = *(const f32x4*)(tab + TAB_TW2 + 8192 + p * 1024); }
  ar[0] = pack8_f16(r0, 0); ar[1] = pack8_f16(r0, 1); ai[0] = pack8_f16(i0, 0); ai[1] = pack8_f16(i0, 1);
  mv_ilv<6>();
  r0 = f32x16{}; i0 = f32x16{};
  cm_dAc(r0, i0, ar[0], ai[0], ea); cm_dAc(r0, i0, ar[1], ai[1], eb);
  tw_apply(r1, i1, tw);
  dr[0] = pack8_f16(r1, 0); dr[1] = pack8_f16(r1, 1); di[0] = pack8_f16(i1, 0); di[1] = pack8_f16(i1, 1);
  mv_ilv<6>();
  r1 = f32x16{}; i1 = f32x16{};
  cm_dAc(r1, i1, dr[0], di[0], ea); cm_dAc(r1, i1, dr[1], di[1], eb);
  SCHED0();
#pragma unroll
  for (int g = 0; g < 4; ++g) {
    const int Q = (lane & 31) * 8 + 2 * g + (lane >> 5), sw = lane & 15, r = 4 * g;
    if (slot_im == -2) {
      *(u32x4*)(img + Q * 16) = u32x4{pack_f16(r0[r], r1[r]), pack_f16(r0[r + 1], r1[r + 1]), pack_f16(r0[r + 2], r1[r + 2]), pack_f16(r0[r + 3], r1[r + 3])};
      continue;
    }
#pragma unroll
    for (int z = 0; z < 2; ++z) {
      const int q = r + 2 * z;
      *(u32x4*)(img + Q * 256 + (((slot_re + z) ^ sw) * 16)) =
          u32x4{pack_f16(r0[q], i0[q]), pack_f16(r1[q], i1[q]), pack_f16(r0[q + 1], i0[q + 1]), pack_f16(r1[q + 1], i1[q + 1])};
    }
  }
}
#undef MF
constexpr int FIN_OS = 2336;
DI void irfft_wave(const char* rec, const char* y288, int w, int lane, char* orow, const char* kt) {
  const int row = lane & 31, hh = lane >> 5, k1 = row & 15, rr = 2 * w + (row >> 4);
  const bool ok = k1 < 10;
  const int k1c = ok ? k1 : 0;
  const char* pd = rec + rr * 4 + 32 * (k1c + 72 * hh);
  const char* pm = rec + rr * 4 + 32 * (576 - k1c - 72 * hh - 18 * 27);
  unsigned v[2][8];
#pragma unroll
  for (int j = 0; j < 8; ++j) {
    v[0][j] = *(const unsigned*)(pd + 576 * (8 * (j >> 2) + (j & 3)));
    v[1][j] = *(const unsigned*)(pm + 576 * (27 - (16 + 8 * (j >> 2) + (j & 3))));
  }
  const unsigned v288 = *(const u16*)(y288 + rr * 2);
  f32x4 tw[8];
  tw_load(tw, kt + 8192);
  if (k1 == 0 && hh == 0) { v[0][0] &= 0xffffu; v[1][0] = v288; }
  f32x16 re = {}, im = {};
#pragma unroll
  for (int u = 0; u < 2; ++u) {
    u32x4 a_r, a_i;
#pragma unroll
    for (int e = 0; e < 4; ++e) {
      a_r[e] = ok ? __builtin_amdgcn_perm(v[u][2 * e + 1], v[u][2 * e], 0x05040100u) : 0u;
      a_i[e] = ok ? (__builtin_amdgcn_perm(v[u][2 * e + 1], v[u][2 * e], 0x07060302u) ^ (u ? 0x80008000u : 0u)) : 0u;
    }
    cm_dAc(re, im, a_r, a_i, ef_load(kt + 2048 + u * 3072));
  }
  const u32x4 cr = *(const u32x4*)kt, ci = *(const u32x4*)(kt + 1024);
  tw_apply(re, im, tw);
#pragma unroll
  for (int u = 0; u < 2; ++u) {
    f32x16 y = {};
    y = mfma32<true>(cr, pack8_f16(re, u), y);
    y = mfma32<true>(ci, pack8_f16(im, u), y);
    float* o = (float*)(orow + u * FIN_OS) + (lane & 31) + 128 * hh;
#pragma unroll
    for (int rho = 0; rho < 8; ++rho) o[32 * ((rho & 3) + 8 * (rho >> 2))] = y[rho];
    if (hh == 0) { o[32 * 16] = y[8]; o[32 * 17] = y[9]; }
  }
}

DI unsigned wmax_exp(const char* ws, int lane) {
  const u32x4 mv = load_dev(ws, OFF_WMAX + lane * 16);
  unsigned mx = max(max(mv[0], mv[1]), max(mv[2], mv[3]));
#pragma unroll
  for (int o = 32; o; o >>= 1) mx = max(mx, (unsigned)__shfl_xor((int)mx, o));
  return min(mx >> 7, 252u);
}

DI unsigned lds_arrive(char* p) { return __hip_atomic_fetch_add((LAS unsigned*)p, 1u, __ATOMIC_RELAXED, __HIP_MEMORY_SCOPE_WORKGROUP); }
DI void lds_arrive_wait(char* p, unsigned n, int lane) {
  if (lane == 0) lds_arrive(p);
  while (__hip_atomic_load((LAS unsigned*)p, __ATOMIC_RELAXED, __HIP_MEMORY_SCOPE_WORKGROUP) < n) __builtin_amdgcn_s_sleep(1);
  asm volatile("" ::: "memory");
}
DI void lds_wait(const char* p) {
  while (__hip_atomic_load((const LAS unsigned*)p, __ATOMIC_RELAXED, __HIP_MEMORY_SCOPE_WORKGROUP) == 0u) __builtin_amdgcn_s_sleep(1);
  asm volatile("" ::: "memory");
}
using CG1 = GC<1, 3, 4, 1, TR128, LIN, 5, 2, 3>;
using CG2 = GC<3, 2, 2, 2, LIN, TR128, 4, 2, 2>;
constexpr int P1_RS = 1216, P1_XS = 3 * CG1::STAGE, P1_HS = P1_XS + 8 * P1_RS, P1_C1 = P1_XS + 16 * P1_RS, P1_E = P1_C1 + 6144,
              P1_TW = P1_E + 6144, P1_O = P1_TW + 8192, P1_WT = P1_O + 9216, P1_RED = P1_WT + 10512, P1_DUMMY = 147456;
static_assert(P1_RED + 48 <= P1_DUMMY && P1_DUMMY >= CG1::LDS_BYTES, "");

__global__ __launch_bounds__(768, 3) void k_all(const u16* __restrict__ x, const u16* __restrict__ wr, const u16* __restrict__ wi,
                                                const char* __restrict__ w1, const u16* __restrict__ b1, const char* __restrict__ w2,
                                                const u16* __restrict__ b2, char* __restrict__ ws, u16* __restrict__ out) {
  extern __shared__ __attribute__((aligned(16))) char smem[];
  const int lane = threadIdx.x & 63, wave = __builtin_amdgcn_readfirstlane(threadIdx.x >> 6);
  const int b = blockIdx.x, g = b & 7, idx = b >> 3;
  const unsigned ldsb = (unsigned)(size_t)(LAS char*)smem;
  const u32x4 rs_ws = mk_rsrc(ws, WS_END), rs_w1 = mk_rsrc(w1, 18874368u), rs_w2 = mk_rsrc(w2, 18874368u);
  if (wave == 11 && idx < 6) {
    const u64 pc = __builtin_amdgcn_s_getpc() & ~(u64)127;
    dma4(mk_rsrc((const void*)pc, CODE_WARM), ldsb + P1_DUMMY + wave * 256, (unsigned)(idx * 64 + lane) * 128u);
  }
  const int tile = g * 27 + idx, ft = tile / 6, dt1 = tile - ft * 6;
  const int ft0 = (g * 27) / 6, nl1 = ((g * 27 + 26) / 6 - ft0 + 1) * 2;
  auto issue_w1x = [&](int lw, int kt, int so) {
    constexpr unsigned ld = F_ * 2;
    const unsigned vo = CG1::voff<TR128>(lane, 64 * ld);
#pragma unroll
    for (int m = 0; m < 4; ++m) {
      const int a = lw * 4 + m;
      dma16<0>(rs_w1, ldsb + so + a * 1024, vo, (unsigned)(((4 * a) & 31) * 64 + 2 * kt + (a >> 3)) * ld + ft * 256u);
    }
  };
  const int tb = (g >> 2) * 128 + idx * 4 + (g & 3);
  auto gate = [&] {
    while (__hip_atomic_load((const LAS unsigned*)(smem + P1_RED + 16), __ATOMIC_RELAXED, __HIP_MEMORY_SCOPE_WORKGROUP) < 3u) __builtin_amdgcn_s_sleep(1);
  };
  u64 tag;
  if (__builtin_expect(wave < 4, 1)) {
    const int tid = threadIdx.x, s2 = tb >> 2, t = (tb >> 1) & 1, h = tb & 1;
    const int rr = tid >> 5, l32 = tid & 31;
    const int s1 = 16 * t + 8 * (rr >> 2) + 4 * h + (rr & 3);
    const unsigned* xr = (const unsigned*)(x + (size_t)(64 * s1 + s2) * D_);
    unsigned raw[9];
#pragma unroll
    for (int i = 0; i < 9; ++i) raw[i] = xr[32 * i + l32];
    float v[18];
#pragma unroll
    for (int i = 0; i < 9; ++i) { v[2 * i] = lo16(raw[i]); v[2 * i + 1] = hi16(raw[i]); }
#pragma unroll
    for (int pass = 0; pass < 2; ++pass) {
      float sm = 0.f;
#pragma unroll
      for (int i = 0; i < 18; ++i) sm += v[i];
      const float mean = sum32(sm) * (1.0f / 576.0f);
      float sq = 0.f;
#pragma unroll
      for (int i = 0; i < 18; ++i) { float dlt = v[i] - mean; sq += dlt * dlt; }
      const float var = sum32(sq) * (1.0f / 576.0f);
      const float mb = rbf(mean);
      const float sd = rbf(__builtin_sqrtf(rbf(rbf(var) + 1e-5f)));
      const float rs = 1.0f / sd;
      char* dst = smem + P1_XS + pass * (8 * P1_RS) + rr * P1_RS + l32 * 4;
#pragma unroll
      for (int i = 0; i < 9; ++i) {
        unsigned p = pack_bf16(v[2 * i] - mb, v[2 * i + 1] - mb);
        p = pack_bf16(lo16(p) * rs, hi16(p) * rs);
        v[2 * i] = lo16(p); v[2 * i + 1] = hi16(p);
        *(unsigned*)(dst + i * 128) = pass == 0 ? pack_f16(v[2 * i], v[2 * i + 1]) : p;
      }
    }
    WAIT_LGKM0();
    BAR();
    { const u64 tv = *(const u64*)(smem + P1_RED + 24);
      tag = (u64)__builtin_amdgcn_readfirstlane((unsigned)(tv >> 32)) << 32 | (unsigned)__builtin_amdgcn_readfirstlane((unsigned)tv); }
    {
      const int hh = lane >> 5, blk = (lane >> 4) & 1, q = (lane & 15) >> 2, p = lane & 3;
      const char* xa = smem + P1_XS + (2 * wave) * P1_RS + blk * 32 + p * 8;
      const char* cf = smem + P1_C1 + lane * 16;
      f32x16 re = {}, im = {};
      f32x4 tw[8];
      tw_load(tw, smem + P1_TW + lane * 16);
#pragma unroll
      for (int ks = 0; ks < 3; ++ks) {
        const char* a0 = ks < 2 ? xa + ks * P1_RS + (4 * hh + q) * 64 : xa + (q >> 1) * P1_RS + (16 + (q & 1)) * 64;
        const u32x4 a = tr_frag(a0, ks < 2 ? a0 + 512 : a0);
        re = mfma32<true>(a, *(const u32x4*)(cf + (2 * ks) * 1024), re);
        im = mfma32<true>(a, *(const u32x4*)(cf + (2 * ks + 1) * 1024), im);
      }
      tw_apply(re, im, tw);
      f32x16 xr_ = {}, xi_ = {};
#pragma unroll
      for (int u = 0; u < 2; ++u) cm_dB(xr_, xi_, pack8_f16(re, u), pack8_f16(im, u), ef_load(smem + P1_E + u * 3072 + lane * 16));
      const int col = lane & 31, k1 = col & 15, row2 = 2 * (2 * wave + (col >> 4));
      if (k1 < 10) {
        char* o1 = smem + P1_O + 32 * k1 + 576 * 4 * hh + row2;
        char* o2 = smem + P1_O + 32 * (576 - k1) - 576 * (4 * hh + 27) + row2;
#pragma unroll
        for (int rho = 0; rho < 8; ++rho) {
          const unsigned pk = pack_f16(xr_[rho] * (1.0f / 32.0f), xi_[rho] * (1.0f / 32.0f));
          char* d = o1 + 576 * ((rho & 3) + 8 * (rho >> 2));
          *(u16*)d = (u16)pk;
          if (rho != 0 || k1 != 0 || hh != 0) *(u16*)(d + 16) = (u16)(pk >> 16);
        }
        if (k1 >= 1 && k1 <= 8) {
#pragma unroll
          for (int rho = 8; rho < 16; ++rho) {
            const unsigned pk = pack_f16(xr_[rho] * (1.0f / 32.0f), xi_[rho] * (-1.0f / 32.0f));
            char* d = o2 + 576 * (27 - ((rho & 3) + 8 * (rho >> 2)));
            *(u16*)d = (u16)pk;
            *(u16*)(d + 16) = (u16)(pk >> 16);
          }
        } else if (k1 == 0 && hh == 0) {
          *(u16*)(smem + P1_O + 16 + row2) = (u16)pack_f16(xr_[8] * (1.0f / 32.0f), 0.f);
        }
      }
    }
    WAIT_LGKM0();
    lds_arrive_wait(smem + P1_RED + 32, 4u, lane);
    gate();
    const unsigned P = ((s2 & 1) * 2 + t) * 64 + h * 32 + (s2 >> 1);
#pragma unroll
    for (int it = 0; it < 3; ++it) {
      const int p = tid + it * 256;
      if (p < 576) store_dev(ws, OFF_X1 + P * 9216u + p * 16, *(const u32x4*)(smem + P1_O + p * 16));
    }
    WAIT_VM(0);
    if (lane == 0 && lds_arrive(smem + P1_RED + 36) == 7u) set_flag(ws, SY_A + b * 8, tag);
  } else if (wave < 8) {
    const int tid = threadIdx.x - 256;
    const int rg = b >> 2, cg = b & 3, c0 = cg * 72 + (cg > 0), nc = cg == 0 ? 73 : 72, kq = rg >> 2, ko = rg & 3;
    unsigned rv[10], iv[10];
#pragma unroll
    for (int it = 0; it < 10; ++it) {
      const int i = tid + it * 256, row = i / 73, col = i - row * 73;
      const bool ok = i < 32 * 73 && col < nc;
      const int off = ok ? (8 * ko + (row & 7) + 32 * (4 * kq + (row >> 3))) * NC_ + c0 + col : 0;
      rv[it] = wr[off]; iv[it] = wi[off];
    }
    BAR();
    { const u64 tv = *(const u64*)(smem + P1_RED + 24);
      tag = (u64)__builtin_amdgcn_readfirstlane((unsigned)(tv >> 32)) << 32 | (unsigned)__builtin_amdgcn_readfirstlane((unsigned)tv); }
    unsigned mx = 0;
#pragma unroll
    for (int it = 0; it < 10; ++it) {
      const int i = tid + it * 256, row = i / 73, col = i - row * 73;
      if (i < 32 * 73 && col < nc) {
        mx = max(mx, max(rv[it] & 0x7fffu, iv[it] & 0x7fffu));
        *(unsigned*)(smem + P1_WT + col * 144 + (row & 7) * 16 + (row >> 3) * 4) = rv[it] | (iv[it] << 16);
      }
    }
#pragma unroll
    for (int o = 32; o; o >>= 1) mx = max(mx, (unsigned)__shfl_xor((int)mx, o));
    unsigned* red = (unsigned*)(smem + P1_RED);
    if (lane == 0) red[wave - 4] = mx;
    WAIT_LGKM0();
    lds_arrive_wait(smem + P1_RED + 44, 4u, lane);
    if (tid == 0) __hip_atomic_store((gu32*)(ws + OFF_WMAX + b * 4), max(max(red[0], red[1]), max(red[2], red[3])), RLX_AGENT);
    const int U = kq >> 3, h = kq & 1, rq = (kq >> 1) & 3;
    gate();
#pragma unroll
    for (int it = 0; it < 3; ++it) {
      const int p = tid + it * 256;
      if (p < nc * 8) {
        const int col = p >> 3, a = p & 7;
        store_dev(ws, OFF_WT + (unsigned)(c0 + col) * 8192u + ((U * 4 + rq) * 64 + h * 32 + 8 * ko + a) * 16,
                  *(const u32x4*)(smem + P1_WT + col * 144 + a * 16));
      }
    }
    WAIT_VM(0);
    if (lane == 0 && lds_arrive(smem + P1_RED + 36) == 7u) set_flag(ws, SY_A + b * 8, tag);
    if (idx < 27) { issue_w1x(wave - 4, 0, 0); issue_w1x(wave - 4, 1, CG1::STAGE); issue_w1x(wave - 4, 2, 2 * CG1::STAGE); }
  } else {
    u64 ep = 0;
    if (threadIdx.x == 512) ep = __hip_atomic_load((gu64*)(ws + SY_EPOCH), RLX_AGENT);
    if (wave != 10) {
      const u32x4 rt = mk_rsrc(&T_FWD, 20480u);
      for (int fr = wave == 11 ? 2 : wave - 8; fr < 20; fr += 3) dma16<0>(rt, ldsb + P1_C1 + fr * 1024, lane * 16, fr * 1024u);
    } else {
      __builtin_amdgcn_s_sleep(32);
#pragma unroll
      for (int i = 0; i < 5; ++i)
        dma4(rs_w1, ldsb + P1_DUMMY + wave * 256, w1_line((i * 32 + idx) * 64 + lane, PF1_ROWS * nl1, 0, nl1, ft0 * 256u));
    }
    if (threadIdx.x == 512) {
      *(u64*)(smem + P1_RED + 24) = ep * 6364136223846793005ull + 1442695040888963407ull;
      *(unsigned*)(smem + P1_RED + 16) = 0u; *(u64*)(smem + P1_RED + 32) = 0ull; *(u64*)(smem + P1_RED + 40) = 0ull;
    }
    if (wave != 10) WAIT_VM(0);
    WAIT_LGKM0();
    BAR();
    { const u64 tv = *(const u64*)(smem + P1_RED + 24);
      tag = (u64)__builtin_amdgcn_readfirstlane((unsigned)(tv >> 32)) << 32 | (unsigned)__builtin_amdgcn_readfirstlane((unsigned)tv); }
    if (wave != 10) {
      const int s2 = tb >> 2, t = (tb >> 1) & 1, h = tb & 1;
      for (int cb = wave == 11 ? 2 : wave - 8; cb < 9; cb += 3) {
        const int gg = lane >> 4, q = (lane & 15) >> 2, pp = lane & 3;
        const char* src = smem + P1_HS + q * P1_RS + (cb * 64 + gg * 16 + pp * 4) * 2;
        const int df = cb * 2 + (gg >> 1), i = 8 * q + 4 * (gg & 1) + pp;
        store_dev(ws, OFF_H + (unsigned)(df * 128 + 2 * s2 + t) * 1024u + (h * 32 + i) * 16, tr_frag(src, src + 4 * P1_RS));
      }
      WAIT_VM(0);
      unsigned last = 0;
      if (lane == 0) last = lds_arrive(smem + P1_RED + 16);
      if (__builtin_amdgcn_readfirstlane(last) == 2u) { if (lane < 8) set_flag(ws, SY_H + lane * 2048 + tb * 8, tag); }
    }
  }
  if (idx < 27) {
    char* ebuf = smem + (29 % 5) * CG1::STAGE;
    u32x2 bv[3][2];
#pragma unroll
    for (int it = 0; it < 3; ++it) {
      const u32x4* bp = (const u32x4*)(b1 + ft * 128 + (min(wave + 11 * it, 23) / 3) * 16);
      const u32x4 b0 = bp[0], b1v = bp[1];
      const bool hi = lane >= 32;
      bv[it][0] = u32x2{hi ? b0[2] : b0[0], hi ? b0[3] : b0[1]}; bv[it][1] = u32x2{hi ? b1v[2] : b1v[0], hi ? b1v[3] : b1v[1]};
      asm volatile("" : "+v"(bv[it][0]), "+v"(bv[it][1]));
    }
    if (wave == 11) {
      const unsigned fh = SY_H + g * 2048 + lane * 8;
      poll_flags<1>(ws, fh, true, tag);
      if (lane == 0) lds_arrive(smem + P1_RED + 40);
      bool ok[3];
#pragma unroll
      for (int q = 0; q < 3; ++q) ok[q] = __all(__hip_atomic_load((gu64*)(ws + fh + (q + 1) * 512), RLX_AGENT) == tag);
      for (int tau = 0; tau < 32; ++tau) {
#pragma unroll
        for (int q = 0; q < 3; ++q)
          if (tau == 8 * q + 5 && !ok[q]) poll_flags<1>(ws, fh + (q + 1) * 512, true, tag);
        if (tau == 21 && b == 0 && lane == 0) set_flag(ws, SY_EPOCH, tag);
        BAR();
        if (tau == 29) {
#pragma unroll
          for (int i = 0; i < PF2T; ++i) dma4(rs_w2, ldsb + P1_DUMMY + wave * 256, w2_line((i * 27 + idx) * 64 + lane, g));
        }
      }
    } else if (wave >= 8) {
      auto issue_h = [&](int kt, int so) {
#pragma unroll
        for (int m = 0; m < 4; ++m)
          dma16<2>(rs_ws, ldsb + so + CG1::A_BYTES + ((wave - 8) * 4 + m) * 1024, lane * 16,
                   OFF_H + (unsigned)((dt1 * 3 + (wave - 8)) * 128 + kt * 4 + m) * 1024u);
      };
      lds_wait(smem + P1_RED + 40);
      issue_h(0, 0); issue_h(1, CG1::STAGE); issue_h(2, 2 * CG1::STAGE);
      CG1::ring<4>(32, issue_h);
    } else if (wave >= 4) {
      CG1::ring<4>(32, [&](int kt, int so) { issue_w1x(wave - 4, kt, so); });
    } else {
      f32x16 acc[1][3] = {};
      int rslot = 0;
      CG1::consume<false>(smem, wave, lane, 32, rslot, acc);
#pragma unroll
      for (int j = 0; j < 3; ++j)
#pragma unroll
        for (int s = 0; s < 2; ++s) {
          u32x4 val;
#pragma unroll
          for (int e = 0; e < 4; ++e) val[e] = pack_bf16(acc[0][j][8 * s + 2 * e], acc[0][j][8 * s + 2 * e + 1]);
          *(u32x4*)(ebuf + ((wave * 2 + s) * 3 + j) * 1024 + lane * 16) = val;
        }
    }
    WAIT_LGKM0();
    BAR();
    if (wave < 11) {
#pragma unroll
      for (int it = 0; it < 3; ++it) {
        const int i = wave + 11 * it;
        if (i < 24) {
          const int z = i / 3, j = i - 3 * z;
          const u32x4 a = *(const u32x4*)(ebuf + i * 1024 + lane * 16);
          u32x4 val;
#pragma unroll
          for (int e = 0; e < 4; ++e) {
            const unsigned bb = bv[it][e >> 1][e & 1];
            const unsigned pk = pack_bf16(lo16(a[e]) + lo16(bb), hi16(a[e]) + hi16(bb));
            val[e] = pack_bf16(gelu_tanh(lo16(pk)), gelu_tanh(hi16(pk)));
          }
          store_dev(ws, OFF_U + (unsigned)((dt1 * 3 + j) * 288 + ft * 8 + z) * 1024u + lane * 16, val);
        }
      }
      WAIT_VM(0);
    } else if (lane == 0) *(unsigned*)(smem + 163832) = 0u;
    WAIT_LGKM0();
    BAR();
    if (threadIdx.x < 2) set_flag(ws, SY_U + threadIdx.x * 2048 + tile * 8, tag);
  } else {
    const int me = (idx - 27) * 8 + g;
    {
      const int total = (2048 - PF1_ROWS) * nl1;
      for (int i0 = ((idx - 27) * 12 + wave) * 64; i0 < total; i0 += 60 * 64) {
        dma4(rs_w1, ldsb + P1_DUMMY + wave * 256, w1_line(i0 + lane, total, PF1_ROWS, nl1, ft0 * 256u));
        WAIT_VM(7);
      }
    }
    if (me < 37) {
      if (wave == 8) poll_flags<4>(ws, SY_A + lane * 8, true, tag);
      BAR();
      const int cbk = me == 36 ? 0 : me;
      char* img = smem + 65536;
      for (int i = wave; i < 64; i += 12) {
        const int P = 4 * i + (lane >> 4);
        dma16<2>(rs_ws, ldsb + 65536 + i * 1024, (unsigned)P * 9216u + (unsigned)(((lane & 15) ^ (P & 15)) * 16), OFF_X1 + cbk * 256u);
      }
      for (int i = wave; i < TAB_BYTES / 1024; i += 12) dma16<0>(mk_rsrc(&T_COL, TAB_BYTES), ldsb + i * 1024, lane * 16, i * 1024u);
      const bool act = me == 36 ? wave == 0 : wave < 8;
      const int c = me == 36 ? 288 : me * 8 + wave;
      const float wsc = __uint_as_float((253u - wmax_exp(ws, lane)) << 23) * (1.0f / 64.0f);
      WAIT_VM(0);
      BAR();
      const int slot_re = me == 36 ? 1 : 2 * wave, slot_im = c == 288 ? -2 : (c == 0 ? -1 : 2 * wave + 1);
      u32x4 zr[2][2], zi[2][2];
      if (act) {
#pragma unroll
        for (int z = 0; z < 4; ++z) {
          const char* p = img + (z * 64 + lane) * 256;
          zr[z >> 1][z & 1] = *(const u32x4*)(p + ((slot_re ^ (lane & 15)) * 16));
          zi[z >> 1][z & 1] = slot_im >= 0 ? *(const u32x4*)(p + ((slot_im ^ (lane & 15)) * 16)) : u32x4{0, 0, 0, 0};
        }
      }
      WAIT_LGKM0();
      BAR();
      if (act) colfft_wave(smem + lane * 16, ws, c, lane, wsc, zr, zi, img, slot_re, slot_im);
      else if (wave >= 8 && idx < 31) {
        __builtin_amdgcn_s_sleep(64);
        for (int i0 = ((idx - 27) * 4 + wave - 8) * 64; i0 < PF2C_ROWS * 16; i0 += 16 * 64) {
          dma4(rs_w2, ldsb + P1_DUMMY + wave * 256, w2_line(PF2T * 27 * 64 + i0 + lane, g));
          WAIT_VM(7);
        }
      }
      WAIT_LGKM0();
      BAR();
      if (me == 36) {
        if (wave < 4) store_dev(ws, OFF_Y288 + wave * 1024 + lane * 16, *(const u32x4*)(img + wave * 1024 + lane * 16));
      } else {
        for (int i = wave; i < 64; i += 12) {
          const int Qy = 4 * i + (lane >> 4);
          store_dev(ws, OFF_Y1 + (unsigned)Qy * 9216u + cbk * 256 + (lane & 15) * 16,
                    *(const u32x4*)(img + Qy * 256 + (((lane & 15) ^ ((Qy >> 3) & 15)) * 16)));
        }
      }
      WAIT_VM(0);
      BAR();
      if (threadIdx.x == 0) set_flag(ws, SY_Y + me * 8, tag);
    }
  }
  if (idx < 24) {
    const int ks = g >> 1, dt = idx % 3, st = (g & 1) * 8 + idx / 3;
    const int rot = (g & 1) * 9;
    const unsigned myflags = SY_T + (unsigned)((st * 4 + ks) * 3 + dt) * 64u;
    if (wave >= 8) {
      auto issue_u = [&](int k0, int so) {
        const int kt = k0 + rot < 18 ? k0 + rot : k0 + rot - 18;
        if (k0 == 0 || k0 == 8) {
          const int kf = k0 == 0 ? rot : 9 - rot;
          if (wave == 8) {
            poll_flags<1>(ws, SY_U + (g & 1) * 2048 + (unsigned)((9 * ks + (kf >> 1) + (lane >> 1)) * 6 + 2 * dt + (lane & 1)) * 8u, lane < 10, tag);
            if (k0 == 0) { if (lane == 0) lds_arrive(smem + 163832); }
          } else if (k0 == 0) lds_wait(smem + 163832);
        }
#pragma unroll
        for (int m = 0; m < 6; ++m) {
          const int a = (wave - 8) * 6 + m;
          dma16<2>(rs_ws, ldsb + so + a * 1024, lane * 16, OFF_U + (unsigned)((dt * 6 + (a >> 2)) * 288 + (ks * 18 + kt) * 4 + (a & 3)) * 1024u);
        }
      };
      issue_u(0, 0); issue_u(1, CG2::STAGE);
      CG2::ring<6>(18, issue_u);
      BAR();
      if (wave == 8) poll_flags<1>(ws, myflags + (lane & 3) * 8, true, tag);
      BAR();
      return;
    }
    if (wave >= 4) {
      auto issue_w2 = [&](int k0, int so) {
        const int kt = k0 + rot < 18 ? k0 + rot : k0 + rot - 18;
        constexpr unsigned ld = S_ * 2;
        const unsigned vo = CG2::voff<TR128>(lane, ld);
#pragma unroll
        for (int m = 0; m < 4; ++m) {
          const int a = (wave - 4) * 4 + m;
          const unsigned sof = (unsigned)((ks * 18 + kt) * 64 + 4 * a) * ld + st * 256u;
          if (k0 >= NTF2) dma16<1>(rs_w2, ldsb + so + CG2::A_BYTES + a * 1024, vo, sof);
          else dma16<0>(rs_w2, ldsb + so + CG2::A_BYTES + a * 1024, vo, sof);
        }
      };
      issue_w2(0, 0); issue_w2(1, CG2::STAGE);
      CG2::ring<4>(18, issue_w2);
      BAR();
      BAR();
      return;
    }
    f32x16 acc[3][2] = {};
    int rslot = 0;
    CG2::consume<false>(smem, wave, lane, 18, rslot, acc);
    const int wa = wave >> 1, wb = wave & 1;
    const bool own0 = wb * 2 == ks, own1 = wb * 2 + 1 == ks;
#pragma unroll
    for (int j = 0; j < 2; ++j) {
      if (j ? own1 : own0) continue;
#pragma unroll
      for (int i = 0; i < 3; ++i) {
        const unsigned o = OFF_SLAB + (unsigned)((ks * 64 + st * 4 + wb * 2 + j) * 18 + dt * 6 + wa * 3 + i) * 2048u + lane * 16;
#pragma unroll
        for (int u = 0; u < 2; ++u) {
          u32x4 val;
#pragma unroll
          for (int e = 0; e < 4; ++e) val[e] = pack_bf16(acc[i][j][8 * u + 2 * e], acc[i][j][8 * u + 2 * e + 1]);
          store_dev(ws, o + u * 1024, val);
        }
      }
    }
    WAIT_VM(0);
    BAR();
    if (wave == 0 && lane < 4 && lane != ks) set_flag(ws, SY_T + (unsigned)((st * 4 + lane) * 3 + dt) * 64u + ks * 8, tag);
    BAR();
    if (!own0 && !own1) return;
    const int sf = st * 4 + ks, s = sf * 32 + (lane & 31);
    auto finish = [&](auto J) {
      constexpr int j = decltype(J)::value;
      u32x4 pv[3][2][4];
#pragma unroll
      for (int i = 0; i < 3; ++i)
#pragma unroll
        for (int u = 0; u < 2; ++u) {
          const unsigned o = (unsigned)(sf * 18 + dt * 6 + wa * 3 + i) * 2048u + u * 1024 + lane * 16;
#pragma unroll
          for (int q = 0; q < 3; ++q) pv[i][u][q] = load_dev(ws, OFF_SLAB + o + ((ks + 1 + q) & 3) * 2359296u);
          pv[i][u][3] = load_dev(ws, OFF_R + o);
        }
#pragma unroll
      for (int i = 0; i < 3; ++i)
#pragma unroll
        for (int u = 0; u < 2; ++u) {
          u32x4 o;
#pragma unroll
          for (int e = 0; e < 4; ++e)
            o[e] = pack_bf16(acc[i][j][8 * u + 2 * e] + (lo16(pv[i][u][0][e]) + lo16(pv[i][u][1][e])) + (lo16(pv[i][u][2][e]) + lo16(pv[i][u][3][e])),
                             acc[i][j][8 * u + 2 * e + 1] + (hi16(pv[i][u][0][e]) + hi16(pv[i][u][1][e])) + (hi16(pv[i][u][2][e]) + hi16(pv[i][u][3][e])));
          *(u32x4*)(out + (size_t)s * D_ + (dt * 6 + wa * 3 + i) * 32 + (lane >> 5) * 16 + u * 8) = o;
        }
    };
    if (own0) finish(IC<0>{}); else finish(IC<1>{});
  } else {
    const int me = (idx - 24) * 8 + g, sr = lane & 31;
    char* kt = smem + 37888; char* yb = kt + 17408;
    u32x4 xv[3];
#pragma unroll
    for (int it = 0; it < 3; ++it) xv[it] = *(const u32x4*)(x + (size_t)(me * 32 + sr) * D_ + ((it * 12 + wave) * 2 + (lane >> 5)) * 8);
    const float bias = lo16(b2[me * 32 + sr]);
    BAR();
    for (int i = wave; i < 16; i += 12) dma16<0>(mk_rsrc(&T_INV, 16384u), ldsb + 37888 + i * 1024, lane * 16, i * 1024u);
    if (wave == 8) { poll_flags<1, 16>(ws, SY_Y + min(lane, 36) * 8, true, tag); }
    BAR();
#pragma unroll
    for (int it = 0; it < 3; ++it) dma16<2>(rs_ws, ldsb + (it * 12 + wave) * 1024, lane * 16, OFF_Y1 + me * 36864u + (it * 12 + wave) * 1024u);
    if (wave == 4) dma16<2>(rs_ws, ldsb + 37888 + 16384, (lane & 3) * 16, OFF_Y288 + me * 64u);
    const float fs = __uint_as_float((wmax_exp(ws, lane) + 1u) << 23) * (1.0f / 9.0f);
    WAIT_VM(0);
    BAR();
    for (int task = wave; task < 16; task += 12)
      irfft_wave(smem + (task >> 2) * 9216, kt + 16384 + (task >> 2) * 16, task & 3, lane, yb + task * 2 * FIN_OS, kt + lane * 16);
    WAIT_LGKM0();
    BAR();
#pragma unroll
    for (int it = 0; it < 3; ++it) {
      const int dp = (it * 12 + wave) * 2 + (lane >> 5);
      const char* yp = yb + sr * FIN_OS + dp * 32;
      const f32x4 y0 = *(const f32x4*)yp, y1 = *(const f32x4*)(yp + 16);
      u32x4 o;
#pragma unroll
      for (int e = 0; e < 4; ++e) {
        const float ya = e < 2 ? y0[2 * e] : y1[2 * e - 4], yc = e < 2 ? y0[2 * e + 1] : y1[2 * e - 3];
        o[e] = pack_bf16(lo16(xv[it][e]) + bias + ya * fs, hi16(xv[it][e]) + bias + yc * fs);
      }
      store_dev(ws, OFF_R + (unsigned)(me * 18 + (dp >> 2)) * 2048u + (dp & 1) * 1024 + (((dp >> 1) & 1) * 32 + sr) * 16, o);
    }
    WAIT_VM(0);
    BAR();
    if (wave == 0 && lane < 3) set_flag(ws, SY_T + (unsigned)(me * 3 + lane) * 64u + (me & 3) * 8, tag);
  }
}

extern "C" void kernel_launch(void* const* d_in, const int* in_sizes, int n_in, void* d_out, int out_size, void* d_ws, size_t ws_size,
                              hipStream_t stream) {
  (void)hipFuncSetAttribute((const void*)k_all, hipFuncAttributeMaxDynamicSharedMemorySize, 163840);
  k_all<<<256, 768, 163840, stream>>>((const u16*)d_in[0], (const u16*)d_in[1], (const u16*)d_in[2], (const char*)d_in[4], (const u16*)d_in[5],
                                      (const char*)d_in[6], (const u16*)d_in[7], (char*)d_ws, (u16*)d_out);
}
```

```cpp
#include <hip/hip_runtime.h>
#include <hip/hip_bf16.h>
#include <stdint.h>

#define DI __device__ __forceinline__
#define LAS __attribute__((address_space(3)))
typedef unsigned short u16;
typedef unsigned long long u64;
typedef __attribute__((ext_vector_type(2))) float f32x2;
typedef __attribute__((ext_vector_type(4))) float f32x4;
typedef __attribute__((ext_vector_type(16))) float f32x16;
typedef __attribute__((ext_vector_type(2))) _Float16 f16x2;
typedef __attribute__((ext_vector_type(8))) _Float16 f16x8;
typedef __attribute__((ext_vector_type(2))) __bf16 bf16x2;
typedef __attribute__((ext_vector_type(8))) __bf16 bf16x8;
typedef __attribute__((ext_vector_type(4))) short s16x4;
typedef __attribute__((ext_vector_type(2))) unsigned u32x2;
typedef __attribute__((ext_vector_type(4))) unsigned u32x4;
typedef __attribute__((address_space(1))) unsigned gu32;
typedef __attribute__((address_space(1))) u64 gu64;
#define RLX_AGENT __ATOMIC_RELAXED, __HIP_MEMORY_SCOPE_AGENT

constexpr int PF1_ROWS = (160 * 64) / 10;
constexpr int PF2T = 2;
constexpr int PF2C_ROWS = 360;
constexpr int NTF2 = 9;
constexpr unsigned CODE_WARM = 38912;
constexpr int POLL_SLEEP = 4;

constexpr int S_ = 2048, D_ = 576, F_ = 4608, NC_ = 289;

constexpr unsigned OFF_H    = 0;
constexpr unsigned OFF_WT   = OFF_H + 2359296;
constexpr unsigned OFF_Y288 = OFF_WT + 2367488;
constexpr unsigned OFF_WMAX = OFF_Y288 + 4096;
constexpr unsigned OFF_X1   = OFF_WMAX + 4096;
constexpr unsigned OFF_Y1   = OFF_X1 + 2359296;
constexpr unsigned OFF_U    = OFF_Y1 + 2359296;
constexpr unsigned OFF_SLAB = OFF_U + 5308416;
constexpr unsigned OFF_R    = OFF_SLAB + 9437184;
constexpr unsigned OFF_SYNC = OFF_R + 2359296;
constexpr unsigned SY_EPOCH = OFF_SYNC, SY_TMO = OFF_SYNC + 256, SY_A = OFF_SYNC + 2048, SY_Y = OFF_SYNC + 4096, SY_U = OFF_SYNC + 6144,
                   SY_H = SY_U + 2 * 2048, SY_T = SY_H + 8 * 2048;
constexpr unsigned WS_END   = SY_T + 192 * 64;
constexpr int TAB_E1 = 0, TAB_EO = 6144, TAB_EOI = 12288, TAB_TW1 = 18432, TAB_TW2 = 34816, TAB_BYTES = 51200;

constexpr double cx_sincos(int n, int N, bool want_sin) {
  n %= N; if (n < 0) n += N;
  int m = 8 * n, oct = m / N; m -= oct * N;
  bool s = want_sin; double sign = 1.0;
  if (oct & 1) m = N - m;
  const int q = ((oct + 1) >> 1) & 3;
  double sg = (oct & 1) ? -1.0 : 1.0;
  bool use_sin = false;
  if (s) { use_sin = (q & 1) == 0; sign = (q == 2 || q == 3) ? -1.0 : 1.0; }
  else { use_sin = (q & 1) == 1; sign = (q == 1 || q == 2) ? -1.0 : 1.0; }
  const double r = 0.78539816339744830962 * (double)m / (double)N, r2 = r * r;
  double t = use_sin ? r : 1.0, sum = t;
  for (int k = use_sin ? 2 : 1; k < 22; k += 2) { t *= -r2 / (double)(k * (k + 1)); sum += t; }
  return sign * (use_sin ? sg * sum : sum);
}
constexpr u16 cx_half(double v) {
  const u16 sg = v < 0 ? 0x8000 : 0; if (v < 0) v = -v;
  if (v < 2.9802322387695312e-08) return sg;
  int e = 1; double p = 2.0;
  while (v < p && e > -14) { p *= 0.5; --e; }
  const double x = v / p * 1024.0;
  int i = (int)x; const double fr = x - i;
  if (fr > 0.5 || (fr == 0.5 && (i & 1))) ++i;
  return (u16)(sg | (v < p ? i : ((e + 15) << 10) + (i - 1024)));
}
struct CxBase { double c576[576], s576[576]; float c2k[2048], s2k[2048]; };
constexpr CxBase cx_base() {
  CxBase b{};
  for (int i = 0; i < 576; ++i) { b.c576[i] = cx_sincos(i, 576, false); b.s576[i] = cx_sincos(i, 576, true); }
  for (int i = 0; i < 2048; ++i) { b.c2k[i] = (float)cx_sincos(i, 2048, false); b.s2k[i] = (float)cx_sincos(i, 2048, true); }
  return b;
}
constexpr CxBase CXB = cx_base();
constexpr double cx_cs(int which, int n576) { return which == 0 ? CXB.c576[n576 % 576] : (which == 1 ? -CXB.s576[n576 % 576] : CXB.s576[n576 % 576]); }
constexpr int cx_kap(int t, int h, int j) { return 16 * t + 8 * (j >> 2) + 4 * h + (j & 3); }
constexpr int cx_row(int rho, int h) { return (rho & 3) + 8 * (rho >> 2) + 4 * h; }
template <int NH, int NF> struct alignas(1024) CxTab { u16 h[NH * 512]; float f[NF * 256]; };
template <class T> constexpr void cx_e32(T& o, int base) {
  for (int fr = 0; fr < 6; ++fr) for (int l = 0; l < 64; ++l) for (int j = 0; j < 8; ++j)
    o.h[((base + fr) * 64 + l) * 8 + j] = cx_half(cx_cs(fr % 3, 18 * ((cx_kap(fr / 3, l >> 5, j) * (l & 31)) & 31)));
}
constexpr CxTab<12, 8> cx_fwd() {
  CxTab<12, 8> o{};
  for (int fr = 0; fr < 6; ++fr) for (int l = 0; l < 64; ++l) for (int j = 0; j < 8; ++j) {
    const int ks = fr >> 1, hh = l >> 5, r = (l & 31) >> 4, k1 = l & 15;
    const int row = ks < 2 ? ks : ((hh == 0 && j < 4) ? (j >> 1) : 2), d1 = ks < 2 ? cx_kap(0, hh, j) : 16 + (j & 1);
    o.h[(fr * 64 + l) * 8 + j] = (row == r && k1 < 10) ? cx_half(cx_cs(fr & 1, 32 * ((d1 * k1) % 18))) : (u16)0;
  }
  cx_e32(o, 6);
  for (int p = 0; p < 8; ++p) for (int l = 0; l < 64; ++l) for (int y = 0; y < 2; ++y) {
    const int n = cx_row(2 * p + y, l >> 5) * (l & 15);
    o.f[(p * 64 + l) * 4 + y] = (float)CXB.c576[n]; o.f[(p * 64 + l) * 4 + 2 + y] = (float)-CXB.s576[n];
  }
  return o;
}
constexpr CxTab<18, 32> cx_col() {
  CxTab<18, 32> o{};
  cx_e32(o, 0);
  for (int fr = 0; fr < 12; ++fr) for (int l = 0; l < 64; ++l) for (int j = 0; j < 8; ++j) {
    const int f2 = fr % 6, r = l & 31, kap = cx_kap(f2 / 3, l >> 5, j);
    o.h[((6 + fr) * 64 + l) * 8 + j] = cx_half(cx_cs(f2 % 3, 9 * ((fr >= 6 ? kap * (2 * r + 1) : r * (2 * kap + 1)) & 63)));
  }
  for (int q = 0; q < 32; ++q) for (int l = 0; l < 64; ++l) for (int z = 0; z < 2; ++z) {
    const bool inv = q >= 16;
    const int T = (q >> 3) & 1, r = l & 31, cr = cx_row(2 * (q & 7) + z, l >> 5), n = (inv ? (2 * r + T) * cr : (2 * cr + T) * r) & 2047;
    o.f[(q * 64 + l) * 4 + z] = CXB.c2k[n] * (inv ? 0.125f : 1.0f); o.f[(q * 64 + l) * 4 + 2 + z] = CXB.s2k[n] * (inv ? 0.125f : -1.0f);
  }
  return o;
}
constexpr CxTab<8, 8> cx_inv() {
  CxTab<8, 8> o{};
  for (int fr = 0; fr < 2; ++fr) for (int l = 0; l < 64; ++l) for (int j = 0; j < 8; ++j) {
    const int r = l & 31, k1 = cx_kap(0, l >> 5, j);
    o.h[(fr * 64 + l) * 8 + j] = (k1 < 10 && r < 18) ? cx_half(cx_cs(fr, 32 * ((r * k1) % 18)) * ((k1 == 0 || k1 == 9) ? 1.0 : 2.0)) : (u16)0;
  }
  cx_e32(o, 2);
  for (int p = 0; p < 8; ++p) for (int l = 0; l < 64; ++l) for (int y = 0; y < 2; ++y) {
    const int n = (cx_row(2 * p + y, l >> 5) & 15) * (l & 31);
    o.f[(p * 64 + l) * 4 + y] = (float)(CXB.c576[n] * 0.125); o.f[(p * 64 + l) * 4 + 2 + y] = (float)(CXB.s576[n] * 0.125);
  }
  return o;
}
__device__ const CxTab<12, 8> T_FWD = cx_fwd();
__device__ const CxTab<18, 32> T_COL = cx_col();
__device__ const CxTab<8, 8> T_INV = cx_inv();

DI unsigned pack_bf16(float a, float b) { f32x2 v = {a, b}; return __builtin_bit_cast(unsigned, __builtin_convertvector(v, bf16x2)); }
DI unsigned pack_f16(float a, float b) { f32x2 v = {a, b}; return __builtin_bit_cast(unsigned, __builtin_convertvector(v, f16x2)); }
DI float lo16(unsigned p) { return __uint_as_float(p << 16); }
DI float hi16(unsigned p) { return __uint_as_float(p & 0xffff0000u); }
DI float rbf(float a) { return lo16(pack_bf16(a, a)); }

template <int CTRL> DI float dpp_add(float v) {
  return v + __int_as_float(__builtin_amdgcn_update_dpp(0, __float_as_int(v), CTRL, 0xf, 0xf, true));
}
DI float sum32(float v) {
  v = dpp_add<0xB1>(v); v = dpp_add<0x4E>(v); v = dpp_add<0x141>(v); v = dpp_add<0x140>(v);
  auto r = __builtin_amdgcn_permlane16_swap(__float_as_uint(v), __float_as_uint(v), false, false);
  return __uint_as_float(r[0]) + __uint_as_float(r[1]);
}

#define WAIT_VM(n) asm volatile("s_waitcnt vmcnt(%0)" ::"n"(n) : "memory")
#define WAIT_LGKM0() asm volatile("s_waitcnt lgkmcnt(0)" ::: "memory")
#define BAR() do { asm volatile("" ::: "memory"); __builtin_amdgcn_s_barrier(); asm volatile("" ::: "memory"); } while (0)
#define SCHED0() __builtin_amdgcn_sched_barrier(0)

DI u32x4 mk_rsrc(const void* p, unsigned bytes) {
  const u64 a = (u64)p;
  return u32x4{(unsigned)a, (unsigned)(a >> 32) & 0xffffu, bytes, 0x00020000u};
}
template <int POL> DI void dma16(u32x4 rsrc, unsigned lds_off, unsigned voff, unsigned soff) {
  if constexpr (POL == 0) asm volatile("s_mov_b32 m0, %0\n\ts_nop 0\n\tbuffer_load_dwordx4 %1, %2, %3 offen lds" ::"s"(lds_off), "v"(voff), "s"(rsrc), "s"(soff) : "memory");
  if constexpr (POL == 1) asm volatile("s_mov_b32 m0, %0\n\ts_nop 0\n\tbuffer_load_dwordx4 %1, %2, %3 offen nt lds" ::"s"(lds_off), "v"(voff), "s"(rsrc), "s"(soff) : "memory");
  if constexpr (POL == 2) asm volatile("s_mov_b32 m0, %0\n\ts_nop 0\n\tbuffer_load_dwordx4 %1, %2, %3 offen sc1 lds" ::"s"(lds_off), "v"(voff), "s"(rsrc), "s"(soff) : "memory");
}
DI void dma4(u32x4 rsrc, unsigned lds_off, unsigned voff) {
  asm volatile("s_mov_b32 m0, %0\n\ts_nop 0\n\tbuffer_load_dword %1, %2, 0 offen lds" ::"s"(lds_off), "v"(voff), "s"(rsrc) : "memory");
}
DI void store_dev(char* ws, unsigned off, u32x4 v) {
  __builtin_amdgcn_raw_buffer_store_b128(v, __builtin_amdgcn_make_buffer_rsrc((void*)ws, 0, WS_END, 0x00020000), off, 0, 16);
}
DI u32x4 load_dev(const char* ws, unsigned off) {
  return __builtin_amdgcn_raw_buffer_load_b128(__builtin_amdgcn_make_buffer_rsrc((void*)ws, 0, WS_END, 0x00020000), off, 0, 16);
}
DI void set_flag(char* ws, unsigned off, u64 tag) { __hip_atomic_store((gu64*)(ws + off), tag, RLX_AGENT); }
template <int N, int SLP = POLL_SLEEP> DI void poll_flags(char* ws, unsigned off, bool active, u64 tag) {
  for (unsigned spins = 0;; ++spins) {
    bool ok = true;
#pragma unroll
    for (int i = 0; i < N; ++i) { const u64 v = __hip_atomic_load((gu64*)(ws + off + i * 512), RLX_AGENT); ok &= (v == tag) || !active; }
    if (__all(ok)) break;
    __builtin_amdgcn_s_sleep(SLP);
    if (spins > (1u << 17)) { set_flag(ws, SY_TMO, tag); break; }
    if ((spins & 63u) == 63u && __all(__hip_atomic_load((gu64*)(ws + SY_TMO), RLX_AGENT) == tag)) break;
  }
  __builtin_amdgcn_fence(__ATOMIC_ACQUIRE, "wavefront");
}
DI unsigned w1_line(int i, int total, int r0, int nl, unsigned c0) {
  const int rr = i / nl, l = i - rr * nl, row = r0 + rr;
  return i < total ? (unsigned)(64 * (row & 31) + (row >> 5)) * (F_ * 2) + c0 + (unsigned)l * 128u : 0xfffffff0u;
}

DI unsigned w2_line(int line, int g) {
  int row = (line >> 4) + (g & 1) * 576; if (row >= 1152) row -= 1152;
  return (unsigned)((g >> 1) * 1152 + row) * 4096u + (g & 1) * 2048u + (line & 15) * 128u;
}

template <bool F16> DI f32x16 mfma32(u32x4 a, u32x4 b, f32x16 c) {
  if constexpr (F16) return __builtin_amdgcn_mfma_f32_32x32x16_f16(__builtin_bit_cast(f16x8, a), __builtin_bit_cast(f16x8, b), c, 0, 0, 0);
  else return __builtin_amdgcn_mfma_f32_32x32x16_bf16(__builtin_bit_cast(bf16x8, a), __builtin_bit_cast(bf16x8, b), c, 0, 0, 0);
}
DI u32x4 pack8_f16(const f32x16& v, int u) {
  return u32x4{pack_f16(v[8 * u], v[8 * u + 1]), pack_f16(v[8 * u + 2], v[8 * u + 3]), pack_f16(v[8 * u + 4], v[8 * u + 5]), pack_f16(v[8 * u + 6], v[8 * u + 7])};
}
DI u32x4 tr_frag(const char* p0, const char* p1) {
  s16x4 lo = __builtin_amdgcn_ds_read_tr16_b64_v4i16((LAS s16x4*)p0);
  s16x4 hi = __builtin_amdgcn_ds_read_tr16_b64_v4i16((LAS s16x4*)p1);
  u32x2 a = __builtin_bit_cast(u32x2, lo), c = __builtin_bit_cast(u32x2, hi);
  return u32x4{a[0], a[1], c[0], c[1]};
}
DI void tw_piece(f32x16& re, f32x16& im, int p, const f32x4 w) {
  const f32x2 r = {re[2 * p], re[2 * p + 1]}, i = {im[2 * p], im[2 * p + 1]}, wr = {w[0], w[1]}, wi = {w[2], w[3]};
  const f32x2 a = __builtin_elementwise_fma(r, wr, -(i * wi)), b = __builtin_elementwise_fma(r, wi, i * wr);
  re[2 * p] = a[0]; re[2 * p + 1] = a[1]; im[2 * p] = b[0]; im[2 * p + 1] = b[1];
}
DI void tw_load(f32x4 (&t)[8], const char* tw) {
#pragma unroll
  for (int p = 0; p < 8; ++p) t[p] = *(const f32x4*)(tw + p * 1024);
}
DI void tw_apply(f32x16& re, f32x16& im, const f32x4 (&t)[8]) {
#pragma unroll
  for (int p = 0; p < 8; ++p) tw_piece(re, im, p, t[p]);
}
enum { LIN = 0, TR128 = 1, TR64 = 2 };

template <int V> struct IC { static constexpr int value = V; };
template <int NA_W, int NB_W, int WA, int WB, int AK, int BK, int NS, int DPM, int DEPTH>
struct GC {
  static constexpr int NC = WA * WB, NFA = NA_W * WA, NFB = NB_W * WB;
  static constexpr int A_BYTES = NFA * 4096, B_BYTES = NFB * 4096, STAGE = A_BYTES + B_BYTES;
  static constexpr int LDS_BYTES = NS * STAGE;
  static_assert(AK == LIN || NFA == (AK == TR128 ? 4 : 2), "");
  static_assert(BK == LIN || NFB == (BK == TR128 ? 4 : 2), "");

  template <int K> static DI unsigned voff(int lane, unsigned rstride) {
    if constexpr (K == LIN) return lane * 16;
    else if constexpr (K == TR128) return (unsigned)(lane >> 4) * rstride + (unsigned)(((lane & 15) ^ ((lane >> 4) << 2)) * 16);
    else return (unsigned)(lane >> 3) * rstride + (unsigned)(((lane & 7) ^ (((lane >> 4) & 1) << 2)) * 16);
  }
  template <int K, int NF> static DI void frag_addr(unsigned (&addr)[K == LIN ? 1 : NF], int opoff, int wpos, int lane) {
    if constexpr (K == LIN) addr[0] = opoff + wpos * NF * 4096 + lane * 16;
    else {
      const int h = lane >> 5, blk = (lane >> 4) & 1, q = (lane & 15) >> 2, p = lane & 3;
#pragma unroll
      for (int i = 0; i < NF; ++i) {
        const int ch = 4 * (wpos * NF + i) + 2 * blk + (p >> 1);
        if constexpr (K == TR128) addr[i] = opoff + (4 * h + q) * 256 + ((ch ^ (q << 2)) * 16) + 8 * (p & 1);
        else addr[i] = opoff + (4 * h + q) * 128 + ((ch ^ (((q >> 1) & 1) << 2)) * 16) + 8 * (p & 1);
      }
    }
  }
  template <int K, int NF> static DI void rd(u32x4 (&f)[NF], const char* smem, const unsigned (&addr)[K == LIN ? 1 : NF], int sb, int hh) {
#pragma unroll
    for (int i = 0; i < NF; ++i) {
      if constexpr (K == LIN) f[i] = *(const u32x4*)(smem + addr[0] + sb + (i * 4 + hh) * 1024);
      else {
        constexpr int RB = K == TR128 ? 256 : 128;
        const char* p = smem + addr[i] + sb + hh * 16 * RB;
        s16x4 lo = __builtin_amdgcn_ds_read_tr16_b64_v4i16((LAS s16x4*)p);
        s16x4 hi = __builtin_amdgcn_ds_read_tr16_b64_v4i16((LAS s16x4*)(p + 8 * RB));
        u32x2 a = __builtin_bit_cast(u32x2, lo), c = __builtin_bit_cast(u32x2, hi);
        f[i] = u32x4{a[0], a[1], c[0], c[1]};
      }
    }
  }
  static DI void ilv() {
    constexpr int NR = NA_W * (AK == LIN ? 1 : 2) + NB_W * (BK == LIN ? 1 : 2), NM = NA_W * NB_W;
    constexpr int NG = (NR + DPM - 1) / DPM < NM ? (NR + DPM - 1) / DPM : NM;
#pragma unroll
    for (int i = 0; i < NG; ++i) {
      __builtin_amdgcn_sched_group_barrier(0x8, 1, 0);
      if (i + 1 < NG) __builtin_amdgcn_sched_group_barrier(0x100, DPM, 0);
      else __builtin_amdgcn_sched_group_barrier(0x100, NR - DPM * (NG - 1), 0);
    }
    if constexpr (NM > NG) __builtin_amdgcn_sched_group_barrier(0x8, NM - NG, 0);
    SCHED0();
  }
  template <int G, class ISSUE> static DI void ring(int nt, ISSUE issue) {
    int wslot = NS - 2;
    for (int tau = 0; tau < nt - (NS - 2); ++tau) {
      WAIT_VM((NS - 3) * G);
      BAR();
      issue(tau + NS - 2, wslot * STAGE);
      wslot = (wslot + 1 == NS) ? 0 : wslot + 1;
    }
    if constexpr (NS >= 5) { WAIT_VM(2 * G); BAR(); }
    if constexpr (NS >= 4) { WAIT_VM(G); BAR(); }
    WAIT_VM(0); BAR();
    static_assert(NS >= 3 && NS <= 5, "");
  }
  template <bool F16>
  static DI void consume(const char* smem, int wave, int lane, int n, int& rslot, f32x16 (&acc)[NA_W][NB_W]) {
    const int wa = wave / WB, wb = wave - wa * WB;
    unsigned addrA[AK == LIN ? 1 : NA_W], addrB[BK == LIN ? 1 : NB_W];
    frag_addr<AK, NA_W>(addrA, 0, wa, lane);
    frag_addr<BK, NB_W>(addrB, A_BYTES, wb, lane);
    u32x4 fa[DEPTH][NA_W], fb[DEPTH][NB_W];
#pragma unroll
    for (int d = 1; d < DEPTH; ++d) {
#pragma unroll
      for (int i = 0; i < NA_W; ++i) fa[d][i] = u32x4{0, 0, 0, 0};
#pragma unroll
      for (int i = 0; i < NB_W; ++i) fb[d][i] = u32x4{0, 0, 0, 0};
    }
    auto mm = [&](const u32x4 (&a)[NA_W], const u32x4 (&b)[NB_W]) {
#pragma unroll
      for (int i = 0; i < NA_W; ++i)
#pragma unroll
        for (int j = 0; j < NB_W; ++j) acc[i][j] = mfma32<F16>(a[i], b[j], acc[i][j]);
    };
    auto step = [&](auto Z0) {
      constexpr int z0 = decltype(Z0)::value;
      BAR();
      SCHED0();
      const int sb = rslot * STAGE;
#pragma unroll
      for (int hh = 0; hh < 4; ++hh) {
        rd<AK, NA_W>(fa[(z0 + hh) % DEPTH], smem, addrA, sb, hh);
        rd<BK, NB_W>(fb[(z0 + hh) % DEPTH], smem, addrB, sb, hh);
        mm(fa[(z0 + hh + 1) % DEPTH], fb[(z0 + hh + 1) % DEPTH]);
        ilv();
      }
      rslot = (rslot + 1 == NS) ? 0 : rslot + 1;
    };
    if constexpr (DEPTH == 2) {
      for (int tau = 0; tau < n; ++tau) step(IC<0>{});
      mm(fa[1], fb[1]);
    } else {
      int tau = 0;
      for (; tau + 3 <= n; tau += 3) { step(IC<0>{}); step(IC<1>{}); step(IC<2>{}); }
      if (n - tau == 2) { step(IC<0>{}); step(IC<1>{}); mm(fa[0], fb[0]); mm(fa[1], fb[1]); }
      else if (n - tau == 1) { step(IC<0>{}); mm(fa[2], fb[2]); mm(fa[0], fb[0]); }
      else { mm(fa[1], fb[1]); mm(fa[2], fb[2]); }
    }
  }
};

DI float gelu_tanh(float u) {
  const float z = u * (1.0f + 0.044715f * u * u) * (-2.0f * 0.7978845608028654f * 1.4426950408889634f);
  return u * __builtin_amdgcn_rcpf(1.0f + __builtin_amdgcn_exp2f(z));
}

template <int NV> DI void mv_ilv() {
#pragma unroll
  for (int i = 0; i < 8; ++i) { __builtin_amdgcn_sched_group_barrier(0x8, 1, 0); __builtin_amdgcn_sched_group_barrier(0x2, NV, 0); }
  SCHED0();
}
struct EF { u32x4 r, i, n; };
DI EF ef_load(const char* p) { EF e; e.r = *(const u32x4*)p; e.i = *(const u32x4*)(p + 1024); e.n = *(const u32x4*)(p + 2048); return e; }
#define MF(a, b, c) mfma32<true>(a, b, c)
DI void cm_dA(f32x16& re, f32x16& im, u32x4 dr, u32x4 di, const EF& e) { re = MF(dr, e.r, re); im = MF(dr, e.i, im); re = MF(di, e.n, re); im = MF(di, e.r, im); }
DI void cm_dB(f32x16& re, f32x16& im, u32x4 dr, u32x4 di, const EF& e) { re = MF(e.r, dr, re); im = MF(e.i, dr, im); re = MF(e.n, di, re); im = MF(e.r, di, im); }
DI void cm_dAc(f32x16& re, f32x16& im, u32x4 dr, u32x4 di, const EF& e) { re = MF(dr, e.r, re); im = MF(dr, e.n, im); re = MF(di, e.i, re); im = MF(di, e.r, im); }
DI void wmul(f32x16& re, f32x16& im, const u32x4 (&wv)[4], float wsc) {
#pragma unroll
  for (int p = 0; p < 8; ++p) {
    const unsigned w0 = wv[p >> 1][(2 * p) & 3], w1 = wv[p >> 1][(2 * p + 1) & 3];
    tw_piece(re, im, p, f32x4{lo16(w0), lo16(w1), hi16(w0), hi16(w1)} * wsc);
  }
}
DI void bfly(f32x16& a, f32x16& b) {
  const f32x16 x = a, y = b; a = x + y; b = x - y;
}
DI void colfft_wave(const char* tab  , const char* ws, int c, int lane, float wsc, const u32x4 (&zr)[2][2], const u32x4 (&zi)[2][2],
                    char* img, int slot_re, int slot_im) {
  f32x4 tw[8];
  f32x16 r0 = {}, i0 = {}, r1 = {}, i1 = {};
  u32x4 ar[2], ai[2];
  EF ea = ef_load(tab + TAB_E1), eb = ef_load(tab + TAB_E1 + 3072);
  tw_load(tw, tab + TAB_TW1);
  SCHED0();
  cm_dA(r0, i0, zr[0][0], zi[0][0], ea); cm_dA(r0, i0, zr[0][1], zi[0][1], eb);
  SCHED0();
  cm_dA(r1, i1, zr[1][0], zi[1][0], ea); cm_dA(r1, i1, zr[1][1], zi[1][1], eb);
#pragma unroll
  for (int p = 0; p < 8; ++p) { tw_piece(r0, i0, p, tw[p]); tw[p] = *(const f32x4*)(tab + TAB_TW1 + 8192 + p * 1024); }
  ar[0] = pack8_f16(r0, 0); ar[1] = pack8_f16(r0, 1); ai[0] = pack8_f16(i0, 0); ai[1] = pack8_f16(i0, 1);
  mv_ilv<6>();
  u32x4 wv0[4], wv1[4];
#pragma unroll
  for (int i = 0; i < 4; ++i) wv0[i] = load_dev(ws, OFF_WT + c * 8192u + i * 1024 + lane * 16);
  r0 = f32x16{}; i0 = f32x16{};
  cm_dB(r0, i0, ar[0], ai[0], ea); ea = ef_load(tab + TAB_EO);
  cm_dB(r0, i0, ar[1], ai[1], eb); eb = ef_load(tab + TAB_EO + 3072);
  tw_apply(r1, i1, tw);
  ar[0] = pack8_f16(r1, 0); ar[1] = pack8_f16(r1, 1); ai[0] = pack8_f16(i1, 0); ai[1] = pack8_f16(i1, 1);
  mv_ilv<6>();
#pragma unroll
  for (int i = 0; i < 4; ++i) wv1[i] = load_dev(ws, OFF_WT + c * 8192u + (4 + i) * 1024 + lane * 16);
  r1 = f32x16{}; i1 = f32x16{};
  cm_dB(r1, i1, ar[0], ai[0], ea); ea = ef_load(tab + TAB_E1);
  cm_dB(r1, i1, ar[1], ai[1], eb); eb = ef_load(tab + TAB_E1 + 3072);
  SCHED0();
  bfly(r0, r1); bfly(i0, i1);
  wmul(r0, i0, wv0, wsc);
  wmul(r1, i1, wv1, wsc);
  bfly(r0, r1); bfly(i0, i1);
  u32x4 dr[2], di[2];
  ar[0] = pack8_f16(r0, 0); ar[1] = pack8_f16(r0, 1); ai[0] = pack8_f16(i0, 0); ai[1] = pack8_f16(i0, 1);
  SCHED0();
  tw_load(tw, tab + TAB_TW2);
  r0 = f32x16{}; i0 = f32x16{};
  cm_dAc(r0, i0, ar[0], ai[0], ea); ea = ef_load(tab + TAB_EOI);
  cm_dAc(r0, i0, ar[1], ai[1], eb); eb = ef_load(tab + TAB_EOI + 3072);
  dr[0] = pack8_f16(r1, 0); dr[1] = pack8_f16(r1, 1); di[0] = pack8_f16(i1, 0); di[1] = pack8_f16(i1, 1);
  mv_ilv<2>();
  r1 = f32x16{}; i1 = f32x16{};
  cm_dAc(r1, i1, dr[0], di[0], ea); ea = ef_load(tab + TAB_E1);
  cm_dAc(r1, i1, dr[1], di[1], eb); eb = ef_load(tab + TAB_E1 + 3072);
#pragma unroll
  for (int p = 0; p < 8; ++p) { tw_piece(r0, i0, p, tw[p]); tw[p] = *(const f32x4*)(tab + TAB_TW2 + 8192 + p * 1024); }
  ar[0] = pack8_f16(r0, 0); ar[1] = pack8_f16(r0, 1); ai[0] = pack8_f16(i0, 0); ai[1] = pack8_f16(i0, 1);
  mv_ilv<6>();
  r0 = f32x16{}; i0 = f32x16{};
  cm_dAc(r0, i0, ar[0], ai[0], ea); cm_dAc(r0, i0, ar[1], ai[1], eb);
  tw_apply(r1, i1, tw);
  dr[0] = pack8_f16(r1, 0); dr[1] = pack8_f16(r1, 1); di[0] = pack8_f16(i1, 0); di[1] = pack8_f16(i1, 1);
  mv_ilv<6>();
  r1 = f32x16{}; i1 = f32x16{};
  cm_dAc(r1, i1, dr[0], di[0], ea); cm_dAc(r1, i1, dr[1], di[1], eb);
  SCHED0();
#pragma unroll
  for (int g = 0; g < 4; ++g) {
    const int Q = (lane & 31) * 8 + 2 * g + (lane >> 5), sw = lane & 15, r = 4 * g;
    if (slot_im == -2) {
      *(u32x4*)(img + Q * 16) = u32x4{pack_f16(r0[r], r1[r]), pack_f16(r0[r + 1], r1[r + 1]), pack_f16(r0[r + 2], r1[r + 2]), pack_f16(r0[r + 3], r1[r + 3])};
      continue;
    }
#pragma unroll
    for (int z = 0; z < 2; ++z) {
      const int q = r + 2 * z;
      *(u32x4*)(img + Q * 256 + (((slot_re + z) ^ sw) * 16)) =
          u32x4{pack_f16(r0[q], i0[q]), pack_f16(r1[q], i1[q]), pack_f16(r0[q + 1], i0[q + 1]), pack_f16(r1[q + 1], i1[q + 1])};
    }
  }
}
#undef MF
constexpr int FIN_OS = 2336;
DI void irfft_wave(const char* rec, const char* y288, int w, int lane, char* orow, const char* kt) {
  const int row = lane & 31, hh = lane >> 5, k1 = row & 15, rr = 2 * w + (row >> 4);
  const bool ok = k1 < 10;
  const int k1c = ok ? k1 : 0;
  const char* pd = rec + rr * 4 + 32 * (k1c + 72 * hh);
  const char* pm = rec + rr * 4 + 32 * (576 - k1c - 72 * hh - 18 * 27);
  unsigned v[2][8];
#pragma unroll
  for (int j = 0; j < 8; ++j) {
    v[0][j] = *(const unsigned*)(pd + 576 * (8 * (j >> 2) + (j & 3)));
    v[1][j] = *(const unsigned*)(pm + 576 * (27 - (16 + 8 * (j >> 2) + (j & 3))));
  }
  const unsigned v288 = *(const u16*)(y288 + rr * 2);
  f32x4 tw[8];
  tw_load(tw, kt + 8192);
  if (k1 == 0 && hh == 0) { v[0][0] &= 0xffffu; v[1][0] = v288; }
  f32x16 re = {}, im = {};
#pragma unroll
  for (int u = 0; u < 2; ++u) {
    u32x4 a_r, a_i;
#pragma unroll
    for (int e = 0; e < 4; ++e) {
      a_r[e] = ok ? __builtin_amdgcn_perm(v[u][2 * e + 1], v[u][2 * e], 0x05040100u) : 0u;
      a_i[e] = ok ? (__builtin_amdgcn_perm(v[u][2 * e + 1], v[u][2 * e], 0x07060302u) ^ (u ? 0x80008000u : 0u)) : 0u;
    }
    cm_dAc(re, im, a_r, a_i, ef_load(kt + 2048 + u * 3072));
  }
  const u32x4 cr = *(const u32x4*)kt, ci = *(const u32x4*)(kt + 1024);
  tw_apply(re, im, tw);
#pragma unroll
  for (int u = 0; u < 2; ++u) {
    f32x16 y = {};
    y = mfma32<true>(cr, pack8_f16(re, u), y);
    y = mfma32<true>(ci, pack8_f16(im, u), y);
    float* o = (float*)(orow + u * FIN_OS) + (lane & 31) + 128 * hh;
#pragma unroll
    for (int rho = 0; rho < 8; ++rho) o[32 * ((rho & 3) + 8 * (rho >> 2))] = y[rho];
    if (hh == 0) { o[32 * 16] = y[8]; o[32 * 17] = y[9]; }
  }
}

DI unsigned wmax_exp(const char* ws, int lane) {
  const u32x4 mv = load_dev(ws, OFF_WMAX + lane * 16);
  unsigned mx = max(max(mv[0], mv[1]), max(mv[2], mv[3]));
#pragma unroll
  for (int o = 32; o; o >>= 1) mx = max(mx, (unsigned)__shfl_xor((int)mx, o));
  return min(mx >> 7, 252u);
}

DI unsigned lds_arrive(char* p) { return __hip_atomic_fetch_add((LAS unsigned*)p, 1u, __ATOMIC_RELAXED, __HIP_MEMORY_SCOPE_WORKGROUP); }
DI void lds_arrive_wait(char* p, unsigned n, int lane) {
  if (lane == 0) lds_arrive(p);
  while (__hip_atomic_load((LAS unsigned*)p, __ATOMIC_RELAXED, __HIP_MEMORY_SCOPE_WORKGROUP) < n) __builtin_amdgcn_s_sleep(1);
  asm volatile("" ::: "memory");
}
DI void lds_wait(const char* p) {
  while (__hip_atomic_load((const LAS unsigned*)p, __ATOMIC_RELAXED, __HIP_MEMORY_SCOPE_WORKGROUP) == 0u) __builtin_amdgcn_s_sleep(1);
  asm volatile("" ::: "memory");
}
using CG1 = GC<1, 3, 4, 1, TR128, LIN, 5, 2, 3>;
using CG2 = GC<3, 2, 2, 2, LIN, TR128, 4, 2, 2>;
constexpr int P1_RS = 1216, P1_XS = 3 * CG1::STAGE, P1_HS = P1_XS + 8 * P1_RS, P1_C1 = P1_XS + 16 * P1_RS, P1_E = P1_C1 + 6144,
              P1_TW = P1_E + 6144, P1_O = P1_TW + 8192, P1_WT = P1_O + 9216, P1_RED = P1_WT + 10512, P1_DUMMY = 147456;
static_assert(P1_RED + 48 <= P1_DUMMY && P1_DUMMY >= CG1::LDS_BYTES, "");

__global__ __launch_bounds__(768, 3) void k_all(const u16* __restrict__ x, const u16* __restrict__ wr, const u16* __restrict__ wi,
                                                const char* __restrict__ w1, const u16* __restrict__ b1, const char* __restrict__ w2,
                                                const u16* __restrict__ b2, char* __restrict__ ws, u16* __restrict__ out) {
  extern __shared__ __attribute__((aligned(16))) char smem[];
  const int lane = threadIdx.x & 63, wave = __builtin_amdgcn_readfirstlane(threadIdx.x >> 6);
  const int b = blockIdx.x, g = b & 7, idx = b >> 3;
  const unsigned ldsb = (unsigned)(size_t)(LAS char*)smem;
  const u32x4 rs_ws = mk_rsrc(ws, WS_END), rs_w1 = mk_rsrc(w1, 18874368u), rs_w2 = mk_rsrc(w2, 18874368u);
  if (wave == 11 && idx < 6) {
    const u64 pc = __builtin_amdgcn_s_getpc() & ~(u64)127;
    dma4(mk_rsrc((const void*)pc, CODE_WARM), ldsb + P1_DUMMY + wave * 256, (unsigned)(idx * 64 + lane) * 128u);
  }
  const int tile = g * 27 + idx, ft = tile / 6, dt1 = tile - ft * 6;
  const int ft0 = (g * 27) / 6, nl1 = ((g * 27 + 26) / 6 - ft0 + 1) * 2;
  auto issue_w1x = [&](int lw, int kt, int so) {
    constexpr unsigned ld = F_ * 2;
    const unsigned vo = CG1::voff<TR128>(lane, 64 * ld);
#pragma unroll
    for (int m = 0; m < 4; ++m) {
      const int a = lw * 4 + m;
      dma16<0>(rs_w1, ldsb + so + a * 1024, vo, (unsigned)(((4 * a) & 31) * 64 + 2 * kt + (a >> 3)) * ld + ft * 256u);
    }
  };
  const int tb = (g >> 2) * 128 + idx * 4 + (g & 3);
  auto gate = [&] {
    while (__hip_atomic_load((const LAS unsigned*)(smem + P1_RED + 16), __ATOMIC_RELAXED, __HIP_MEMORY_SCOPE_WORKGROUP) < 3u) __builtin_amdgcn_s_sleep(1);
  };
  u64 tag;
  if (__builtin_expect(wave < 4, 1)) {
    const int tid = threadIdx.x, s2 = tb >> 2, t = (tb >> 1) & 1, h = tb & 1;
    const int rr = tid >> 5, l32 = tid & 31;
    const int s1 = 16 * t + 8 * (rr >> 2) + 4 * h + (rr & 3);
    const unsigned* xr = (const unsigned*)(x + (size_t)(64 * s1 + s2) * D_);
    unsigned raw[9];
#pragma unroll
    for (int i = 0; i < 9; ++i) raw[i] = xr[32 * i + l32];
    float v[18];
#pragma unroll
    for (int i = 0; i < 9; ++i) { v[2 * i] = lo16(raw[i]); v[2 * i + 1] = hi16(raw[i]); }
#pragma unroll
    for (int pass = 0; pass < 2; ++pass) {
      float sm = 0.f;
#pragma unroll
      for (int i = 0; i < 18; ++i) sm += v[i];
      const float mean = sum32(sm) * (1.0f / 576.0f);
      float sq = 0.f;
#pragma unroll
      for (int i = 0; i < 18; ++i) { float dlt = v[i] - mean; sq += dlt * dlt; }
      const float var = sum32(sq) * (1.0f / 576.0f);
      const float mb = rbf(mean);
      const float sd = rbf(__builtin_sqrtf(rbf(rbf(var) + 1e-5f)));
      const float rs = 1.0f / sd;
      char* dst = smem + P1_XS + pass * (8 * P1_RS) + rr * P1_RS + l32 * 4;
#pragma unroll
      for (int i = 0; i < 9; ++i) {
        unsigned p = pack_bf16(v[2 * i] - mb, v[2 * i + 1] - mb);
        p = pack_bf16(lo16(p) * rs, hi16(p) * rs);
        v[2 * i] = lo16(p); v[2 * i + 1] = hi16(p);
        *(unsigned*)(dst + i * 128) = pass == 0 ? pack_f16(v[2 * i], v[2 * i + 1]) : p;
      }
    }
    WAIT_LGKM0();
    BAR();
    { const u64 tv = *(const u64*)(smem + P1_RED + 24);
      tag = (u64)__builtin_amdgcn_readfirstlane((unsigned)(tv >> 32)) << 32 | (unsigned)__builtin_amdgcn_readfirstlane((unsigned)tv); }
    {
      const int hh = lane >> 5, blk = (lane >> 4) & 1, q = (lane & 15) >> 2, p = lane & 3;
      const char* xa = smem + P1_XS + (2 * wave) * P1_RS + blk * 32 + p * 8;
      const char* cf = smem + P1_C1 + lane * 16;
      f32x16 re = {}, im = {};
      f32x4 tw[8];
      tw_load(tw, smem + P1_TW + lane * 16);
#pragma unroll
      for (int ks = 0; ks < 3; ++ks) {
        const char* a0 = ks < 2 ? xa + ks * P1_RS + (4 * hh + q) * 64 : xa + (q >> 1) * P1_RS + (16 + (q & 1)) * 64;
        const u32x4 a = tr_frag(a0, ks < 2 ? a0 + 512 : a0);
        re = mfma32<true>(a, *(const u32x4*)(cf + (2 * ks) * 1024), re);
        im = mfma32<true>(a, *(const u32x4*)(cf + (2 * ks + 1) * 1024), im);
      }
      tw_apply(re, im, tw);
      f32x16 xr_ = {}, xi_ = {};
#pragma unroll
      for (int u = 0; u < 2; ++u) cm_dB(xr_, xi_, pack8_f16(re, u), pack8_f16(im, u), ef_load(smem + P1_E + u * 3072 + lane * 16));
      const int col = lane & 31, k1 = col & 15, row2 = 2 * (2 * wave + (col >> 4));
      if (k1 < 10) {
        char* o1 = smem + P1_O + 32 * k1 + 576 * 4 * hh + row2;
        char* o2 = smem + P1_O + 32 * (576 - k1) - 576 * (4 * hh + 27) + row2;
#pragma unroll
        for (int rho = 0; rho < 8; ++rho) {
          const unsigned pk = pack_f16(xr_[rho] * (1.0f / 32.0f), xi_[rho] * (1.0f / 32.0f));
          char* d = o1 + 576 * ((rho & 3) + 8 * (rho >> 2));
          *(u16*)d = (u16)pk;
          if (rho != 0 || k1 != 0 || hh != 0) *(u16*)(d + 16) = (u16)(pk >> 16);
        }
        if (k1 >= 1 && k1 <= 8) {
#pragma unroll
          for (int rho = 8; rho < 16; ++rho) {
            const unsigned pk = pack_f16(xr_[rho] * (1.0f / 32.0f), xi_[rho] * (-1.0f / 32.0f));
            char* d = o2 + 576 * (27 - ((rho & 3) + 8 * (rho >> 2)));
            *(u16*)d = (u16)pk;
            *(u16*)(d + 16) = (u16)(pk >> 16);
          }
        } else if (k1 == 0 && hh == 0) {
          *(u16*)(smem + P1_O + 16 + row2) = (u16)pack_f16(xr_[8] * (1.0f / 32.0f), 0.f);
        }
      }
    }
    WAIT_LGKM0();
    lds_arrive_wait(smem + P1_RED + 32, 4u, lane);
    gate();
    const unsigned P = ((s2 & 1) * 2 + t) * 64 + h * 32 + (s2 >> 1);
#pragma unroll
    for (int it = 0; it < 3; ++it) {
      const int p = tid + it * 256;
      if (p < 576) store_dev(ws, OFF_X1 + P * 9216u + p * 16, *(const u32x4*)(smem + P1_O + p * 16));
    }
    WAIT_VM(0);
    if (lane == 0 && lds_arrive(smem + P1_RED + 36) == 7u) set_flag(ws, SY_A + b * 8, tag);
  } else if (wave < 8) {
    const int tid = threadIdx.x - 256;
    const int rg = b >> 2, cg = b & 3, c0 = cg * 72 + (cg > 0), nc = cg == 0 ? 73 : 72, kq = rg >> 2, ko = rg & 3;
    unsigned rv[10], iv[10];
#pragma unroll
    for (int it = 0; it < 10; ++it) {
      const int i = tid + it * 256, row = i / 73, col = i - row * 73;
      const bool ok = i < 32 * 73 && col < nc;
      const int off = ok ? (8 * ko + (row & 7) + 32 * (4 * kq + (row >> 3))) * NC_ + c0 + col : 0;
      rv[it] = wr[off]; iv[it] = wi[off];
    }
    BAR();
    { const u64 tv = *(const u64*)(smem + P1_RED + 24);
      tag = (u64)__builtin_amdgcn_readfirstlane((unsigned)(tv >> 32)) << 32 | (unsigned)__builtin_amdgcn_readfirstlane((unsigned)tv); }
    unsigned mx = 0;
#pragma unroll
    for (int it = 0; it < 10; ++it) {
      const int i = tid + it * 256, row = i / 73, col = i - row * 73;
      if (i < 32 * 73 && col < nc) {
        mx = max(mx, max(rv[it] & 0x7fffu, iv[it] & 0x7fffu));
        *(unsigned*)(smem + P1_WT + col * 144 + (row & 7) * 16 + (row >> 3) * 4) = rv[it] | (iv[it] << 16);
      }
    }
#pragma unroll
    for (int o = 32; o; o >>= 1) mx = max(mx, (unsigned)__shfl_xor((int)mx, o));
    unsigned* red = (unsigned*)(smem + P1_RED);
    if (lane == 0) red[wave - 4] = mx;
    WAIT_LGKM0();
    lds_arrive_wait(smem + P1_RED + 44, 4u, lane);
    if (tid == 0) __hip_atomic_store((gu32*)(ws + OFF_WMAX + b * 4), max(max(red[0], red[1]), max(red[2], red[3])), RLX_AGENT);
    const int U = kq >> 3, h = kq & 1, rq = (kq >> 1) & 3;
    gate();
#pragma unroll
    for (int it = 0; it < 3; ++it) {
      const int p = tid + it * 256;
      if (p < nc * 8) {
        const int col = p >> 3, a = p & 7;
        store_dev(ws, OFF_WT + (unsigned)(c0 + col) * 8192u + ((U * 4 + rq) * 64 + h * 32 + 8 * ko + a) * 16,
                  *(const u32x4*)(smem + P1_WT + col * 144 + a * 16));
      }
    }
    WAIT_VM(0);
    if (lane == 0 && lds_arrive(smem + P1_RED + 36) == 7u) set_flag(ws, SY_A + b * 8, tag);
    if (idx < 27) { issue_w1x(wave - 4, 0, 0); issue_w1x(wave - 4, 1, CG1::STAGE); issue_w1x(wave - 4, 2, 2 * CG1::STAGE); }
  } else {
    u64 ep = 0;
    if (threadIdx.x == 512) ep = __hip_atomic_load((gu64*)(ws + SY_EPOCH), RLX_AGENT);
    if (wave != 10) {
      const u32x4 rt = mk_rsrc(&T_FWD, 20480u);
      for (int fr = wave == 11 ? 2 : wave - 8; fr < 20; fr += 3) dma16<0>(rt, ldsb + P1_C1 + fr * 1024, lane * 16, fr * 1024u);
    } else {
      __builtin_amdgcn_s_sleep(32);
#pragma unroll
      for (int i = 0; i < 5; ++i)
        dma4(rs_w1, ldsb + P1_DUMMY + wave * 256, w1_line((i * 32 + idx) * 64 + lane, PF1_ROWS * nl1, 0, nl1, ft0 * 256u));
    }
    if (threadIdx.x == 512) {
      *(u64*)(smem + P1_RED + 24) = ep * 6364136223846793005ull + 1442695040888963407ull;
      *(unsigned*)(smem + P1_RED + 16) = 0u; *(u64*)(smem + P1_RED + 32) = 0ull; *(u64*)(smem + P1_RED + 40) = 0ull;
    }
    if (wave != 10) WAIT_VM(0);
    WAIT_LGKM0();
    BAR();
    { const u64 tv = *(const u64*)(smem + P1_RED + 24);
      tag = (u64)__builtin_amdgcn_readfirstlane((unsigned)(tv >> 32)) << 32 | (unsigned)__builtin_amdgcn_readfirstlane((unsigned)tv); }
    if (wave != 10) {
      const int s2 = tb >> 2, t = (tb >> 1) & 1, h = tb & 1;
      for (int cb = wave == 11 ? 2 : wave - 8; cb < 9; cb += 3) {
        const int gg = lane >> 4, q = (lane & 15) >> 2, pp = lane & 3;
        const char* src = smem + P1_HS + q * P1_RS + (cb * 64 + gg * 16 + pp * 4) * 2;
        const int df = cb * 2 + (gg >> 1), i = 8 * q + 4 * (gg & 1) + pp;
        store_dev(ws, OFF_H + (unsigned)(df * 128 + 2 * s2 + t) * 1024u + (h * 32 + i) * 16, tr_frag(src, src + 4 * P1_RS));
      }
      WAIT_VM(0);
      unsigned last = 0;
      if (lane == 0) last = lds_arrive(smem + P1_RED + 16);
      if (__builtin_amdgcn_readfirstlane(last) == 2u) { if (lane < 8) set_flag(ws, SY_H + lane * 2048 + tb * 8, tag); }
    }
  }
  if (idx < 27) {
    char* ebuf = smem + (29 % 5) * CG1::STAGE;
    u32x2 bv[3][2];
#pragma unroll
    for (int it = 0; it < 3; ++it) {
      const u32x4* bp = (const u32x4*)(b1 + ft * 128 + (min(wave + 11 * it, 23) / 3) * 16);
      const u32x4 b0 = bp[0], b1v = bp[1];
      const bool hi = lane >= 32;
      bv[it][0] = u32x2{hi ? b0[2] : b0[0], hi ? b0[3] : b0[1]}; bv[it][1] = u32x2{hi ? b1v[2] : b1v[0], hi ? b1v[3] : b1v[1]};
      asm volatile("" : "+v"(bv[it][0]), "+v"(bv[it][1]));
    }
    if (wave == 11) {
      const unsigned fh = SY_H + g * 2048 + lane * 8;
      poll_flags<1>(ws, fh, true, tag);
      if (lane == 0) lds_arrive(smem + P1_RED + 40);
      bool ok[3];
#pragma unroll
      for (int q = 0; q < 3; ++q) ok[q] = __all(__hip_atomic_load((gu64*)(ws + fh + (q + 1) * 512), RLX_AGENT) == tag);
      for (int tau = 0; tau < 32; ++tau) {
#pragma unroll
        for (int q = 0; q < 3; ++q)
          if (tau == 8 * q + 5 && !ok[q]) poll_flags<1>(ws, fh + (q + 1) * 512, true, tag);
        if (tau == 21 && b == 0 && lane == 0) set_flag(ws, SY_EPOCH, tag);
        BAR();
        if (tau == 29) {
#pragma unroll
          for (int i = 0; i < PF2T; ++i) dma4(rs_w2, ldsb + P1_DUMMY + wave * 256, w2_line((i * 27 + idx) * 64 + lane, g));
        }
      }
    } else if (wave >= 8) {
      auto issue_h = [&](int kt, int so) {
#pragma unroll
        for (int m = 0; m < 4; ++m)
          dma16<2>(rs_ws, ldsb + so + CG1::A_BYTES + ((wave - 8) * 4 + m) * 1024, lane * 16,
                   OFF_H + (unsigned)((dt1 * 3 + (wave - 8)) * 128 + kt * 4 + m) * 1024u);
      };
      lds_wait(smem + P1_RED + 40);
      issue_h(0, 0); issue_h(1, CG1::STAGE); issue_h(2, 2 * CG1::STAGE);
      CG1::ring<4>(32, issue_h);
    } else if (wave >= 4) {
      CG1::ring<4>(32, [&](int kt, int so) { issue_w1x(wave - 4, kt, so); });
    } else {
      f32x16 acc[1][3] = {};
      int rslot = 0;
      CG1::consume<false>(smem, wave, lane, 32, rslot, acc);
#pragma unroll
      for (int j = 0; j < 3; ++j)
#pragma unroll
        for (int s = 0; s < 2; ++s) {
          u32x4 val;
#pragma unroll
          for (int e = 0; e < 4; ++e) val[e] = pack_bf16(acc[0][j][8 * s + 2 * e], acc[0][j][8 * s + 2 * e + 1]);
          *(u32x4*)(ebuf + ((wave * 2 + s) * 3 + j) * 1024 + lane * 16) = val;
        }
    }
    WAIT_LGKM0();
    BAR();
    if (wave < 11) {
#pragma unroll
      for (int it = 0; it < 3; ++it) {
        const int i = wave + 11 * it;
        if (i < 24) {
          const int z = i / 3, j = i - 3 * z;
          const u32x4 a = *(const u32x4*)(ebuf + i * 1024 + lane * 16);
          u32x4 val;
#pragma unroll
          for (int e = 0; e < 4; ++e) {
            const unsigned bb = bv[it][e >> 1][e & 1];
            const unsigned pk = pack_bf16(lo16(a[e]) + lo16(bb), hi16(a[e]) + hi16(bb));
            val[e] = pack_bf16(gelu_tanh(lo16(pk)), gelu_tanh(hi16(pk)));
          }
          store_dev(ws, OFF_U + (unsigned)((dt1 * 3 + j) * 288 + ft * 8 + z) * 1024u + lane * 16, val);
        }
      }
      WAIT_VM(0);
    } else if (lane == 0) *(unsigned*)(smem + 163832) = 0u;
    WAIT_LGKM0();
    BAR();
    if (threadIdx.x < 2) set_flag(ws, SY_U + threadIdx.x * 2048 + tile * 8, tag);
  } else {
    const int me = (idx - 27) * 8 + g;
    {
      const int total = (2048 - PF1_ROWS) * nl1;
      for (int i0 = ((idx - 27) * 12 + wave) * 64; i0 < total; i0 += 60 * 64) {
        dma4(rs_w1, ldsb + P1_DUMMY + wave * 256, w1_line(i0 + lane, total, PF1_ROWS, nl1, ft0 * 256u));
        WAIT_VM(7);
      }
    }
    if (me < 37) {
      if (wave == 8) poll_flags<4>(ws, SY_A + lane * 8, true, tag);
      BAR();
      const int cbk = me == 36 ? 0 : me;
      char* img = smem + 65536;
      for (int i = wave; i < 64; i += 12) {
        const int P = 4 * i + (lane >> 4);
        dma16<2>(rs_ws, ldsb + 65536 + i * 1024, (unsigned)P * 9216u + (unsigned)(((lane & 15) ^ (P & 15)) * 16), OFF_X1 + cbk * 256u);
      }
      for (int i = wave; i < TAB_BYTES / 1024; i += 12) dma16<0>(mk_rsrc(&T_COL, TAB_BYTES), ldsb + i * 1024, lane * 16, i * 1024u);
      const bool act = me == 36 ? wave == 0 : wave < 8;
      const int c = me == 36 ? 288 : me * 8 + wave;
      const float wsc = __uint_as_float((253u - wmax_exp(ws, lane)) << 23) * (1.0f / 64.0f);
      WAIT_VM(0);
      BAR();
      const int slot_re = me == 36 ? 1 : 2 * wave, slot_im = c == 288 ? -2 : (c == 0 ? -1 : 2 * wave + 1);
      u32x4 zr[2][2], zi[2][2];
      if (act) {
#pragma unroll
        for (int z = 0; z < 4; ++z) {
          const char* p = img + (z * 64 + lane) * 256;
          zr[z >> 1][z & 1] = *(const u32x4*)(p + ((slot_re ^ (lane & 15)) * 16));
          zi[z >> 1][z & 1] = slot_im >= 0 ? *(const u32x4*)(p + ((slot_im ^ (lane & 15)) * 16)) : u32x4{0, 0, 0, 0};
        }
      }
      WAIT_LGKM0();
      BAR();
      if (act) colfft_wave(smem + lane * 16, ws, c, lane, wsc, zr, zi, img, slot_re, slot_im);
      else if (wave >= 8 && idx < 31) {
        __builtin_amdgcn_s_sleep(64);
        for (int i0 = ((idx - 27) * 4 + wave - 8) * 64; i0 < PF2C_ROWS * 16; i0 += 16 * 64) {
          dma4(rs_w2, ldsb + P1_DUMMY + wave * 256, w2_line(PF2T * 27 * 64 + i0 + lane, g));
          WAIT_VM(7);
        }
      }
      WAIT_LGKM0();
      BAR();
      if (me == 36) {
        if (wave < 4) store_dev(ws, OFF_Y288 + wave * 1024 + lane * 16, *(const u32x4*)(img + wave * 1024 + lane * 16));
      } else {
        for (int i = wave; i < 64; i += 12) {
          const int Qy = 4 * i + (lane >> 4);
          store_dev(ws, OFF_Y1 + (unsigned)Qy * 9216u + cbk * 256 + (lane & 15) * 16,
                    *(const u32x4*)(img + Qy * 256 + (((lane & 15) ^ ((Qy >> 3) & 15)) * 16)));
        }
      }
      WAIT_VM(0);
      BAR();
      if (threadIdx.x == 0) set_flag(ws, SY_Y + me * 8, tag);
    }
  }
  if (idx < 24) {
    const int ks = g >> 1, dt = idx % 3, st = (g & 1) * 8 + idx / 3;
    const int rot = (g & 1) * 9;
    char* fbuf = smem + (15 & 3) * CG2::STAGE;
    const unsigned myflags = SY_T + (unsigned)((st * 4 + ks) * 3 + dt) * 64u;
    if (wave >= 8) {
      auto issue_u = [&](int k0, int so) {
        const int kt = k0 + rot < 18 ? k0 + rot : k0 + rot - 18;
        if (k0 == 0 || k0 == 8) {
          const int kf = k0 == 0 ? rot : 9 - rot;
          if (wave == 8) {
            poll_flags<1>(ws, SY_U + (g & 1) * 2048 + (unsigned)((9 * ks + (kf >> 1) + (lane >> 1)) * 6 + 2 * dt + (lane & 1)) * 8u, lane < 10, tag);
            if (k0 == 0) { if (lane == 0) lds_arrive(smem + 163832); }
          } else if (k0 == 0) lds_wait(smem + 163832);
        }
#pragma unroll
        for (int m = 0; m < 6; ++m) {
          const int a = (wave - 8) * 6 + m;
          dma16<2>(rs_ws, ldsb + so + a * 1024, lane * 16, OFF_U + (unsigned)((dt * 6 + (a >> 2)) * 288 + (ks * 18 + kt) * 4 + (a & 3)) * 1024u);
        }
      };
      issue_u(0, 0); issue_u(1, CG2::STAGE);
      CG2::ring<6>(18, issue_u);
      BAR();
      if (wave == 8) poll_flags<1>(ws, myflags + (lane & 3) * 8, true, tag);
    } else if (wave >= 4) {
      auto issue_w2 = [&](int k0, int so) {
        const int kt = k0 + rot < 18 ? k0 + rot : k0 + rot - 18;
        constexpr unsigned ld = S_ * 2;
        const unsigned vo = CG2::voff<TR128>(lane, ld);
#pragma unroll
        for (int m = 0; m < 4; ++m) {
          const int a = (wave - 4) * 4 + m;
          const unsigned sof = (unsigned)((ks * 18 + kt) * 64 + 4 * a) * ld + st * 256u;
          if (k0 >= NTF2) dma16<1>(rs_w2, ldsb + so + CG2::A_BYTES + a * 1024, vo, sof);
          else dma16<0>(rs_w2, ldsb + so + CG2::A_BYTES + a * 1024, vo, sof);
        }
      };
      issue_w2(0, 0); issue_w2(1, CG2::STAGE);
      CG2::ring<4>(18, issue_w2);
      BAR();
    } else {
    f32x16 acc[3][2] = {};
    int rslot = 0;
    CG2::consume<false>(smem, wave, lane, 18, rslot, acc);
    const int wa = wave >> 1, wb = wave & 1;
    const bool own0 = wb * 2 == ks, own1 = wb * 2 + 1 == ks;
#pragma unroll
    for (int j = 0; j < 2; ++j) {
      if (j ? own1 : own0) {
#pragma unroll
        for (int i = 0; i < 3; ++i)
#pragma unroll
          for (int q = 0; q < 4; ++q)
            *(f32x4*)(fbuf + ((wa * 3 + i) * 4 + q) * 1024 + lane * 16) =
                f32x4{acc[i][j][4 * q], acc[i][j][4 * q + 1], acc[i][j][4 * q + 2], acc[i][j][4 * q + 3]};
        continue;
      }
#pragma unroll
      for (int i = 0; i < 3; ++i) {
        const unsigned o = OFF_SLAB + (unsigned)((ks * 64 + st * 4 + wb * 2 + j) * 18 + dt * 6 + wa * 3 + i) * 2048u + lane * 16;
#pragma unroll
        for (int u = 0; u < 2; ++u) {
          u32x4 val;
#pragma unroll
          for (int e = 0; e < 4; ++e) val[e] = pack_bf16(acc[i][j][8 * u + 2 * e], acc[i][j][8 * u + 2 * e + 1]);
          store_dev(ws, o + u * 1024, val);
        }
      }
    }
    WAIT_VM(0); WAIT_LGKM0();
    BAR();
    if (wave == 0 && lane < 4 && lane != ks) set_flag(ws, SY_T + (unsigned)((st * 4 + lane) * 3 + dt) * 64u + ks * 8, tag);
    }
    BAR();
    const int sf = st * 4 + ks;
    const unsigned o = (unsigned)(sf * 18 + dt * 6 + (wave >> 1)) * 2048u + (wave & 1) * 1024 + lane * 16;
    u32x4 pv[4];
#pragma unroll
    for (int q = 0; q < 3; ++q) pv[q] = load_dev(ws, OFF_SLAB + o + ((ks + 1 + q) & 3) * 2359296u);
    pv[3] = load_dev(ws, OFF_R + o);
    const f32x4 a0 = *(const f32x4*)(fbuf + wave * 2048 + lane * 16), a1 = *(const f32x4*)(fbuf + wave * 2048 + 1024 + lane * 16);
    u32x4 ov;
#pragma unroll
    for (int e = 0; e < 4; ++e)
      ov[e] = pack_bf16((e < 2 ? a0[2 * e] : a1[2 * e - 4]) + (lo16(pv[0][e]) + lo16(pv[1][e])) + (lo16(pv[2][e]) + lo16(pv[3][e])),
                        (e < 2 ? a0[2 * e + 1] : a1[2 * e - 3]) + (hi16(pv[0][e]) + hi16(pv[1][e])) + (hi16(pv[2][e]) + hi16(pv[3][e])));
    *(u32x4*)(out + (size_t)(sf * 32 + (lane & 31)) * D_ + (dt * 6 + (wave >> 1)) * 32 + (lane >> 5) * 16 + (wave & 1) * 8) = ov;
  } else {
    const int me = (idx - 24) * 8 + g, sr = lane & 31;
    char* kt = smem + 37888; char* yb = kt + 17408;
    u32x4 xv[3];
#pragma unroll
    for (int it = 0; it < 3; ++it) xv[it] = *(const u32x4*)(x + (size_t)(me * 32 + sr) * D_ + ((it * 12 + wave) * 2 + (lane >> 5)) * 8);
    const float bias = lo16(b2[me * 32 + sr]);
    BAR();
    for (int i = wave; i < 16; i += 12) dma16<0>(mk_rsrc(&T_INV, 16384u), ldsb + 37888 + i * 1024, lane * 16, i * 1024u);
    if (wave == 8) { poll_flags<1, 16>(ws, SY_Y + min(lane, 36) * 8, true, tag); }
    BAR();
#pragma unroll
    for (int it = 0; it < 3; ++it) dma16<2>(rs_ws, ldsb + (it * 12 + wave) * 1024, lane * 16, OFF_Y1 + me * 36864u + (it * 12 + wave) * 1024u);
    if (wave == 4) dma16<2>(rs_ws, ldsb + 37888 + 16384, (lane & 3) * 16, OFF_Y288 + me * 64u);
    const float fs = __uint_as_float((wmax_exp(ws, lane) + 1u) << 23) * (1.0f / 9.0f);
    WAIT_VM(0);
    BAR();
    for (int task = wave; task < 16; task += 12)
      irfft_wave(smem + (task >> 2) * 9216, kt + 16384 + (task >> 2) * 16, task & 3, lane, yb + task * 2 * FIN_OS, kt + lane * 16);
    WAIT_LGKM0();
    BAR();
#pragma unroll
    for (int it = 0; it < 3; ++it) {
      const int dp = (it * 12 + wave) * 2 + (lane >> 5);
      const char* yp = yb + sr * FIN_OS + dp * 32;
      const f32x4 y0 = *(const f32x4*)yp, y1 = *(const f32x4*)(yp + 16);
      u32x4 o;
#pragma unroll
      for (int e = 0; e < 4; ++e) {
        const float ya = e < 2 ? y0[2 * e] : y1[2 * e - 4], yc = e < 2 ? y0[2 * e + 1] : y1[2 * e - 3];
        o[e] = pack_bf16(lo16(xv[it][e]) + bias + ya * fs, hi16(xv[it][e]) + bias + yc * fs);
      }
      store_dev(ws, OFF_R + (unsigned)(me * 18 + (dp >> 2)) * 2048u + (dp & 1) * 1024 + (((dp >> 1) & 1) * 32 + sr) * 16, o);
    }
    WAIT_VM(0);
    BAR();
    if (wave == 0 && lane < 3) set_flag(ws, SY_T + (unsigned)(me * 3 + lane) * 64u + (me & 3) * 8, tag);
  }
}

extern "C" void kernel_launch(void* const* d_in, const int* in_sizes, int n_in, void* d_out, int out_size, void* d_ws, size_t ws_size,
                              hipStream_t stream) {
  (void)hipFuncSetAttribute((const void*)k_all, hipFuncAttributeMaxDynamicSharedMemorySize, 163840);
  k_all<<<256, 768, 163840, stream>>>((const u16*)d_in[0], (const u16*)d_in[1], (const u16*)d_in[2], (const char*)d_in[4], (const u16*)d_in[5],
                                      (const char*)d_in[6], (const u16*)d_in[7], (char*)d_ws, (u16*)d_out);
}
```

```cpp
#include <hip/hip_runtime.h>
#include <hip/hip_bf16.h>
#include <stdint.h>

#define DI __device__ __forceinline__
#define LAS __attribute__((address_space(3)))
typedef unsigned short u16;
typedef unsigned long long u64;
typedef __attribute__((ext_vector_type(2))) float f32x2;
typedef __attribute__((ext_vector_type(4))) float f32x4;
typedef __attribute__((ext_vector_type(16))) float f32x16;
typedef __attribute__((ext_vector_type(2))) _Float16 f16x2;
typedef __attribute__((ext_vector_type(8))) _Float16 f16x8;
typedef __attribute__((ext_vector_type(2))) __bf16 bf16x2;
typedef __attribute__((ext_vector_type(8))) __bf16 bf16x8;
typedef __attribute__((ext_vector_type(4))) short s16x4;
typedef __attribute__((ext_vector_type(2))) unsigned u32x2;
typedef __attribute__((ext_vector_type(4))) unsigned u32x4;
typedef __attribute__((address_space(1))) unsigned gu32;
typedef __attribute__((address_space(1))) u64 gu64;
#define RLX_AGENT __ATOMIC_RELAXED, __HIP_MEMORY_SCOPE_AGENT

constexpr int HF = 2;
constexpr int PF2T = 3;
constexpr int PF2C_ROWS = 316;
constexpr int NTF2 = 10;
constexpr unsigned CODE_WARM = 38912;
constexpr int POLL_SLEEP = 4;

constexpr int S_ = 2048, D_ = 576, F_ = 4608, NC_ = 289;

constexpr unsigned OFF_H    = 0;
constexpr unsigned OFF_WT   = OFF_H + 2359296;
constexpr unsigned OFF_Y288 = OFF_WT + 2367488;
constexpr unsigned OFF_WMAX = OFF_Y288 + 4096;
constexpr unsigned OFF_X1   = OFF_WMAX + 4096;
constexpr unsigned OFF_Y1   = OFF_X1 + 2359296;
constexpr unsigned OFF_U    = OFF_Y1 + 2359296;
constexpr unsigned OFF_SLAB = OFF_U + 5308416;
constexpr unsigned OFF_R    = OFF_SLAB + 9437184;
constexpr unsigned OFF_SYNC = OFF_R + 2359296;
constexpr unsigned SY_EPOCH = OFF_SYNC, SY_TMO = OFF_SYNC + 256, SY_A = OFF_SYNC + 2048, SY_Y = OFF_SYNC + 4096, SY_U = OFF_SYNC + 6144,
                   SY_H = SY_U + 2 * 2048, SY_T = SY_H + 8 * 2048;
constexpr unsigned WS_END   = SY_T + 192 * 64;
constexpr int TAB_E1 = 0, TAB_EO = 6144, TAB_EOI = 12288, TAB_TW1 = 18432, TAB_TW2 = 34816, TAB_BYTES = 51200;

constexpr double cx_sincos(int n, int N, bool want_sin) {
  n %= N; if (n < 0) n += N;
  int m = 8 * n, oct = m / N; m -= oct * N;
  bool s = want_sin; double sign = 1.0;
  if (oct & 1) m = N - m;
  const int q = ((oct + 1) >> 1) & 3;
  double sg = (oct & 1) ? -1.0 : 1.0;
  bool use_sin = false;
  if (s) { use_sin = (q & 1) == 0; sign = (q == 2 || q == 3) ? -1.0 : 1.0; }
  else { use_sin = (q & 1) == 1; sign = (q == 1 || q == 2) ? -1.0 : 1.0; }
  const double r = 0.78539816339744830962 * (double)m / (double)N, r2 = r * r;
  double t = use_sin ? r : 1.0, sum = t;
  for (int k = use_sin ? 2 : 1; k < 22; k += 2) { t *= -r2 / (double)(k * (k + 1)); sum += t; }
  return sign * (use_sin ? sg * sum : sum);
}
constexpr u16 cx_half(double v) {
  const u16 sg = v < 0 ? 0x8000 : 0; if (v < 0) v = -v;
  if (v < 2.9802322387695312e-08) return sg;
  int e = 1; double p = 2.0;
  while (v < p && e > -14) { p *= 0.5; --e; }
  const double x = v / p * 1024.0;
  int i = (int)x; const double fr = x - i;
  if (fr > 0.5 || (fr == 0.5 && (i & 1))) ++i;
  return (u16)(sg | (v < p ? i : ((e + 15) << 10) + (i - 1024)));
}
struct CxBase { double c576[576], s576[576]; float c2k[2048], s2k[2048]; };
constexpr CxBase cx_base() {
  CxBase b{};
  for (int i = 0; i < 576; ++i) { b.c576[i] = cx_sincos(i, 576, false); b.s576[i] = cx_sincos(i, 576, true); }
  for (int i = 0; i < 2048; ++i) { b.c2k[i] = (float)cx_sincos(i, 2048, false); b.s2k[i] = (float)cx_sincos(i, 2048, true); }
  return b;
}
constexpr CxBase CXB = cx_base();
constexpr double cx_cs(int which, int n576) { return which == 0 ? CXB.c576[n576 % 576] : (which == 1 ? -CXB.s576[n576 % 576] : CXB.s576[n576 % 576]); }
constexpr int cx_kap(int t, int h, int j) { return 16 * t + 8 * (j >> 2) + 4 * h + (j & 3); }
constexpr int cx_row(int rho, int h) { return (rho & 3) + 8 * (rho >> 2) + 4 * h; }
template <int NH, int NF> struct alignas(1024) CxTab { u16 h[NH * 512]; float f[NF * 256]; };
template <class T> constexpr void cx_e32(T& o, int base) {
  for (int fr = 0; fr < 6; ++fr) for (int l = 0; l < 64; ++l) for (int j = 0; j < 8; ++j)
    o.h[((base + fr) * 64 + l) * 8 + j] = cx_half(cx_cs(fr % 3, 18 * ((cx_kap(fr / 3, l >> 5, j) * (l & 31)) & 31)));
}
constexpr CxTab<12, 8> cx_fwd() {
  CxTab<12, 8> o{};
  for (int fr = 0; fr < 6; ++fr) for (int l = 0; l < 64; ++l) for (int j = 0; j < 8; ++j) {
    const int ks = fr >> 1, hh = l >> 5, r = (l & 31) >> 4, k1 = l & 15;
    const int row = ks < 2 ? ks : ((hh == 0 && j < 4) ? (j >> 1) : 2), d1 = ks < 2 ? cx_kap(0, hh, j) : 16 + (j & 1);
    o.h[(fr * 64 + l) * 8 + j] = (row == r && k1 < 10) ? cx_half(cx_cs(fr & 1, 32 * ((d1 * k1) % 18))) : (u16)0;
  }
  cx_e32(o, 6);
  for (int p = 0; p < 8; ++p) for (int l = 0; l < 64; ++l) for (int y = 0; y < 2; ++y) {
    const int n = cx_row(2 * p + y, l >> 5) * (l & 15);
    o.f[(p * 64 + l) * 4 + y] = (float)CXB.c576[n]; o.f[(p * 64 + l) * 4 + 2 + y] = (float)-CXB.s576[n];
  }
  return o;
}
constexpr CxTab<18, 32> cx_col() {
  CxTab<18, 32> o{};
  cx_e32(o, 0);
  for (int fr = 0; fr < 12; ++fr) for (int l = 0; l < 64; ++l) for (int j = 0; j < 8; ++j) {
    const int f2 = fr % 6, r = l & 31, kap = cx_kap(f2 / 3, l >> 5, j);
    o.h[((6 + fr) * 64 + l) * 8 + j] = cx_half(cx_cs(f2 % 3, 9 * ((fr >= 6 ? kap * (2 * r + 1) : r * (2 * kap + 1)) & 63)));
  }
  for (int q = 0; q < 32; ++q) for (int l = 0; l < 64; ++l) for (int z = 0; z < 2; ++z) {
    const bool inv = q >= 16;
    const int T = (q >> 3) & 1, r = l & 31, cr = cx_row(2 * (q & 7) + z, l >> 5), n = (inv ? (2 * r + T) * cr : (2 * cr + T) * r) & 2047;
    o.f[(q * 64 + l) * 4 + z] = CXB.c2k[n] * (inv ? 0.125f : 1.0f); o.f[(q * 64 + l) * 4 + 2 + z] = CXB.s2k[n] * (inv ? 0.125f : -1.0f);
  }
  return o;
}
constexpr CxTab<8, 8> cx_inv() {
  CxTab<8, 8> o{};
  for (int fr = 0; fr < 2; ++fr) for (int l = 0; l < 64; ++l) for (int j = 0; j < 8; ++j) {
    const int r = l & 31, k1 = cx_kap(0, l >> 5, j);
    o.h[(fr * 64 + l) * 8 + j] = (k1 < 10 && r < 18) ? cx_half(cx_cs(fr, 32 * ((r * k1) % 18)) * ((k1 == 0 || k1 == 9) ? 1.0 : 2.0)) : (u16)0;
  }
  cx_e32(o, 2);
  for (int p = 0; p < 8; ++p) for (int l = 0; l < 64; ++l) for (int y = 0; y < 2; ++y) {
    const int n = (cx_row(2 * p + y, l >> 5) & 15) * (l & 31);
    o.f[(p * 64 + l) * 4 + y] = (float)(CXB.c576[n] * 0.125); o.f[(p * 64 + l) * 4 + 2 + y] = (float)(CXB.s576[n] * 0.125);
  }
  return o;
}
__device__ const CxTab<12, 8> T_FWD = cx_fwd();
__device__ const CxTab<18, 32> T_COL = cx_col();
__device__ const CxTab<8, 8> T_INV = cx_inv();

DI unsigned pack_bf16(float a, float b) { f32x2 v = {a, b}; return __builtin_bit_cast(unsigned, __builtin_convertvector(v, bf16x2)); }
DI unsigned pack_f16(float a, float b) { f32x2 v = {a, b}; return __builtin_bit_cast(unsigned, __builtin_convertvector(v, f16x2)); }
DI float lo16(unsigned p) { return __uint_as_float(p << 16); }
DI float hi16(unsigned p) { return __uint_as_float(p & 0xffff0000u); }
DI float rbf(float a) { return lo16(pack_bf16(a, a)); }

template <int CTRL> DI float dpp_add(float v) {
  return v + __int_as_float(__builtin_amdgcn_update_dpp(0, __float_as_int(v), CTRL, 0xf, 0xf, true));
}
DI float sum32(float v) {
  v = dpp_add<0xB1>(v); v = dpp_add<0x4E>(v); v = dpp_add<0x141>(v); v = dpp_add<0x140>(v);
  auto r = __builtin_amdgcn_permlane16_swap(__float_as_uint(v), __float_as_uint(v), false, false);
  return __uint_as_float(r[0]) + __uint_as_float(r[1]);
}

#define WAIT_VM(n) asm volatile("s_waitcnt vmcnt(%0)" ::"n"(n) : "memory")
#define WAIT_LGKM0() asm volatile("s_waitcnt lgkmcnt(0)" ::: "memory")
#define BAR() do { asm volatile("" ::: "memory"); __builtin_amdgcn_s_barrier(); asm volatile("" ::: "memory"); } while (0)
#define SCHED0() __builtin_amdgcn_sched_barrier(0)

DI u32x4 mk_rsrc(const void* p, unsigned bytes) {
  const u64 a = (u64)p;
  return u32x4{(unsigned)a, (unsigned)(a >> 32) & 0xffffu, bytes, 0x00020000u};
}
template <int POL> DI void dma16(u32x4 rsrc, unsigned lds_off, unsigned voff, unsigned soff) {
  if constexpr (POL == 0) asm volatile("s_mov_b32 m0, %0\n\ts_nop 0\n\tbuffer_load_dwordx4 %1, %2, %3 offen lds" ::"s"(lds_off), "v"(voff), "s"(rsrc), "s"(soff) : "memory");
  if constexpr (POL == 1) asm volatile("s_mov_b32 m0, %0\n\ts_nop 0\n\tbuffer_load_dwordx4 %1, %2, %3 offen nt lds" ::"s"(lds_off), "v"(voff), "s"(rsrc), "s"(soff) : "memory");
  if constexpr (POL == 2) asm volatile("s_mov_b32 m0, %0\n\ts_nop 0\n\tbuffer_load_dwordx4 %1, %2, %3 offen sc1 lds" ::"s"(lds_off), "v"(voff), "s"(rsrc), "s"(soff) : "memory");
}
DI void dma4(u32x4 rsrc, unsigned lds_off, unsigned voff) {
  asm volatile("s_mov_b32 m0, %0\n\ts_nop 0\n\tbuffer_load_dword %1, %2, 0 offen lds" ::"s"(lds_off), "v"(voff), "s"(rsrc) : "memory");
}
DI void store_dev(char* ws, unsigned off, u32x4 v) {
  __builtin_amdgcn_raw_buffer_store_b128(v, __builtin_amdgcn_make_buffer_rsrc((void*)ws, 0, WS_END, 0x00020000), off, 0, 16);
}
DI u32x4 load_dev(const char* ws, unsigned off) {
  return __builtin_amdgcn_raw_buffer_load_b128(__builtin_amdgcn_make_buffer_rsrc((void*)ws, 0, WS_END, 0x00020000), off, 0, 16);
}
DI void set_flag(char* ws, unsigned off, u64 tag) { __hip_atomic_store((gu64*)(ws + off), tag, RLX_AGENT); }
template <int N, int SLP = POLL_SLEEP> DI void poll_flags(char* ws, unsigned off, bool active, u64 tag) {
  for (unsigned spins = 0;; ++spins) {
    bool ok = true;
#pragma unroll
    for (int i = 0; i < N; ++i) { const u64 v = __hip_atomic_load((gu64*)(ws + off + i * 512), RLX_AGENT); ok &= (v == tag) || !active; }
    if (__all(ok)) break;
    __builtin_amdgcn_s_sleep(SLP);
    if (spins > (1u << 17)) { set_flag(ws, SY_TMO, tag); break; }
    if ((spins & 63u) == 63u && __all(__hip_atomic_load((gu64*)(ws + SY_TMO), RLX_AGENT) == tag)) break;
  }
  __builtin_amdgcn_fence(__ATOMIC_ACQUIRE, "wavefront");
}
DI unsigned w1_line(int i, int total, int r0, int nl, unsigned c0) {
  const int rr = i / nl, l = i - rr * nl, row = r0 + rr;
  return i < total ? (unsigned)(64 * (row & 31) + (row >> 5)) * (F_ * 2) + c0 + (unsigned)l * 128u : 0xfffffff0u;
}

DI unsigned w2_line(int line, int g) {
  int row = (line >> 4) + (g & 1) * 576; if (row >= 1152) row -= 1152;
  return (unsigned)((g >> 1) * 1152 + row) * 4096u + (g & 1) * 2048u + (line & 15) * 128u;
}

template <bool F16> DI f32x16 mfma32(u32x4 a, u32x4 b, f32x16 c) {
  if constexpr (F16) return __builtin_amdgcn_mfma_f32_32x32x16_f16(__builtin_bit_cast(f16x8, a), __builtin_bit_cast(f16x8, b), c, 0, 0, 0);
  else return __builtin_amdgcn_mfma_f32_32x32x16_bf16(__builtin_bit_cast(bf16x8, a), __builtin_bit_cast(bf16x8, b), c, 0, 0, 0);
}
DI u32x4 pack8_f16(const f32x16& v, int u) {
  return u32x4{pack_f16(v[8 * u], v[8 * u + 1]), pack_f16(v[8 * u + 2], v[8 * u + 3]), pack_f16(v[8 * u + 4], v[8 * u + 5]), pack_f16(v[8 * u + 6], v[8 * u + 7])};
}
DI u32x4 tr_frag(const char* p0, const char* p1) {
  s16x4 lo = __builtin_amdgcn_ds_read_tr16_b64_v4i16((LAS s16x4*)p0);
  s16x4 hi = __builtin_amdgcn_ds_read_tr16_b64_v4i16((LAS s16x4*)p1);
  u32x2 a = __builtin_bit_cast(u32x2, lo), c = __builtin_bit_cast(u32x2, hi);
  return u32x4{a[0], a[1], c[0], c[1]};
}
DI void tw_piece(f32x16& re, f32x16& im, int p, const f32x4 w) {
  const f32x2 r = {re[2 * p], re[2 * p + 1]}, i = {im[2 * p], im[2 * p + 1]}, wr = {w[0], w[1]}, wi = {w[2], w[3]};
  const f32x2 a = __builtin_elementwise_fma(r, wr, -(i * wi)), b = __builtin_elementwise_fma(r, wi, i * wr);
  re[2 * p] = a[0]; re[2 * p + 1] = a[1]; im[2 * p] = b[0]; im[2 * p + 1] = b[1];
}
DI void tw_load(f32x4 (&t)[8], const char* tw) {
#pragma unroll
  for (int p = 0; p < 8; ++p) t[p] = *(const f32x4*)(tw + p * 1024);
}
DI void tw_apply(f32x16& re, f32x16& im, const f32x4 (&t)[8]) {
#pragma unroll
  for (int p = 0; p < 8; ++p) tw_piece(re, im, p, t[p]);
}
enum { LIN = 0, TR128 = 1, TR64 = 2 };

template <int V> struct IC { static constexpr int value = V; };
template <int NA_W, int NB_W, int WA, int WB, int AK, int BK, int NS, int DPM, int DEPTH>
struct GC {
  static constexpr int NC = WA * WB, NFA = NA_W * WA, NFB = NB_W * WB;
  static constexpr int A_BYTES = NFA * 4096, B_BYTES = NFB * 4096, STAGE = A_BYTES + B_BYTES;
  static constexpr int LDS_BYTES = NS * STAGE;
  static_assert(AK == LIN || NFA == (AK == TR128 ? 4 : 2), "");
  static_assert(BK == LIN || NFB == (BK == TR128 ? 4 : 2), "");

  template <int K> static DI unsigned voff(int lane, unsigned rstride) {
    if constexpr (K == LIN) return lane * 16;
    else if constexpr (K == TR128) return (unsigned)(lane >> 4) * rstride + (unsigned)(((lane & 15) ^ ((lane >> 4) << 2)) * 16);
    else return (unsigned)(lane >> 3) * rstride + (unsigned)(((lane & 7) ^ (((lane >> 4) & 1) << 2)) * 16);
  }
  template <int K, int NF> static DI void frag_addr(unsigned (&addr)[K == LIN ? 1 : NF], int opoff, int wpos, int lane) {
    if constexpr (K == LIN) addr[0] = opoff + wpos * NF * 4096 + lane * 16;
    else {
      const int h = lane >> 5, blk = (lane >> 4) & 1, q = (lane & 15) >> 2, p = lane & 3;
#pragma unroll
      for (int i = 0; i < NF; ++i) {
        const int ch = 4 * (wpos * NF + i) + 2 * blk + (p >> 1);
        if constexpr (K == TR128) addr[i] = opoff + (4 * h + q) * 256 + ((ch ^ (q << 2)) * 16) + 8 * (p & 1);
        else addr[i] = opoff + (4 * h + q) * 128 + ((ch ^ (((q >> 1) & 1) << 2)) * 16) + 8 * (p & 1);
      }
    }
  }
  template <int K, int NF> static DI void rd(u32x4 (&f)[NF], const char* smem, const unsigned (&addr)[K == LIN ? 1 : NF], int sb, int hh) {
#pragma unroll
    for (int i = 0; i < NF; ++i) {
      if constexpr (K == LIN) f[i] = *(const u32x4*)(smem + addr[0] + sb + (i * 4 + hh) * 1024);
      else {
        constexpr int RB = K == TR128 ? 256 : 128;
        const char* p = smem + addr[i] + sb + hh * 16 * RB;
        s16x4 lo = __builtin_amdgcn_ds_read_tr16_b64_v4i16((LAS s16x4*)p);
        s16x4 hi = __builtin_amdgcn_ds_read_tr16_b64_v4i16((LAS s16x4*)(p + 8 * RB));
        u32x2 a = __builtin_bit_cast(u32x2, lo), c = __builtin_bit_cast(u32x2, hi);
        f[i] = u32x4{a[0], a[1], c[0], c[1]};
      }
    }
  }
  static DI void ilv() {
    constexpr int NR = NA_W * (AK == LIN ? 1 : 2) + NB_W * (BK == LIN ? 1 : 2), NM = NA_W * NB_W;
    constexpr int NG = (NR + DPM - 1) / DPM < NM ? (NR + DPM - 1) / DPM : NM;
#pragma unroll
    for (int i = 0; i < NG; ++i) {
      __builtin_amdgcn_sched_group_barrier(0x8, 1, 0);
      if (i + 1 < NG) __builtin_amdgcn_sched_group_barrier(0x100, DPM, 0);
      else __builtin_amdgcn_sched_group_barrier(0x100, NR - DPM * (NG - 1), 0);
    }
    if constexpr (NM > NG) __builtin_amdgcn_sched_group_barrier(0x8, NM - NG, 0);
    SCHED0();
  }
  template <int G, class ISSUE> static DI void ring(int nt, ISSUE issue) {
    int wslot = NS - 2;
    for (int tau = 0; tau < nt - (NS - 2); ++tau) {
      WAIT_VM((NS - 3) * G);
      BAR();
      issue(tau + NS - 2, wslot * STAGE);
      wslot = (wslot + 1 == NS) ? 0 : wslot + 1;
    }
    if constexpr (NS >= 5) { WAIT_VM(2 * G); BAR(); }
    if constexpr (NS >= 4) { WAIT_VM(G); BAR(); }
    WAIT_VM(0); BAR();
    static_assert(NS >= 3 && NS <= 5, "");
  }
  template <bool F16>
  static DI void consume(const char* smem, int wave, int lane, int n, int& rslot, f32x16 (&acc)[NA_W][NB_W]) {
    const int wa = wave / WB, wb = wave - wa * WB;
    unsigned addrA[AK == LIN ? 1 : NA_W], addrB[BK == LIN ? 1 : NB_W];
    frag_addr<AK, NA_W>(addrA, 0, wa, lane);
    frag_addr<BK, NB_W>(addrB, A_BYTES, wb, lane);
    u32x4 fa[DEPTH][NA_W], fb[DEPTH][NB_W];
#pragma unroll
    for (int d = 1; d < DEPTH; ++d) {
#pragma unroll
      for (int i = 0; i < NA_W; ++i) fa[d][i] = u32x4{0, 0, 0, 0};
#pragma unroll
      for (int i = 0; i < NB_W; ++i) fb[d][i] = u32x4{0, 0, 0, 0};
    }
    auto mm = [&](const u32x4 (&a)[NA_W], const u32x4 (&b)[NB_W]) {
#pragma unroll
      for (int i = 0; i < NA_W; ++i)
#pragma unroll
        for (int j = 0; j < NB_W; ++j) acc[i][j] = mfma32<F16>(a[i], b[j], acc[i][j]);
    };
    auto step = [&](auto Z0) {
      constexpr int z0 = decltype(Z0)::value;
      BAR();
      SCHED0();
      const int sb = rslot * STAGE;
#pragma unroll
      for (int hh = 0; hh < 4; ++hh) {
        rd<AK, NA_W>(fa[(z0 + hh) % DEPTH], smem, addrA, sb, hh);
        rd<BK, NB_W>(fb[(z0 + hh) % DEPTH], smem, addrB, sb, hh);
        mm(fa[(z0 + hh + 1) % DEPTH], fb[(z0 + hh + 1) % DEPTH]);
        ilv();
      }
      rslot = (rslot + 1 == NS) ? 0 : rslot + 1;
    };
    if constexpr (DEPTH == 2) {
      for (int tau = 0; tau < n; ++tau) step(IC<0>{});
      mm(fa[1], fb[1]);
    } else {
      int tau = 0;
      for (; tau + 3 <= n; tau += 3) { step(IC<0>{}); step(IC<1>{}); step(IC<2>{}); }
      if (n - tau == 2) { step(IC<0>{}); step(IC<1>{}); mm(fa[0], fb[0]); mm(fa[1], fb[1]); }
      else if (n - tau == 1) { step(IC<0>{}); mm(fa[2], fb[2]); mm(fa[0], fb[0]); }
      else { mm(fa[1], fb[1]); mm(fa[2], fb[2]); }
    }
  }
};

DI float gelu_tanh(float u) {
  const float z = u * (1.0f + 0.044715f * u * u) * (-2.0f * 0.7978845608028654f * 1.4426950408889634f);
  return u * __builtin_amdgcn_rcpf(1.0f + __builtin_amdgcn_exp2f(z));
}

template <int NV> DI void mv_ilv() {
#pragma unroll
  for (int i = 0; i < 8; ++i) { __builtin_amdgcn_sched_group_barrier(0x8, 1, 0); __builtin_amdgcn_sched_group_barrier(0x2, NV, 0); }
  SCHED0();
}
struct EF { u32x4 r, i, n; };
DI EF ef_load(const char* p) { EF e; e.r = *(const u32x4*)p; e.i = *(const u32x4*)(p + 1024); e.n = *(const u32x4*)(p + 2048); return e; }
#define MF(a, b, c) mfma32<true>(a, b, c)
DI void cm_dA(f32x16& re, f32x16& im, u32x4 dr, u32x4 di, const EF& e) { re = MF(dr, e.r, re); im = MF(dr, e.i, im); re = MF(di, e.n, re); im = MF(di, e.r, im); }
DI void cm_dB(f32x16& re, f32x16& im, u32x4 dr, u32x4 di, const EF& e) { re = MF(e.r, dr, re); im = MF(e.i, dr, im); re = MF(e.n, di, re); im = MF(e.r, di, im); }
DI void cm_dAc(f32x16& re, f32x16& im, u32x4 dr, u32x4 di, const EF& e) { re = MF(dr, e.r, re); im = MF(dr, e.n, im); re = MF(di, e.i, re); im = MF(di, e.r, im); }
DI void wmul(f32x16& re, f32x16& im, const u32x4 (&wv)[4], float wsc) {
#pragma unroll
  for (int p = 0; p < 8; ++p) {
    const unsigned w0 = wv[p >> 1][(2 * p) & 3], w1 = wv[p >> 1][(2 * p + 1) & 3];
    tw_piece(re, im, p, f32x4{lo16(w0), lo16(w1), hi16(w0), hi16(w1)} * wsc);
  }
}
DI void bfly(f32x16& a, f32x16& b) {
  const f32x16 x = a, y = b; a = x + y; b = x - y;
}
DI void colfft_wave(const char* tab  , const char* ws, int c, int lane, float wsc, const u32x4 (&zr)[2][2], const u32x4 (&zi)[2][2],
                    char* img, int slot_re, int slot_im) {
  f32x4 tw[8];
  f32x16 r0 = {}, i0 = {}, r1 = {}, i1 = {};
  u32x4 ar[2], ai[2];
  EF ea = ef_load(tab + TAB_E1), eb = ef_load(tab + TAB_E1 + 3072);
  tw_load(tw, tab + TAB_TW1);
  SCHED0();
  cm_dA(r0, i0, zr[0][0], zi[0][0], ea); cm_dA(r0, i0, zr[0][1], zi[0][1], eb);
  SCHED0();
  cm_dA(r1, i1, zr[1][0], zi[1][0], ea); cm_dA(r1, i1, zr[1][1], zi[1][1], eb);
#pragma unroll
  for (int p = 0; p < 8; ++p) { tw_piece(r0, i0, p, tw[p]); tw[p] = *(const f32x4*)(tab + TAB_TW1 + 8192 + p * 1024); }
  ar[0] = pack8_f16(r0, 0); ar[1] = pack8_f16(r0, 1); ai[0] = pack8_f16(i0, 0); ai[1] = pack8_f16(i0, 1);
  mv_ilv<6>();
  u32x4 wv0[4], wv1[4];
#pragma unroll
  for (int i = 0; i < 4; ++i) wv0[i] = load_dev(ws, OFF_WT + c * 8192u + i * 1024 + lane * 16);
  r0 = f32x16{}; i0 = f32x16{};
  cm_dB(r0, i0, ar[0], ai[0], ea); ea = ef_load(tab + TAB_EO);
  cm_dB(r0, i0, ar[1], ai[1], eb); eb = ef_load(tab + TAB_EO + 3072);
  tw_apply(r1, i1, tw);
  ar[0] = pack8_f16(r1, 0); ar[1] = pack8_f16(r1, 1); ai[0] = pack8_f16(i1, 0); ai[1] = pack8_f16(i1, 1);
  mv_ilv<6>();
#pragma unroll
  for (int i = 0; i < 4; ++i) wv1[i] = load_dev(ws, OFF_WT + c * 8192u + (4 + i) * 1024 + lane * 16);
  r1 = f32x16{}; i1 = f32x16{};
  cm_dB(r1, i1, ar[0], ai[0], ea); ea = ef_load(tab + TAB_E1);
  cm_dB(r1, i1, ar[1], ai[1], eb); eb = ef_load(tab + TAB_E1 + 3072);
  SCHED0();
  bfly(r0, r1); bfly(i0, i1);
  wmul(r0, i0, wv0, wsc);
  wmul(r1, i1, wv1, wsc);
  bfly(r0, r1); bfly(i0, i1);
  u32x4 dr[2], di[2];
  ar[0] = pack8_f16(r0, 0); ar[1] = pack8_f16(r0, 1); ai[0] = pack8_f16(i0, 0); ai[1] = pack8_f16(i0, 1);
  SCHED0();
  tw_load(tw, tab + TAB_TW2);
  r0 = f32x16{}; i0 = f32x16{};
  cm_dAc(r0, i0, ar[0], ai[0], ea); ea = ef_load(tab + TAB_EOI);
  cm_dAc(r0, i0, ar[1], ai[1], eb); eb = ef_load(tab + TAB_EOI + 3072);
  dr[0] = pack8_f16(r1, 0); dr[1] = pack8_f16(r1, 1); di[0] = pack8_f16(i1, 0); di[1] = pack8_f16(i1, 1);
  mv_ilv<2>();
  r1 = f32x16{}; i1 = f32x16{};
  cm_dAc(r1, i1, dr[0], di[0], ea); ea = ef_load(tab + TAB_E1);
  cm_dAc(r1, i1, dr[1], di[1], eb); eb = ef_load(tab + TAB_E1 + 3072);
#pragma unroll
  for (int p = 0; p < 8; ++p) { tw_piece(r0, i0, p, tw[p]); tw[p] = *(const f32x4*)(tab + TAB_TW2 + 8192 + p * 1024); }
  ar[0] = pack8_f16(r0, 0); ar[1] = pack8_f16(r0, 1); ai[0] = pack8_f16(i0, 0); ai[1] = pack8_f16(i0, 1);
  mv_ilv<6>();
  r0 = f32x16{}; i0 = f32x16{};
  cm_dAc(r0, i0, ar[0], ai[0], ea); cm_dAc(r0, i0, ar[1], ai[1], eb);
  tw_apply(r1, i1, tw);
  dr[0] = pack8_f16(r1, 0); dr[1] = pack8_f16(r1, 1); di[0] = pack8_f16(i1, 0); di[1] = pack8_f16(i1, 1);
  mv_ilv<6>();
  r1 = f32x16{}; i1 = f32x16{};
  cm_dAc(r1, i1, dr[0], di[0], ea); cm_dAc(r1, i1, dr[1], di[1], eb);
  SCHED0();
#pragma unroll
  for (int g = 0; g < 4; ++g) {
    const int Q = (lane & 31) * 8 + 2 * g + (lane >> 5), sw = lane & 15, r = 4 * g;
    if (slot_im == -2) {
      *(u32x4*)(img + Q * 16) = u32x4{pack_f16(r0[r], r1[r]), pack_f16(r0[r + 1], r1[r + 1]), pack_f16(r0[r + 2], r1[r + 2]), pack_f16(r0[r + 3], r1[r + 3])};
      continue;
    }
#pragma unroll
    for (int z = 0; z < 2; ++z) {
      const int q = r + 2 * z;
      *(u32x4*)(img + Q * 256 + (((slot_re + z) ^ sw) * 16)) =
          u32x4{pack_f16(r0[q], i0[q]), pack_f16(r1[q], i1[q]), pack_f16(r0[q + 1], i0[q + 1]), pack_f16(r1[q + 1], i1[q + 1])};
    }
  }
}
#undef MF
constexpr int FIN_OS = 2336;
DI void irfft_wave(const char* rec, const char* y288, int w, int lane, char* orow, const char* kt) {
  const int row = lane & 31, hh = lane >> 5, k1 = row & 15, rr = 2 * w + (row >> 4);
  const bool ok = k1 < 10;
  const int k1c = ok ? k1 : 0;
  const char* pd = rec + rr * 4 + 32 * (k1c + 72 * hh);
  const char* pm = rec + rr * 4 + 32 * (576 - k1c - 72 * hh - 18 * 27);
  unsigned v[2][8];
#pragma unroll
  for (int j = 0; j < 8; ++j) {
    v[0][j] = *(const unsigned*)(pd + 576 * (8 * (j >> 2) + (j & 3)));
    v[1][j] = *(const unsigned*)(pm + 576 * (27 - (16 + 8 * (j >> 2) + (j & 3))));
  }
  const unsigned v288 = *(const u16*)(y288 + rr * 2);
  f32x4 tw[8];
  tw_load(tw, kt + 8192);
  if (k1 == 0 && hh == 0) { v[0][0] &= 0xffffu; v[1][0] = v288; }
  f32x16 re = {}, im = {};
#pragma unroll
  for (int u = 0; u < 2; ++u) {
    u32x4 a_r, a_i;
#pragma unroll
    for (int e = 0; e < 4; ++e) {
      a_r[e] = ok ? __builtin_amdgcn_perm(v[u][2 * e + 1], v[u][2 * e], 0x05040100u) : 0u;
      a_i[e] = ok ? (__builtin_amdgcn_perm(v[u][2 * e + 1], v[u][2 * e], 0x07060302u) ^ (u ? 0x80008000u : 0u)) : 0u;
    }
    cm_dAc(re, im, a_r, a_i, ef_load(kt + 2048 + u * 3072));
  }
  const u32x4 cr = *(const u32x4*)kt, ci = *(const u32x4*)(kt + 1024);
  tw_apply(re, im, tw);
#pragma unroll
  for (int u = 0; u < 2; ++u) {
    f32x16 y = {};
    y = mfma32<true>(cr, pack8_f16(re, u), y);
    y = mfma32<true>(ci, pack8_f16(im, u), y);
    float* o = (float*)(orow + u * FIN_OS) + (lane & 31) + 128 * hh;
#pragma unroll
    for (int rho = 0; rho < 8; ++rho) o[32 * ((rho & 3) + 8 * (rho >> 2))] = y[rho];
    if (hh == 0) { o[32 * 16] = y[8]; o[32 * 17] = y[9]; }
  }
}

DI unsigned wmax_exp(const char* ws, int lane) {
  const u32x4 mv = load_dev(ws, OFF_WMAX + lane * 16);
  unsigned mx = max(max(mv[0], mv[1]), max(mv[2], mv[3]));
#pragma unroll
  for (int o = 32; o; o >>= 1) mx = max(mx, (unsigned)__shfl_xor((int)mx, o));
  return min(mx >> 7, 252u);
}

DI unsigned lds_arrive(char* p) { return __hip_atomic_fetch_add((LAS unsigned*)p, 1u, __ATOMIC_RELAXED, __HIP_MEMORY_SCOPE_WORKGROUP); }
DI void lds_arrive_wait(char* p, unsigned n, int lane) {
  if (lane == 0) lds_arrive(p);
  while (__hip_atomic_load((LAS unsigned*)p, __ATOMIC_RELAXED, __HIP_MEMORY_SCOPE_WORKGROUP) < n) __builtin_amdgcn_s_sleep(1);
  asm volatile("" ::: "memory");
}
DI void lds_wait(const char* p) {
  while (__hip_atomic_load((const LAS unsigned*)p, __ATOMIC_RELAXED, __HIP_MEMORY_SCOPE_WORKGROUP) == 0u) __builtin_amdgcn_s_sleep(1);
  asm volatile("" ::: "memory");
}
using CG1 = GC<1, 3, 4, 1, TR128, LIN, 5, 2, 3>;
using CG2 = GC<3, 2, 2, 2, LIN, TR128, 4, 2, 2>;
constexpr int P1_RS = 1216, P1_XS = 3 * CG1::STAGE, P1_HS = P1_XS + 8 * P1_RS, P1_C1 = P1_XS + 16 * P1_RS, P1_E = P1_C1 + 6144,
              P1_TW = P1_E + 6144, P1_O = P1_TW + 8192, P1_WT = P1_O + 9216, P1_RED = P1_WT + 10512, P1_DUMMY = 147456;
static_assert(P1_RED + 48 <= P1_DUMMY && P1_DUMMY >= CG1::LDS_BYTES, "");

__global__ __launch_bounds__(768, 3) void k_all(const u16* __restrict__ x, const u16* __restrict__ wr, const u16* __restrict__ wi,
                                                const char* __restrict__ w1, const u16* __restrict__ b1, const char* __restrict__ w2,
                                                const u16* __restrict__ b2, char* __restrict__ ws, u16* __restrict__ out) {
  extern __shared__ __attribute__((aligned(16))) char smem[];
  const int lane = threadIdx.x & 63, wave = __builtin_amdgcn_readfirstlane(threadIdx.x >> 6);
  const int b = blockIdx.x, g = b & 7, idx = b >> 3;
  const unsigned ldsb = (unsigned)(size_t)(LAS char*)smem;
  const u32x4 rs_ws = mk_rsrc(ws, WS_END), rs_w1 = mk_rsrc(w1, 18874368u), rs_w2 = mk_rsrc(w2, 18874368u);
  if (wave == 11 && idx < 6) {
    const u64 pc = __builtin_amdgcn_s_getpc() & ~(u64)127;
    dma4(mk_rsrc((const void*)pc, CODE_WARM), ldsb + P1_DUMMY + wave * 256, (unsigned)(idx * 64 + lane) * 128u);
  }
  const int tile = g * 27 + idx, ft = tile / 6, dt1 = tile - ft * 6;
  const int ft0 = (g * 27) / 6, nl1 = ((g * 27 + 26) / 6 - ft0 + 1) * 2;
  auto issue_w1x = [&](int lw, int kt, int so) {
    constexpr unsigned ld = F_ * 2;
    const unsigned vo = CG1::voff<TR128>(lane, 64 * ld);
#pragma unroll
    for (int m = 0; m < 4; ++m) {
      const int a = lw * 4 + m;
      dma16<0>(rs_w1, ldsb + so + a * 1024, vo, (unsigned)(((4 * a) & 31) * 64 + 2 * kt + (a >> 3)) * ld + ft * 256u);
    }
  };
  const int tb = idx < 27 ? (g >> 2) * 108 + idx * 4 + (g & 3) : 216 + (idx - 27) * 8 + g;
  auto gate = [&] {
    while (__hip_atomic_load((const LAS unsigned*)(smem + P1_RED + 16), __ATOMIC_RELAXED, __HIP_MEMORY_SCOPE_WORKGROUP) < 3u) __builtin_amdgcn_s_sleep(1);
  };
  u64 tag;
  if (__builtin_expect(wave < 4, 1)) {
    const int tid = threadIdx.x, s2 = tb >> 2, t = (tb >> 1) & 1, h = tb & 1;
    const int rr = tid >> 5, l32 = tid & 31;
    const int s1 = 16 * t + 8 * (rr >> 2) + 4 * h + (rr & 3);
    const unsigned* xr = (const unsigned*)(x + (size_t)(64 * s1 + s2) * D_);
    unsigned raw[9];
#pragma unroll
    for (int i = 0; i < 9; ++i) raw[i] = xr[32 * i + l32];
    float v[18];
#pragma unroll
    for (int i = 0; i < 9; ++i) { v[2 * i] = lo16(raw[i]); v[2 * i + 1] = hi16(raw[i]); }
#pragma unroll
    for (int pass = 0; pass < 2; ++pass) {
      float sm = 0.f;
#pragma unroll
      for (int i = 0; i < 18; ++i) sm += v[i];
      const float mean = sum32(sm) * (1.0f / 576.0f);
      float sq = 0.f;
#pragma unroll
      for (int i = 0; i < 18; ++i) { float dlt = v[i] - mean; sq += dlt * dlt; }
      const float var = sum32(sq) * (1.0f / 576.0f);
      const float mb = rbf(mean);
      const float sd = rbf(__builtin_sqrtf(rbf(rbf(var) + 1e-5f)));
      const float rs = 1.0f / sd;
      char* dst = smem + P1_XS + pass * (8 * P1_RS) + rr * P1_RS + l32 * 4;
#pragma unroll
      for (int i = 0; i < 9; ++i) {
        unsigned p = pack_bf16(v[2 * i] - mb, v[2 * i + 1] - mb);
        p = pack_bf16(lo16(p) * rs, hi16(p) * rs);
        v[2 * i] = lo16(p); v[2 * i + 1] = hi16(p);
        *(unsigned*)(dst + i * 128) = pass == 0 ? pack_f16(v[2 * i], v[2 * i + 1]) : p;
      }
    }
    WAIT_LGKM0();
    BAR();
    { const u64 tv = *(const u64*)(smem + P1_RED + 24);
      tag = (u64)__builtin_amdgcn_readfirstlane((unsigned)(tv >> 32)) << 32 | (unsigned)__builtin_amdgcn_readfirstlane((unsigned)tv); }
    {
      const int hh = lane >> 5, blk = (lane >> 4) & 1, q = (lane & 15) >> 2, p = lane & 3;
      const char* xa = smem + P1_XS + (2 * wave) * P1_RS + blk * 32 + p * 8;
      const char* cf = smem + P1_C1 + lane * 16;
      f32x16 re = {}, im = {};
      f32x4 tw[8];
      tw_load(tw, smem + P1_TW + lane * 16);
#pragma unroll
      for (int ks = 0; ks < 3; ++ks) {
        const char* a0 = ks < 2 ? xa + ks * P1_RS + (4 * hh + q) * 64 : xa + (q >> 1) * P1_RS + (16 + (q & 1)) * 64;
        const u32x4 a = tr_frag(a0, ks < 2 ? a0 + 512 : a0);
        re = mfma32<true>(a, *(const u32x4*)(cf + (2 * ks) * 1024), re);
        im = mfma32<true>(a, *(const u32x4*)(cf + (2 * ks + 1) * 1024), im);
      }
      tw_apply(re, im, tw);
      f32x16 xr_ = {}, xi_ = {};
#pragma unroll
      for (int u = 0; u < 2; ++u) cm_dB(xr_, xi_, pack8_f16(re, u), pack8_f16(im, u), ef_load(smem + P1_E + u * 3072 + lane * 16));
      const int col = lane & 31, k1 = col & 15, row2 = 2 * (2 * wave + (col >> 4));
      if (k1 < 10) {
        char* o1 = smem + P1_O + 32 * k1 + 576 * 4 * hh + row2;
        char* o2 = smem + P1_O + 32 * (576 - k1) - 576 * (4 * hh + 27) + row2;
#pragma unroll
        for (int rho = 0; rho < 8; ++rho) {
          const unsigned pk = pack_f16(xr_[rho] * (1.0f / 32.0f), xi_[rho] * (1.0f / 32.0f));
          char* d = o1 + 576 * ((rho & 3) + 8 * (rho >> 2));
          *(u16*)d = (u16)pk;
          if (rho != 0 || k1 != 0 || hh != 0) *(u16*)(d + 16) = (u16)(pk >> 16);
        }
        if (k1 >= 1 && k1 <= 8) {
#pragma unroll
          for (int rho = 8; rho < 16; ++rho) {
            const unsigned pk = pack_f16(xr_[rho] * (1.0f / 32.0f), xi_[rho] * (-1.0f / 32.0f));
            char* d = o2 + 576 * (27 - ((rho & 3) + 8 * (rho >> 2)));
            *(u16*)d = (u16)pk;
            *(u16*)(d + 16) = (u16)(pk >> 16);
          }
        } else if (k1 == 0 && hh == 0) {
          *(u16*)(smem + P1_O + 16 + row2) = (u16)pack_f16(xr_[8] * (1.0f / 32.0f), 0.f);
        }
      }
    }
    WAIT_LGKM0();
    lds_arrive_wait(smem + P1_RED + 32, 4u, lane);
    gate();
    const unsigned P = ((s2 & 1) * 2 + t) * 64 + h * 32 + (s2 >> 1);
#pragma unroll
    for (int it = 0; it < 3; ++it) {
      const int p = tid + it * 256;
      if (p < 576) store_dev(ws, OFF_X1 + P * 9216u + p * 16, *(const u32x4*)(smem + P1_O + p * 16));
    }
    WAIT_VM(0);
    if (lane == 0 && lds_arrive(smem + P1_RED + 36) == 7u) set_flag(ws, SY_A + b * 8, tag);
  } else if (wave < 8) {
    const int tid = threadIdx.x - 256;
    const int rg = b >> 2, cg = b & 3, c0 = cg * 72 + (cg > 0), nc = cg == 0 ? 73 : 72, kq = rg >> 2, ko = rg & 3;
    unsigned rv[10], iv[10];
#pragma unroll
    for (int it = 0; it < 10; ++it) {
      const int i = tid + it * 256, row = i / 73, col = i - row * 73;
      const bool ok = i < 32 * 73 && col < nc;
      const int off = ok ? (8 * ko + (row & 7) + 32 * (4 * kq + (row >> 3))) * NC_ + c0 + col : 0;
      rv[it] = wr[off]; iv[it] = wi[off];
    }
    BAR();
    { const u64 tv = *(const u64*)(smem + P1_RED + 24);
      tag = (u64)__builtin_amdgcn_readfirstlane((unsigned)(tv >> 32)) << 32 | (unsigned)__builtin_amdgcn_readfirstlane((unsigned)tv); }
    unsigned mx = 0;
#pragma unroll
    for (int it = 0; it < 10; ++it) {
      const int i = tid + it * 256, row = i / 73, col = i - row * 73;
      if (i < 32 * 73 && col < nc) {
        mx = max(mx, max(rv[it] & 0x7fffu, iv[it] & 0x7fffu));
        *(unsigned*)(smem + P1_WT + col * 144 + (row & 7) * 16 + (row >> 3) * 4) = rv[it] | (iv[it] << 16);
      }
    }
#pragma unroll
    for (int o = 32; o; o >>= 1) mx = max(mx, (unsigned)__shfl_xor((int)mx, o));
    unsigned* red = (unsigned*)(smem + P1_RED);
    if (lane == 0) red[wave - 4] = mx;
    WAIT_LGKM0();
    lds_arrive_wait(smem + P1_RED + 44, 4u, lane);
    if (tid == 0) __hip_atomic_store((gu32*)(ws + OFF_WMAX + b * 4), max(max(red[0], red[1]), max(red[2], red[3])), RLX_AGENT);
    const int U = kq >> 3, h = kq & 1, rq = (kq >> 1) & 3;
    gate();
#pragma unroll
    for (int it = 0; it < 3; ++it) {
      const int p = tid + it * 256;
      if (p < nc * 8) {
        const int col = p >> 3, a = p & 7;
        store_dev(ws, OFF_WT + (unsigned)(c0 + col) * 8192u + ((U * 4 + rq) * 64 + h * 32 + 8 * ko + a) * 16,
                  *(const u32x4*)(smem + P1_WT + col * 144 + a * 16));
      }
    }
    WAIT_VM(0);
    if (lane == 0 && lds_arrive(smem + P1_RED + 36) == 7u) set_flag(ws, SY_A + b * 8, tag);
    if (idx < 27) { issue_w1x(wave - 4, 0, 0); issue_w1x(wave - 4, 1, CG1::STAGE); issue_w1x(wave - 4, 2, 2 * CG1::STAGE); }
  } else {
    u64 ep = 0;
    if (threadIdx.x == 512) ep = __hip_atomic_load((gu64*)(ws + SY_EPOCH), RLX_AGENT);
    if (wave != 10) {
      const u32x4 rt = mk_rsrc(&T_FWD, 20480u);
      for (int fr = wave == 11 ? 2 : wave - 8; fr < 20; fr += 3) dma16<0>(rt, ldsb + P1_C1 + fr * 1024, lane * 16, fr * 1024u);
    } else {
      __builtin_amdgcn_s_sleep(32);
      if (idx >= 27) {
#pragma unroll
        for (int i = 0; i < 6; ++i)
          dma4(rs_w1, ldsb + P1_DUMMY + wave * 256, w1_line((HF * 27 + i * 5 + idx - 27) * 64 + lane, 2048 * nl1, 0, nl1, ft0 * 256u));
      } else {
#pragma unroll
        for (int i = 0; i < HF; ++i)
          dma4(rs_w1, ldsb + P1_DUMMY + wave * 256, w1_line((i * 27 + idx) * 64 + lane, 2048 * nl1, 0, nl1, ft0 * 256u));
      }
    }
    if (threadIdx.x == 512) {
      *(u64*)(smem + P1_RED + 24) = ep * 6364136223846793005ull + 1442695040888963407ull;
      *(unsigned*)(smem + P1_RED + 16) = 0u; *(u64*)(smem + P1_RED + 32) = 0ull; *(u64*)(smem + P1_RED + 40) = 0ull;
    }
    if (wave != 10) WAIT_VM(0);
    WAIT_LGKM0();
    BAR();
    { const u64 tv = *(const u64*)(smem + P1_RED + 24);
      tag = (u64)__builtin_amdgcn_readfirstlane((unsigned)(tv >> 32)) << 32 | (unsigned)__builtin_amdgcn_readfirstlane((unsigned)tv); }
    if (wave != 10) {
      const int s2 = tb >> 2, t = (tb >> 1) & 1, h = tb & 1;
      for (int cb = wave == 11 ? 2 : wave - 8; cb < 9; cb += 3) {
        const int gg = lane >> 4, q = (lane & 15) >> 2, pp = lane & 3;
        const char* src = smem + P1_HS + q * P1_RS + (cb * 64 + gg * 16 + pp * 4) * 2;
        const int df = cb * 2 + (gg >> 1), i = 8 * q + 4 * (gg & 1) + pp;
        store_dev(ws, OFF_H + (unsigned)(df * 128 + 2 * s2 + t) * 1024u + (h * 32 + i) * 16, tr_frag(src, src + 4 * P1_RS));
      }
      WAIT_VM(0);
      unsigned last = 0;
      if (lane == 0) last = lds_arrive(smem + P1_RED + 16);
      if (__builtin_amdgcn_readfirstlane(last) == 2u) { if (lane < 8) set_flag(ws, SY_H + lane * 2048 + tb * 8, tag); }
    } else if (idx >= 27) {
      for (int i0 = (HF * 27 + 30 + idx - 27) * 64; i0 < 2048 * nl1; i0 += 5 * 64) {
        dma4(rs_w1, ldsb + P1_DUMMY + wave * 256, w1_line(i0 + lane, 2048 * nl1, 0, nl1, ft0 * 256u));
        WAIT_VM(7);
      }
    }
  }
  if (idx < 27) {
    char* ebuf = smem + (29 % 5) * CG1::STAGE;
    u32x2 bv[3][2];
#pragma unroll
    for (int it = 0; it < 3; ++it) {
      const u32x4* bp = (const u32x4*)(b1 + ft * 128 + (min(wave + 11 * it, 23) / 3) * 16);
      const u32x4 b0 = bp[0], b1v = bp[1];
      const bool hi = lane >= 32;
      bv[it][0] = u32x2{hi ? b0[2] : b0[0], hi ? b0[3] : b0[1]}; bv[it][1] = u32x2{hi ? b1v[2] : b1v[0], hi ? b1v[3] : b1v[1]};
      asm volatile("" : "+v"(bv[it][0]), "+v"(bv[it][1]));
    }
    if (wave == 11) {
      const unsigned fh = SY_H + g * 2048 + lane * 8;
      poll_flags<1>(ws, fh, true, tag);
      if (lane == 0) lds_arrive(smem + P1_RED + 40);
      bool ok[3];
#pragma unroll
      for (int q = 0; q < 3; ++q) ok[q] = __all(__hip_atomic_load((gu64*)(ws + fh + (q + 1) * 512), RLX_AGENT) == tag);
      for (int tau = 0; tau < 32; ++tau) {
#pragma unroll
        for (int q = 0; q < 3; ++q)
          if (tau == 8 * q + 5 && !ok[q]) poll_flags<1>(ws, fh + (q + 1) * 512, true, tag);
        if (tau == 21 && b == 0 && lane == 0) set_flag(ws, SY_EPOCH, tag);
        BAR();
        if (tau == 29) {
#pragma unroll
          for (int i = 0; i < PF2T; ++i) dma4(rs_w2, ldsb + P1_DUMMY + wave * 256, w2_line((i * 27 + idx) * 64 + lane, g));
        }
      }
    } else if (wave >= 8) {
      auto issue_h = [&](int kt, int so) {
#pragma unroll
        for (int m = 0; m < 4; ++m)
          dma16<2>(rs_ws, ldsb + so + CG1::A_BYTES + ((wave - 8) * 4 + m) * 1024, lane * 16,
                   OFF_H + (unsigned)((dt1 * 3 + (wave - 8)) * 128 + kt * 4 + m) * 1024u);
      };
      lds_wait(smem + P1_RED + 40);
      issue_h(0, 0); issue_h(1, CG1::STAGE); issue_h(2, 2 * CG1::STAGE);
      CG1::ring<4>(32, issue_h);
    } else if (wave >= 4) {
      CG1::ring<4>(32, [&](int kt, int so) { issue_w1x(wave - 4, kt, so); });
    } else {
      f32x16 acc[1][3] = {};
      int rslot = 0;
      CG1::consume<false>(smem, wave, lane, 32, rslot, acc);
#pragma unroll
      for (int j = 0; j < 3; ++j)
#pragma unroll
        for (int s = 0; s < 2; ++s) {
          u32x4 val;
#pragma unroll
          for (int e = 0; e < 4; ++e) val[e] = pack_bf16(acc[0][j][8 * s + 2 * e], acc[0][j][8 * s + 2 * e + 1]);
          *(u32x4*)(ebuf + ((wave * 2 + s) * 3 + j) * 1024 + lane * 16) = val;
        }
    }
    WAIT_LGKM0();
    BAR();
    if (wave < 11) {
#pragma unroll
      for (int it = 0; it < 3; ++it) {
        const int i = wave + 11 * it;
        if (i < 24) {
          const int z = i / 3, j = i - 3 * z;
          const u32x4 a = *(const u32x4*)(ebuf + i * 1024 + lane * 16);
          u32x4 val;
#pragma unroll
          for (int e = 0; e < 4; ++e) {
            const unsigned bb = bv[it][e >> 1][e & 1];
            const unsigned pk = pack_bf16(lo16(a[e]) + lo16(bb), hi16(a[e]) + hi16(bb));
            val[e] = pack_bf16(gelu_tanh(lo16(pk)), gelu_tanh(hi16(pk)));
          }
          store_dev(ws, OFF_U + (unsigned)((dt1 * 3 + j) * 288 + ft * 8 + z) * 1024u + lane * 16, val);
        }
      }
      WAIT_VM(0);
    } else if (lane == 0) *(unsigned*)(smem + 163832) = 0u;
    WAIT_LGKM0();
    BAR();
    if (threadIdx.x < 2) set_flag(ws, SY_U + threadIdx.x * 2048 + tile * 8, tag);
  } else {
    const int me = (idx - 27) * 8 + g;
    if (me < 37) {
      if (wave == 8) poll_flags<4>(ws, SY_A + lane * 8, true, tag);
      BAR();
      const int cbk = me == 36 ? 0 : me;
      char* img = smem + 65536;
      for (int i = wave; i < 64; i += 12) {
        const int P = 4 * i + (lane >> 4);
        dma16<2>(rs_ws, ldsb + 65536 + i * 1024, (unsigned)P * 9216u + (unsigned)(((lane & 15) ^ (P & 15)) * 16), OFF_X1 + cbk * 256u);
      }
      for (int i = wave; i < TAB_BYTES / 1024; i += 12) dma16<0>(mk_rsrc(&T_COL, TAB_BYTES), ldsb + i * 1024, lane * 16, i * 1024u);
      const bool act = me == 36 ? wave == 0 : wave < 8;
      const int c = me == 36 ? 288 : me * 8 + wave;
      const float wsc = __uint_as_float((253u - wmax_exp(ws, lane)) << 23) * (1.0f / 64.0f);
      WAIT_VM(0);
      BAR();
      const int slot_re = me == 36 ? 1 : 2 * wave, slot_im = c == 288 ? -2 : (c == 0 ? -1 : 2 * wave + 1);
      u32x4 zr[2][2], zi[2][2];
      if (act) {
#pragma unroll
        for (int z = 0; z < 4; ++z) {
          const char* p = img + (z * 64 + lane) * 256;
          zr[z >> 1][z & 1] = *(const u32x4*)(p + ((slot_re ^ (lane & 15)) * 16));
          zi[z >> 1][z & 1] = slot_im >= 0 ? *(const u32x4*)(p + ((slot_im ^ (lane & 15)) * 16)) : u32x4{0, 0, 0, 0};
        }
      }
      WAIT_LGKM0();
      BAR();
      if (act) colfft_wave(smem + lane * 16, ws, c, lane, wsc, zr, zi, img, slot_re, slot_im);
      else if (wave >= 8 && idx < 31) {
        __builtin_amdgcn_s_sleep(64);
        for (int i0 = ((idx - 27) * 4 + wave - 8) * 64; i0 < PF2C_ROWS * 16; i0 += 16 * 64) {
          dma4(rs_w2, ldsb + P1_DUMMY + wave * 256, w2_line(PF2T * 27 * 64 + i0 + lane, g));
          WAIT_VM(7);
        }
      }
      WAIT_LGKM0();
      BAR();
      if (me == 36) {
        if (wave < 4) store_dev(ws, OFF_Y288 + wave * 1024 + lane * 16, *(const u32x4*)(img + wave * 1024 + lane * 16));
      } else {
        for (int i = wave; i < 64; i += 12) {
          const int Qy = 4 * i + (lane >> 4);
          store_dev(ws, OFF_Y1 + (unsigned)Qy * 9216u + cbk * 256 + (lane & 15) * 16,
                    *(const u32x4*)(img + Qy * 256 + (((lane & 15) ^ ((Qy >> 3) & 15)) * 16)));
        }
      }
      WAIT_VM(0);
      BAR();
      if (threadIdx.x == 0) set_flag(ws, SY_Y + me * 8, tag);
    }
  }
  if (idx < 24) {
    const int ks = g >> 1, dt = idx % 3, st = (g & 1) * 8 + idx / 3;
    const int rot = (g & 1) * 9;
    char* fbuf = smem + (15 & 3) * CG2::STAGE;
    const unsigned myflags = SY_T + (unsigned)((st * 4 + ks) * 3 + dt) * 64u;
    if (wave >= 8) {
      auto issue_u = [&](int k0, int so) {
        const int kt = k0 + rot < 18 ? k0 + rot : k0 + rot - 18;
        if (k0 == 0 || k0 == 8) {
          const int kf = k0 == 0 ? rot : 9 - rot;
          if (wave == 8) {
            poll_flags<1>(ws, SY_U + (g & 1) * 2048 + (unsigned)((9 * ks + (kf >> 1) + (lane >> 1)) * 6 + 2 * dt + (lane & 1)) * 8u, lane < 10, tag);
            if (k0 == 0) { if (lane == 0) lds_arrive(smem + 163832); }
          } else if (k0 == 0) lds_wait(smem + 163832);
        }
#pragma unroll
        for (int m = 0; m < 6; ++m) {
          const int a = (wave - 8) * 6 + m;
          dma16<2>(rs_ws, ldsb + so + a * 1024, lane * 16, OFF_U + (unsigned)((dt * 6 + (a >> 2)) * 288 + (ks * 18 + kt) * 4 + (a & 3)) * 1024u);
        }
      };
      issue_u(0, 0); issue_u(1, CG2::STAGE);
      CG2::ring<6>(18, issue_u);
      BAR();
      if (wave == 8) poll_flags<1>(ws, myflags + (lane & 3) * 8, true, tag);
    } else if (wave >= 4) {
      auto issue_w2 = [&](int k0, int so) {
        const int kt = k0 + rot < 18 ? k0 + rot : k0 + rot - 18;
        constexpr unsigned ld = S_ * 2;
        const unsigned vo = CG2::voff<TR128>(lane, ld);
#pragma unroll
        for (int m = 0; m < 4; ++m) {
          const int a = (wave - 4) * 4 + m;
          const unsigned sof = (unsigned)((ks * 18 + kt) * 64 + 4 * a) * ld + st * 256u;
          if (k0 >= NTF2) dma16<1>(rs_w2, ldsb + so + CG2::A_BYTES + a * 1024, vo, sof);
          else dma16<0>(rs_w2, ldsb + so + CG2::A_BYTES + a * 1024, vo, sof);
        }
      };
      issue_w2(0, 0); issue_w2(1, CG2::STAGE);
      CG2::ring<4>(18, issue_w2);
      BAR();
    } else {
    f32x16 acc[3][2] = {};
    int rslot = 0;
    CG2::consume<false>(smem, wave, lane, 18, rslot, acc);
    const int wa = wave >> 1, wb = wave & 1;
    const bool own0 = wb * 2 == ks, own1 = wb * 2 + 1 == ks;
#pragma unroll
    for (int j = 0; j < 2; ++j) {
      if (j ? own1 : own0) {
#pragma unroll
        for (int i = 0; i < 3; ++i)
#pragma unroll
          for (int q = 0; q < 4; ++q)
            *(f32x4*)(fbuf + ((wa * 3 + i) * 4 + q) * 1024 + lane * 16) =
                f32x4{acc[i][j][4 * q], acc[i][j][4 * q + 1], acc[i][j][4 * q + 2], acc[i][j][4 * q + 3]};
        continue;
      }
#pragma unroll
      for (int i = 0; i < 3; ++i) {
        const unsigned o = OFF_SLAB + (unsigned)((ks * 64 + st * 4 + wb * 2 + j) * 18 + dt * 6 + wa * 3 + i) * 2048u + lane * 16;
#pragma unroll
        for (int u = 0; u < 2; ++u) {
          u32x4 val;
#pragma unroll
          for (int e = 0; e < 4; ++e) val[e] = pack_bf16(acc[i][j][8 * u + 2 * e], acc[i][j][8 * u + 2 * e + 1]);
          store_dev(ws, o + u * 1024, val);
        }
      }
    }
    WAIT_VM(0); WAIT_LGKM0();
    BAR();
    if (wave == 0 && lane < 4 && lane != ks) set_flag(ws, SY_T + (unsigned)((st * 4 + lane) * 3 + dt) * 64u + ks * 8, tag);
    }
    BAR();
    const int sf = st * 4 + ks;
    const unsigned o = (unsigned)(sf * 18 + dt * 6 + (wave >> 1)) * 2048u + (wave & 1) * 1024 + lane * 16;
    u32x4 pv[4];
#pragma unroll
    for (int q = 0; q < 3; ++q) pv[q] = load_dev(ws, OFF_SLAB + o + ((ks + 1 + q) & 3) * 2359296u);
    pv[3] = load_dev(ws, OFF_R + o);
    const f32x4 a0 = *(const f32x4*)(fbuf + wave * 2048 + lane * 16), a1 = *(const f32x4*)(fbuf + wave * 2048 + 1024 + lane * 16);
    u32x4 ov;
#pragma unroll
    for (int e = 0; e < 4; ++e)
      ov[e] = pack_bf16((e < 2 ? a0[2 * e] : a1[2 * e - 4]) + (lo16(pv[0][e]) + lo16(pv[1][e])) + (lo16(pv[2][e]) + lo16(pv[3][e])),
                        (e < 2 ? a0[2 * e + 1] : a1[2 * e - 3]) + (hi16(pv[0][e]) + hi16(pv[1][e])) + (hi16(pv[2][e]) + hi16(pv[3][e])));
    __builtin_nontemporal_store(ov, (u32x4*)(out + (size_t)(sf * 32 + (lane & 31)) * D_ + (dt * 6 + (wave >> 1)) * 32 + (lane >> 5) * 16 + (wave & 1) * 8));
  } else {
    const int me = (idx - 24) * 8 + g, sr = lane & 31;
    char* kt = smem + 37888; char* yb = kt + 17408;
    u32x4 xv[3];
#pragma unroll
    for (int it = 0; it < 3; ++it) xv[it] = *(const u32x4*)(x + (size_t)(me * 32 + sr) * D_ + ((it * 12 + wave) * 2 + (lane >> 5)) * 8);
    const float bias = lo16(b2[me * 32 + sr]);
    BAR();
    for (int i = wave; i < 16; i += 12) dma16<0>(mk_rsrc(&T_INV, 16384u), ldsb + 37888 + i * 1024, lane * 16, i * 1024u);
    if (wave == 8) { poll_flags<1, 16>(ws, SY_Y + min(lane, 36) * 8, true, tag); }
    BAR();
#pragma unroll
    for (int it = 0; it < 3; ++it) dma16<2>(rs_ws, ldsb + (it * 12 + wave) * 1024, lane * 16, OFF_Y1 + me * 36864u + (it * 12 + wave) * 1024u);
    if (wave == 4) dma16<2>(rs_ws, ldsb + 37888 + 16384, (lane & 3) * 16, OFF_Y288 + me * 64u);
    const float fs = __uint_as_float((wmax_exp(ws, lane) + 1u) << 23) * (1.0f / 9.0f);
    WAIT_VM(0);
    BAR();
    for (int task = wave; task < 16; task += 12)
      irfft_wave(smem + (task >> 2) * 9216, kt + 16384 + (task >> 2) * 16, task & 3, lane, yb + task * 2 * FIN_OS, kt + lane * 16);
    WAIT_LGKM0();
    BAR();
#pragma unroll
    for (int it = 0; it < 3; ++it) {
      const int dp = (it * 12 + wave) * 2 + (lane >> 5);
      const char* yp = yb + sr * FIN_OS + dp * 32;
      const f32x4 y0 = *(const f32x4*)yp, y1 = *(const f32x4*)(yp + 16);
      u32x4 o;
#pragma unroll
      for (int e = 0; e < 4; ++e) {
        const float ya = e < 2 ? y0[2 * e] : y1[2 * e - 4], yc = e < 2 ? y0[2 * e + 1] : y1[2 * e - 3];
        o[e] = pack_bf16(lo16(xv[it][e]) + bias + ya * fs, hi16(xv[it][e]) + bias + yc * fs);
      }
      store_dev(ws, OFF_R + (unsigned)(me * 18 + (dp >> 2)) * 2048u + (dp & 1) * 1024 + (((dp >> 1) & 1) * 32 + sr) * 16, o);
    }
    WAIT_VM(0);
    BAR();
    if (wave == 0 && lane < 3) set_flag(ws, SY_T + (unsigned)(me * 3 + lane) * 64u + (me & 3) * 8, tag);
  }
}

extern "C" void kernel_launch(void* const* d_in, const int* in_sizes, int n_in, void* d_out, int out_size, void* d_ws, size_t ws_size,
                              hipStream_t stream) {
  (void)hipFuncSetAttribute((const void*)k_all, hipFuncAttributeMaxDynamicSharedMemorySize, 163840);
  k_all<<<256, 768, 163840, stream>>>((const u16*)d_in[0], (const u16*)d_in[1], (const u16*)d_in[2], (const char*)d_in[4], (const u16*)d_in[5],
                                      (const char*)d_in[6], (const u16*)d_in[7], (char*)d_ws, (u16*)d_out);
}
```
